```python
import torch

def _softmax(x, axis=-1):
    return torch.softmax(x, dim=axis)
def _gelu(x):
    return torch.nn.functional.gelu(x, approximate="tanh")
def _silu(x):
    return torch.nn.functional.silu(x)
def _sigmoid(x):
    return torch.sigmoid(x)
def _layernorm(x, axis=-1, eps=1e-5):
    m = x.mean(dim=axis, keepdim=True)
    v = x.var(dim=axis, keepdim=True, unbiased=False)
    return (x - m) / torch.sqrt(v + eps)
def _scatter_add(tgt, idx, src, axis=0):
    return tgt.clone().index_add_(axis, idx, src)
def _segment_sum(src, seg, n):
    out = torch.zeros((n,) + src.shape[1:], dtype=src.dtype)
    return out.index_add_(0, seg, src)
def _pad(x, pw):
    flat = tuple(v for pair in reversed(pw) for v in pair)
    return torch.nn.functional.pad(x, flat)

kernel_name = 'standalone__mha_block_cached__near_ridge'

def setup_inputs():

    _w = (lambda a: (lambda t: t.to(torch.bfloat16) if t.is_floating_point() else t)(torch.as_tensor(a)))
    B, Sq, D = 8, 256, 512
    H, Sk, Dh = 32, 2048, 128
    return {
        "x": _w((lambda *s: torch.randn(*s, dtype=torch.bfloat16) * 0.8)(B, Sq, D)),
        "k_cache": _w((lambda *s: torch.randn(*s, dtype=torch.bfloat16) * 0.8)(B, H, Sk, Dh)),
        "v_cache": _w((lambda *s: torch.randn(*s, dtype=torch.bfloat16) * 0.05)(B, H, Sk, Dh)),
        "wq": _w((lambda *s: torch.randn(*s, dtype=torch.bfloat16) * 0.8)(D, H * Dh) * (D ** -0.5)),
        "wo": _w((lambda *s: torch.randn(*s, dtype=torch.bfloat16) * 0.05)(H * Dh, D) * ((H * Dh) ** -0.5)),
    }

def reference(x, k_cache, v_cache, wq, wo):
    B, Sq, D = x.shape
    H, Dh = 32, 128
    q = (lambda x, *d: x.permute(*d))((x @ wq).reshape(B, Sq, H, Dh), 0, 2, 1, 3)
    scores = torch.einsum("bhqd,bhkd->bhqk", q, k_cache) * (Dh ** -0.5)
    p = _softmax(scores, axis=-1)
    o = torch.einsum("bhqk,bhkd->bhqd", p, v_cache)
    return (lambda x, *d: x.permute(*d))(o, 0, 2, 1, 3).reshape(B, Sq, H * Dh) @ wo
```

```cpp
#include <hip/hip_runtime.h>
#include <hip/hip_bf16.h>
typedef unsigned short u16;
__device__ __forceinline__ float bf2f(u16 v){ return __uint_as_float(((unsigned)v)<<16); }
__device__ __forceinline__ u16 f2bf(float f){ __bf16 b=(__bf16)f; return __builtin_bit_cast(u16,b); }
constexpr int B=8,SQ=256,D=512,H=32,SK=2048,DH=128;
__global__ void qproj(const u16* x,const u16* wq,u16* q){
  int row=blockIdx.x;
  int n=blockIdx.y*256+threadIdx.x;
  float acc=0;
  for(int k=0;k<D;k++) acc+=bf2f(x[row*D+k])*bf2f(wq[k*(H*DH)+n]);
  q[(size_t)row*(H*DH)+n]=f2bf(acc);
}
__global__ void attn(const u16* q,const u16* k,const u16* v,u16* o){
  __shared__ float sq[DH]; __shared__ float sc[SK]; __shared__ float red[256];
  int row=blockIdx.x, h=blockIdx.y; int b=row/SQ; int t=threadIdx.x;
  if(t<DH) sq[t]=bf2f(q[(size_t)row*(H*DH)+h*DH+t]);
  __syncthreads();
  const u16* kb=k+((size_t)(b*H+h))*SK*DH; const u16* vb=v+((size_t)(b*H+h))*SK*DH;
  float mx=-1e30f;
  for(int j=t;j<SK;j+=256){ float a=0; for(int d=0;d<DH;d++) a+=sq[d]*bf2f(kb[j*DH+d]); a*=0.08838834764f; sc[j]=a; mx=fmaxf(mx,a);}
  red[t]=mx; __syncthreads();
  for(int s=128;s>0;s>>=1){ if(t<s) red[t]=fmaxf(red[t],red[t+s]); __syncthreads(); }
  mx=red[0]; __syncthreads();
  float sum=0; for(int j=t;j<SK;j+=256){ float e=__expf(sc[j]-mx); sc[j]=e; sum+=e; }
  red[t]=sum; __syncthreads();
  for(int s=128;s>0;s>>=1){ if(t<s) red[t]+=red[t+s]; __syncthreads(); }
  sum=red[0];
  if(t<DH){ float a=0; for(int j=0;j<SK;j++) a+=sc[j]*bf2f(vb[j*DH+t]); o[(size_t)row*(H*DH)+h*DH+t]=f2bf(a/sum); }
}
__global__ void oproj(const u16* o,const u16* wo,u16* out){
  int row=blockIdx.x; int n=blockIdx.y*256+threadIdx.x; float acc=0;
  for(int k=0;k<H*DH;k++) acc+=bf2f(o[(size_t)row*(H*DH)+k])*bf2f(wo[k*D+n]);
  out[row*D+n]=f2bf(acc);
}
extern "C" void kernel_launch(void* const* d_in,const int* in_sizes,int n_in,void* d_out,int out_size,void* d_ws,size_t ws_size,hipStream_t stream){
  const u16* x=(const u16*)d_in[0]; const u16* k=(const u16*)d_in[1]; const u16* v=(const u16*)d_in[2]; const u16* wq=(const u16*)d_in[3]; const u16* wo=(const u16*)d_in[4];
  u16* q=(u16*)d_ws; u16* o=q+(size_t)B*SQ*H*DH;
  qproj<<<dim3(B*SQ,H*DH/256),256,0,stream>>>(x,wq,q);
  attn<<<dim3(B*SQ,H),256,0,stream>>>(q,k,v,o);
  oproj<<<dim3(B*SQ,D/256),256,0,stream>>>(o,wo,(u16*)d_out);
}
```

```cpp
#include <hip/hip_runtime.h>
#include <hip/hip_bf16.h>

extern "C" __global__ __launch_bounds__(256,1) void mha_block_fused(const void* x,const void* kc,const void* vc,const void* wq,const void* wo,void* ws,void* out){
  __shared__ __attribute__((aligned(1024))) char lds[163840];
  unsigned wid=__builtin_amdgcn_readfirstlane(threadIdx.x>>6);
  unsigned bid=blockIdx.x;
  asm volatile(
    "s_mov_b32 s51, 0\n"
    "START%=:\n"
    "s_mov_b64 s[88:89], %0\n"
    "s_mov_b64 s[20:21], %1\n"
    "s_mov_b64 s[24:25], %2\n"
    "s_mov_b64 s[52:53], %3\n"
    "s_mov_b64 s[60:61], %4\n"
    "s_mov_b64 s[70:71], %5\n"
    "s_mov_b64 s[68:69], %6\n"
    "s_add_u32 s36, s70, 0x4000000\n"
    "s_addc_u32 s37, s71, 0\n"
    "s_mov_b32 s48, %7\n"
    "s_mov_b32 s49, %8\n"
    "s_mov_b32 s50, %9\n"
    "; ---- set-up, first x / wq chunks and K(0) by LDS-DMA\n"
    "s_and_b32 s46, s49, 7\n"
    "s_lshr_b32 s47, s49, 3\n"
    "s_and_b32 s57, s46, 1\n"
    "s_lshl_b32 s57, s57, 2\n"
    "s_and_b32 s58, s47, 3\n"
    "s_add_u32 s57, s57, s58\n"
    "s_lshr_b32 s58, s46, 1\n"
    "s_lshl_b32 s58, s58, 3\n"
    "s_lshr_b32 s47, s47, 2\n"
    "s_add_u32 s58, s58, s47\n"
    "v_mbcnt_lo_u32_b32 v236, -1, 0\n"
    "v_mbcnt_hi_u32_b32 v236, -1, v236\n"
    "s_lshl_b32 s46, s57, 2\n"
    "s_add_u32 s46, s46, s48\n"
    "s_lshl_b32 s46, s46, 16\n"
    "s_add_u32 s88, s88, s46\n"
    "s_addc_u32 s89, s89, 0\n"
    "s_and_b32 s89, s89, 0xffff\n"
    "s_mov_b32 s90, 65536\n"
    "s_mov_b32 s91, 0x00020000\n"
    "s_and_b32 s46, s48, 1\n"
    "s_lshl_b32 s46, s46, 13\n"
    "s_add_u32 s46, s46, s50\n"
    "s_cmp_lt_u32 s48, 2\n"
    "s_cselect_b32 s92, 2, 5\n"
    "s_cselect_b32 s93, 3, 6\n"
    "s_lshl_b32 s92, s92, 14\n"
    "s_lshl_b32 s93, s93, 14\n"
    "s_add_u32 s92, s92, s46\n"
    "s_add_u32 s93, s93, s46\n"
    "s_and_b32 s53, s53, 0xffff\n"
    "s_mov_b32 s54, 0x400000\n"
    "s_mov_b32 s55, 0x00020000\n"
    "s_lshl_b32 s99, s48, 17\n"
    "s_lshl_b32 s46, s58, 8\n"
    "s_add_u32 s99, s99, s46\n"
    "s_and_b32 s46, s57, 3\n"
    "s_lshl_b32 s46, s46, 1\n"
    "s_add_u32 s46, s46, s58\n"
    "s_and_b32 s98, s46, 7\n"
    "s_lshl_b32 s46, s98, 19\n"
    "s_add_u32 s56, s99, s46\n"
    "s_lshl_b32 s94, s98, 7\n"
    "s_add_u32 s95, s94, 32768\n"
    "s_lshl_b32 s30, s48, 12\n"
    "s_add_u32 s30, s30, s50\n"
    "v_lshrrev_b32 v215, 3, v236\n"
    "v_and_b32 v216, 7, v236\n"
    "v_lshrrev_b32 v217, 1, v215\n"
    "v_xor_b32 v218, 0, v217\n"
    "v_xor_b32 v218, v218, v216\n"
    "v_lshlrev_b32 v218, 4, v218\n"
    "v_lshl_add_u32 v218, v215, 10, v218\n"
    "v_add_u32 v198, 0, v218\n"
    "v_add_u32 v200, 14336, v198\n"
    "v_xor_b32 v218, 4, v217\n"
    "v_xor_b32 v218, v218, v216\n"
    "v_lshlrev_b32 v218, 4, v218\n"
    "v_lshl_add_u32 v218, v215, 10, v218\n"
    "v_add_u32 v199, 7168, v218\n"
    "v_add_u32 v201, 14336, v199\n"
    "v_and_b32 v212, 15, v236\n"
    "v_lshrrev_b32 v213, 4, v236\n"
    "v_lshlrev_b32 v214, 2, v213\n"
    "v_xor_b32 v214, v214, v212\n"
    "v_lshlrev_b32 v214, 4, v214\n"
    "v_lshl_add_u32 v206, v213, 13, v214\n"
    "v_add_u32 v207, 31744, v206\n"
    "v_add_u32 v208, 63488, v206\n"
    "v_add_u32 v209, 95232, v206\n"
    "s_add_u32 m0, s92, 0x0\n"
    "s_nop 0\n"
    "buffer_load_dwordx4 v198, s[88:91], s94 offen offset:0 lds\n"
    "buffer_load_dwordx4 v199, s[88:91], s94 offen offset:1024 lds\n"
    "buffer_load_dwordx4 v200, s[88:91], s94 offen offset:2048 lds\n"
    "buffer_load_dwordx4 v201, s[88:91], s94 offen offset:3072 lds\n"
    "s_add_u32 m0, s92, 0x1000\n"
    "s_nop 0\n"
    "buffer_load_dwordx4 v198, s[88:91], s95 offen offset:0 lds\n"
    "buffer_load_dwordx4 v199, s[88:91], s95 offen offset:1024 lds\n"
    "buffer_load_dwordx4 v200, s[88:91], s95 offen offset:2048 lds\n"
    "buffer_load_dwordx4 v201, s[88:91], s95 offen offset:3072 lds\n"
    "s_add_u32 s94, s94, 128\n"
    "s_and_b32 s94, s94, 1023\n"
    "s_add_u32 s95, s94, 32768\n"
    "s_add_u32 m0, s30, 0x20000\n"
    "s_nop 0\n"
    "buffer_load_dwordx4 v206, s[52:55], s56 offen offset:0 lds\n"
    "buffer_load_dwordx4 v207, s[52:55], s56 offen offset:1024 lds\n"
    "buffer_load_dwordx4 v208, s[52:55], s56 offen offset:2048 lds\n"
    "buffer_load_dwordx4 v209, s[52:55], s56 offen offset:3072 lds\n"
    "s_add_u32 s98, s98, 1\n"
    "s_and_b32 s98, s98, 7\n"
    "s_lshl_b32 s56, s98, 19\n"
    "s_add_u32 s56, s56, s99\n"
    "s_add_u32 m0, s93, 0x0\n"
    "s_nop 0\n"
    "buffer_load_dwordx4 v198, s[88:91], s94 offen offset:0 lds\n"
    "buffer_load_dwordx4 v199, s[88:91], s94 offen offset:1024 lds\n"
    "buffer_load_dwordx4 v200, s[88:91], s94 offen offset:2048 lds\n"
    "buffer_load_dwordx4 v201, s[88:91], s94 offen offset:3072 lds\n"
    "s_add_u32 m0, s93, 0x1000\n"
    "s_nop 0\n"
    "buffer_load_dwordx4 v198, s[88:91], s95 offen offset:0 lds\n"
    "buffer_load_dwordx4 v199, s[88:91], s95 offen offset:1024 lds\n"
    "buffer_load_dwordx4 v200, s[88:91], s95 offen offset:2048 lds\n"
    "buffer_load_dwordx4 v201, s[88:91], s95 offen offset:3072 lds\n"
    "s_add_u32 s94, s94, 128\n"
    "s_and_b32 s94, s94, 1023\n"
    "s_add_u32 s95, s94, 32768\n"
    "s_add_u32 m0, s30, 0x24000\n"
    "s_nop 0\n"
    "buffer_load_dwordx4 v206, s[52:55], s56 offen offset:0 lds\n"
    "buffer_load_dwordx4 v207, s[52:55], s56 offen offset:1024 lds\n"
    "buffer_load_dwordx4 v208, s[52:55], s56 offen offset:2048 lds\n"
    "buffer_load_dwordx4 v209, s[52:55], s56 offen offset:3072 lds\n"
    "s_add_u32 s98, s98, 1\n"
    "s_and_b32 s98, s98, 7\n"
    "s_lshl_b32 s56, s98, 19\n"
    "s_add_u32 s56, s56, s99\n"
    "s_lshl_b32 s46, s57, 5\n"
    "s_add_u32 s46, s46, s58\n"
    "s_lshr_b32 s47, s46, 13\n"
    "s_lshl_b32 s28, s46, 19\n"
    "s_add_u32 s20, s20, s28\n"
    "s_addc_u32 s21, s21, s47\n"
    "s_and_b32 s21, s21, 0xffff\n"
    "s_mov_b32 s22, 524288\n"
    "s_mov_b32 s23, 0x00020000\n"
    "s_add_u32 s24, s24, s28\n"
    "s_addc_u32 s25, s25, s47\n"
    "s_and_b32 s25, s25, 0xffff\n"
    "s_mov_b32 s26, 524288\n"
    "s_mov_b32 s27, 0x00020000\n"
    "s_lshl_b32 s46, s46, 2\n"
    "s_add_u32 s46, s46, s48\n"
    "s_lshl_b32 s46, s46, 14\n"
    "s_add_u32 s34, s70, s46\n"
    "s_addc_u32 s35, s71, 0\n"
    "s_and_b32 s61, s61, 0xffff\n"
    "s_mov_b32 s62, 0x400000\n"
    "s_mov_b32 s63, 0x00020000\n"
    "s_lshl_b32 s46, s58, 2\n"
    "s_add_u32 s46, s46, s48\n"
    "s_lshl_b32 s46, s46, 15\n"
    "s_lshl_b32 s59, s57, 7\n"
    "s_add_u32 s59, s59, s46\n"
    "s_lshl_b32 s46, s57, 5\n"
    "s_add_u32 s46, s46, s58\n"
    "s_lshl_b32 s46, s46, 2\n"
    "s_add_u32 s46, s46, s48\n"
    "s_lshl_b32 s46, s46, 12\n"
    "s_add_u32 s46, s46, 0x1000000\n"
    "s_add_u32 s64, s70, s46\n"
    "s_addc_u32 s65, s71, 0\n"
    "s_lshl_b32 s28, s48, 12\n"
    "s_and_b32 s46, s58, 31\n"
    "s_lshl_b32 s46, s46, 14\n"
    "s_add_u32 s28, s28, s46\n"
    "s_mov_b32 s29, s28\n"
    "s_mov_b32 s66, 0x3e0293ee\n"
    "s_mov_b32 s67, 0x3e0293ee\n"
    "s_add_u32 s32, s70, 0x1400000\n"
    "s_addc_u32 s33, s71, 0\n"
    "v_and_b32 v212, 15, v236\n"
    "v_lshrrev_b32 v213, 4, v236\n"
    "v_lshlrev_b32 v237, 4, v236\n"
    "v_add_u32 v215, 0, v213\n"
    "v_xor_b32 v215, v215, v212\n"
    "v_lshlrev_b32 v215, 4, v215\n"
    "v_lshl_add_u32 v192, v213, 8, v215\n"
    "v_add_u32 v215, 4, v213\n"
    "v_xor_b32 v215, v215, v212\n"
    "v_lshlrev_b32 v215, 4, v215\n"
    "v_lshl_add_u32 v193, v213, 8, v215\n"
    "v_add_u32 v215, 8, v213\n"
    "v_xor_b32 v215, v215, v212\n"
    "v_lshlrev_b32 v215, 4, v215\n"
    "v_lshl_add_u32 v194, v213, 8, v215\n"
    "v_add_u32 v215, 12, v213\n"
    "v_xor_b32 v215, v215, v212\n"
    "v_lshlrev_b32 v215, 4, v215\n"
    "v_lshl_add_u32 v195, v213, 8, v215\n"
    "v_add_u32 v215, 0, v213\n"
    "v_and_b32 v215, 7, v215\n"
    "v_lshlrev_b32 v215, 1, v215\n"
    "v_xor_b32 v215, v215, v212\n"
    "v_lshlrev_b32 v215, 4, v215\n"
    "v_lshl_add_u32 v196, v213, 8, v215\n"
    "v_add_u32 v215, 4, v213\n"
    "v_and_b32 v215, 7, v215\n"
    "v_lshlrev_b32 v215, 1, v215\n"
    "v_xor_b32 v215, v215, v212\n"
    "v_lshlrev_b32 v215, 4, v215\n"
    "v_lshl_add_u32 v197, v213, 8, v215\n"
    "s_add_u32 m0, s30, 0x0\n"
    "s_nop 0\n"
    "buffer_load_dwordx4 v192, s[20:23], s28 offen offset:0 nt lds\n"
    "buffer_load_dwordx4 v193, s[20:23], s28 offen offset:1024 nt lds\n"
    "buffer_load_dwordx4 v194, s[20:23], s28 offen offset:2048 nt lds\n"
    "buffer_load_dwordx4 v195, s[20:23], s28 offen offset:3072 nt lds\n"
    "s_add_u32 s28, s28, 16384\n"
    "s_and_b32 s28, s28, 0x7ffff\n"
    "v_and_b32 v212, 15, v236\n"
    "v_lshrrev_b32 v213, 4, v236\n"
    "v_add_u32 v215, 0, v213\n"
    "v_xor_b32 v215, v215, v212\n"
    "v_lshlrev_b32 v215, 4, v215\n"
    "v_lshl_add_u32 v215, v212, 8, v215\n"
    "v_add_u32 v180, s50, v215\n"
    "v_add_u32 v215, 4, v213\n"
    "v_xor_b32 v215, v215, v212\n"
    "v_lshlrev_b32 v215, 4, v215\n"
    "v_lshl_add_u32 v215, v212, 8, v215\n"
    "v_add_u32 v181, s50, v215\n"
    "v_add_u32 v215, 8, v213\n"
    "v_xor_b32 v215, v215, v212\n"
    "v_lshlrev_b32 v215, 4, v215\n"
    "v_lshl_add_u32 v215, v212, 8, v215\n"
    "v_add_u32 v182, s50, v215\n"
    "v_add_u32 v215, 12, v213\n"
    "v_xor_b32 v215, v215, v212\n"
    "v_lshlrev_b32 v215, 4, v215\n"
    "v_lshl_add_u32 v215, v212, 8, v215\n"
    "v_add_u32 v183, s50, v215\n"
    "v_bfe_u32 v216, v236, 2, 2\n"
    "v_and_b32 v217, 3, v236\n"
    "v_lshl_add_u32 v218, v213, 2, v216\n"
    "v_and_b32 v219, 7, v218\n"
    "v_lshrrev_b32 v220, 1, v217\n"
    "v_and_b32 v222, 1, v217\n"
    "v_lshlrev_b32 v222, 3, v222\n"
    "v_lshl_add_u32 v222, v218, 8, v222\n"
    "v_add_u32 v222, s50, v222\n"
    "v_add_u32 v222, 0x10000, v222\n"
    "v_xor_b32 v215, 0, v219\n"
    "v_lshl_add_u32 v215, v215, 1, v220\n"
    "v_lshl_add_u32 v184, v215, 4, v222\n"
    "v_xor_b32 v215, 1, v219\n"
    "v_lshl_add_u32 v215, v215, 1, v220\n"
    "v_lshl_add_u32 v185, v215, 4, v222\n"
    "v_xor_b32 v215, 2, v219\n"
    "v_lshl_add_u32 v215, v215, 1, v220\n"
    "v_lshl_add_u32 v186, v215, 4, v222\n"
    "v_xor_b32 v215, 3, v219\n"
    "v_lshl_add_u32 v215, v215, 1, v220\n"
    "v_lshl_add_u32 v187, v215, 4, v222\n"
    "v_xor_b32 v215, 4, v219\n"
    "v_lshl_add_u32 v215, v215, 1, v220\n"
    "v_lshl_add_u32 v188, v215, 4, v222\n"
    "v_xor_b32 v215, 5, v219\n"
    "v_lshl_add_u32 v215, v215, 1, v220\n"
    "v_lshl_add_u32 v189, v215, 4, v222\n"
    "v_xor_b32 v215, 6, v219\n"
    "v_lshl_add_u32 v215, v215, 1, v220\n"
    "v_lshl_add_u32 v190, v215, 4, v222\n"
    "v_xor_b32 v215, 7, v219\n"
    "v_lshl_add_u32 v215, v215, 1, v220\n"
    "v_lshl_add_u32 v191, v215, 4, v222\n"
    "v_mov_b32 v176, 0x3f803f80\n"
    "v_mov_b32 v177, 0x3f803f80\n"
    "v_mov_b32 v178, 0x3f803f80\n"
    "v_mov_b32 v179, 0x3f803f80\n"
    "v_lshrrev_b32 v221, 1, v212\n"
    "v_and_b32 v221, 7, v221\n"
    "v_add_u32 v215, 0, v213\n"
    "v_xor_b32 v215, v215, v221\n"
    "v_lshlrev_b32 v215, 4, v215\n"
    "v_lshl_add_u32 v215, v212, 7, v215\n"
    "v_add_u32 v244, s92, v215\n"
    "v_add_u32 v246, s93, v215\n"
    "v_add_u32 v215, 4, v213\n"
    "v_xor_b32 v215, v215, v221\n"
    "v_lshlrev_b32 v215, 4, v215\n"
    "v_lshl_add_u32 v215, v212, 7, v215\n"
    "v_add_u32 v245, s92, v215\n"
    "v_add_u32 v247, s93, v215\n"
    "v_lshl_add_u32 v218, v213, 3, v216\n"
    "v_xor_b32 v215, 0, v216\n"
    "v_lshl_add_u32 v215, v215, 2, v217\n"
    "v_lshlrev_b32 v215, 4, v215\n"
    "v_lshl_add_u32 v215, v218, 8, v215\n"
    "v_add_u32 v202, s50, v215\n"
    "v_xor_b32 v215, 1, v216\n"
    "v_lshl_add_u32 v215, v215, 2, v217\n"
    "v_lshlrev_b32 v215, 4, v215\n"
    "v_lshl_add_u32 v215, v218, 8, v215\n"
    "v_add_u32 v203, s50, v215\n"
    "v_xor_b32 v215, 2, v216\n"
    "v_lshl_add_u32 v215, v215, 2, v217\n"
    "v_lshlrev_b32 v215, 4, v215\n"
    "v_lshl_add_u32 v215, v218, 8, v215\n"
    "v_add_u32 v204, s50, v215\n"
    "v_xor_b32 v215, 3, v216\n"
    "v_lshl_add_u32 v215, v215, 2, v217\n"
    "v_lshlrev_b32 v215, 4, v215\n"
    "v_lshl_add_u32 v215, v218, 8, v215\n"
    "v_add_u32 v205, s50, v215\n"
    "v_lshrrev_b32 v226, 3, v236\n"
    "v_and_b32 v227, 7, v236\n"
    "v_lshlrev_b32 v227, 4, v227\n"
    "v_lshl_add_u32 v226, v226, 10, v227\n"
    "; ---- Q projection: 8 chunks of K=64, acc v[0:127] = Q^T (128 x 64), then bf16(c*q) -> a[0:63]\n"
    "v_lshrrev_b32 v224, 4, v236\n"
    "v_bfe_u32 v225, v236, 2, 2\n"
    "v_lshl_add_u32 v224, v224, 2, v225\n"
    "v_and_b32 v225, 3, v236\n"
    "v_lshlrev_b32 v225, 5, v225\n"
    "v_lshl_add_u32 v224, v224, 7, v225\n"
    "v_add_u32 v224, s30, v224\n"
    "s_waitcnt vmcnt(16)\n"
    "s_barrier\n"
    "v_add_u32 v202, 0x20000, v202\n"
    "v_add_u32 v203, 0x20000, v203\n"
    "v_add_u32 v204, 0x20000, v204\n"
    "v_add_u32 v205, 0x20000, v205\n"
    "ds_read_b128 a[64:67], v244 offset:0\n"
    "ds_read_b128 a[68:71], v244 offset:2048\n"
    "ds_read_b128 a[72:75], v244 offset:4096\n"
    "ds_read_b128 a[76:79], v244 offset:6144\n"
    "ds_read_b128 a[80:83], v245 offset:0\n"
    "ds_read_b128 a[84:87], v245 offset:2048\n"
    "ds_read_b128 a[88:91], v245 offset:4096\n"
    "ds_read_b128 a[92:95], v245 offset:6144\n"
    "ds_read_b64_tr_b16 a[192:193], v202 offset:0\n"
    "ds_read_b64_tr_b16 a[194:195], v202 offset:1024\n"
    "ds_read_b64_tr_b16 a[196:197], v202 offset:8\n"
    "ds_read_b64_tr_b16 a[198:199], v202 offset:1032\n"
    "ds_read_b64_tr_b16 a[200:201], v203 offset:0\n"
    "ds_read_b64_tr_b16 a[202:203], v203 offset:1024\n"
    "ds_read_b64_tr_b16 a[204:205], v203 offset:8\n"
    "ds_read_b64_tr_b16 a[206:207], v203 offset:1032\n"
    "ds_read_b64_tr_b16 a[208:209], v204 offset:0\n"
    "ds_read_b64_tr_b16 a[210:211], v204 offset:1024\n"
    "ds_read_b64_tr_b16 a[212:213], v204 offset:8\n"
    "ds_read_b64_tr_b16 a[214:215], v204 offset:1032\n"
    "ds_read_b64_tr_b16 a[216:217], v205 offset:0\n"
    "ds_read_b64_tr_b16 a[218:219], v205 offset:1024\n"
    "ds_read_b64_tr_b16 a[220:221], v205 offset:8\n"
    "ds_read_b64_tr_b16 a[222:223], v205 offset:1032\n"
    "s_waitcnt lgkmcnt(0)\n"
    "v_mfma_f32_16x16x32_bf16 v[0:3], a[192:195], a[64:67], 0\n"
    "v_mfma_f32_16x16x32_bf16 v[4:7], a[192:195], a[68:71], 0\n"
    "s_add_u32 m0, s30, 0x1c000\n"
    "ds_read_b64_tr_b16 a[224:225], v202 offset:8192\n"
    "v_mfma_f32_16x16x32_bf16 v[8:11], a[192:195], a[72:75], 0\n"
    "s_nop 0\n"
    "ds_read_b64_tr_b16 a[226:227], v202 offset:9216\n"
    "v_mfma_f32_16x16x32_bf16 v[12:15], a[192:195], a[76:79], 0\n"
    "buffer_load_dwordx4 v206, s[52:55], s56 offen offset:0 lds\n"
    "v_mfma_f32_16x16x32_bf16 v[16:19], a[196:199], a[64:67], 0\n"
    "ds_read_b64_tr_b16 a[228:229], v202 offset:8200\n"
    "buffer_load_dwordx4 v207, s[52:55], s56 offen offset:1024 lds\n"
    "v_mfma_f32_16x16x32_bf16 v[20:23], a[196:199], a[68:71], 0\n"
    "buffer_load_dwordx4 v208, s[52:55], s56 offen offset:2048 lds\n"
    "v_mfma_f32_16x16x32_bf16 v[24:27], a[196:199], a[72:75], 0\n"
    "ds_read_b64_tr_b16 a[230:231], v202 offset:9224\n"
    "buffer_load_dwordx4 v209, s[52:55], s56 offen offset:3072 lds\n"
    "v_mfma_f32_16x16x32_bf16 v[28:31], a[196:199], a[76:79], 0\n"
    "s_add_u32 s98, s98, 1\n"
    "v_mfma_f32_16x16x32_bf16 v[32:35], a[200:203], a[64:67], 0\n"
    "s_and_b32 s98, s98, 7\n"
    "ds_read_b64_tr_b16 a[232:233], v203 offset:8192\n"
    "v_mfma_f32_16x16x32_bf16 v[36:39], a[200:203], a[68:71], 0\n"
    "s_lshl_b32 s56, s98, 19\n"
    "v_mfma_f32_16x16x32_bf16 v[40:43], a[200:203], a[72:75], 0\n"
    "s_add_u32 s56, s56, s99\n"
    "ds_read_b64_tr_b16 a[234:235], v203 offset:9216\n"
    "v_mfma_f32_16x16x32_bf16 v[44:47], a[200:203], a[76:79], 0\n"
    "s_add_u32 m0, s30, 0x4000\n"
    "ds_read_b64_tr_b16 a[236:237], v203 offset:8200\n"
    "v_mfma_f32_16x16x32_bf16 v[48:51], a[204:207], a[64:67], 0\n"
    "buffer_load_dwordx4 v226, s[60:63], s59 offen offset:0 lds\n"
    "v_mfma_f32_16x16x32_bf16 v[52:55], a[204:207], a[68:71], 0\n"
    "ds_read_b64_tr_b16 a[238:239], v203 offset:9224\n"
    "v_add_u32 v226, 7168, v226\n"
    "v_mfma_f32_16x16x32_bf16 v[56:59], a[204:207], a[72:75], 0\n"
    "buffer_load_dwordx4 v226, s[60:63], s59 offen offset:1024 lds\n"
    "v_mfma_f32_16x16x32_bf16 v[60:63], a[204:207], a[76:79], 0\n"
    "ds_read_b64_tr_b16 a[240:241], v204 offset:8192\n"
    "v_add_u32 v226, 7168, v226\n"
    "v_mfma_f32_16x16x32_bf16 v[64:67], a[208:211], a[64:67], 0\n"
    "buffer_load_dwordx4 v226, s[60:63], s59 offen offset:2048 lds\n"
    "v_mfma_f32_16x16x32_bf16 v[68:71], a[208:211], a[68:71], 0\n"
    "ds_read_b64_tr_b16 a[242:243], v204 offset:9216\n"
    "v_add_u32 v226, 7168, v226\n"
    "v_mfma_f32_16x16x32_bf16 v[72:75], a[208:211], a[72:75], 0\n"
    "buffer_load_dwordx4 v226, s[60:63], s59 offen offset:3072 lds\n"
    "v_mfma_f32_16x16x32_bf16 v[76:79], a[208:211], a[76:79], 0\n"
    "ds_read_b64_tr_b16 a[244:245], v204 offset:8200\n"
    "v_mov_b32 v228, 0\n"
    "v_mfma_f32_16x16x32_bf16 v[80:83], a[212:215], a[64:67], 0\n"
    "ds_read_b64_tr_b16 a[246:247], v204 offset:9224\n"
    "global_load_dwordx2 v[248:249], v228, s[32:33] sc1\n"
    "v_mfma_f32_16x16x32_bf16 v[84:87], a[212:215], a[68:71], 0\n"
    "v_mfma_f32_16x16x32_bf16 v[88:91], a[212:215], a[72:75], 0\n"
    "ds_read_b64_tr_b16 a[248:249], v205 offset:8192\n"
    "v_mfma_f32_16x16x32_bf16 v[92:95], a[212:215], a[76:79], 0\n"
    "v_mfma_f32_16x16x32_bf16 v[96:99], a[216:219], a[64:67], 0\n"
    "ds_read_b64_tr_b16 a[250:251], v205 offset:9216\n"
    "v_mfma_f32_16x16x32_bf16 v[100:103], a[216:219], a[68:71], 0\n"
    "s_add_u32 m0, s92, 0x0\n"
    "v_mfma_f32_16x16x32_bf16 v[104:107], a[216:219], a[72:75], 0\n"
    "ds_read_b64_tr_b16 a[252:253], v205 offset:8200\n"
    "buffer_load_dwordx4 v198, s[88:91], s94 offen offset:0 lds\n"
    "v_mfma_f32_16x16x32_bf16 v[108:111], a[216:219], a[76:79], 0\n"
    "v_mfma_f32_16x16x32_bf16 v[112:115], a[220:223], a[64:67], 0\n"
    "ds_read_b64_tr_b16 a[254:255], v205 offset:9224\n"
    "buffer_load_dwordx4 v199, s[88:91], s94 offen offset:1024 lds\n"
    "v_mfma_f32_16x16x32_bf16 v[116:119], a[220:223], a[68:71], 0\n"
    "v_mfma_f32_16x16x32_bf16 v[120:123], a[220:223], a[72:75], 0\n"
    "buffer_load_dwordx4 v200, s[88:91], s94 offen offset:2048 lds\n"
    "v_mfma_f32_16x16x32_bf16 v[124:127], a[220:223], a[76:79], 0\n"
    "s_waitcnt lgkmcnt(1)\n"
    "v_mfma_f32_16x16x32_bf16 v[0:3], a[224:227], a[80:83], v[0:3]\n"
    "buffer_load_dwordx4 v201, s[88:91], s94 offen offset:3072 lds\n"
    "v_mfma_f32_16x16x32_bf16 v[4:7], a[224:227], a[84:87], v[4:7]\n"
    "s_add_u32 m0, s92, 0x1000\n"
    "v_mfma_f32_16x16x32_bf16 v[8:11], a[224:227], a[88:91], v[8:11]\n"
    "buffer_load_dwordx4 v198, s[88:91], s95 offen offset:0 lds\n"
    "v_mfma_f32_16x16x32_bf16 v[12:15], a[224:227], a[92:95], v[12:15]\n"
    "v_mfma_f32_16x16x32_bf16 v[16:19], a[228:231], a[80:83], v[16:19]\n"
    "buffer_load_dwordx4 v199, s[88:91], s95 offen offset:1024 lds\n"
    "v_mfma_f32_16x16x32_bf16 v[20:23], a[228:231], a[84:87], v[20:23]\n"
    "s_waitcnt vmcnt(19)\n"
    "s_waitcnt lgkmcnt(0)\n"
    "v_mfma_f32_16x16x32_bf16 v[24:27], a[228:231], a[88:91], v[24:27]\n"
    "s_barrier\n"
    "buffer_load_dwordx4 v200, s[88:91], s95 offen offset:2048 lds\n"
    "v_add_u32 v202, 0x4000, v202\n"
    "v_add_u32 v203, 0x4000, v203\n"
    "v_add_u32 v204, 0x4000, v204\n"
    "v_add_u32 v205, 0x4000, v205\n"
    "v_mfma_f32_16x16x32_bf16 v[28:31], a[228:231], a[92:95], v[28:31]\n"
    "ds_read_b128 a[96:99], v246 offset:0\n"
    "ds_read_b64_tr_b16 a[192:193], v202 offset:0\n"
    "v_mfma_f32_16x16x32_bf16 v[32:35], a[232:235], a[80:83], v[32:35]\n"
    "ds_read_b64_tr_b16 a[194:195], v202 offset:1024\n"
    "buffer_load_dwordx4 v201, s[88:91], s95 offen offset:3072 lds\n"
    "v_mfma_f32_16x16x32_bf16 v[36:39], a[232:235], a[84:87], v[36:39]\n"
    "s_add_u32 s94, s94, 128\n"
    "s_and_b32 s94, s94, 1023\n"
    "s_add_u32 s95, s94, 32768\n"
    "ds_read_b128 a[100:103], v246 offset:2048\n"
    "v_mfma_f32_16x16x32_bf16 v[40:43], a[232:235], a[88:91], v[40:43]\n"
    "ds_read_b64_tr_b16 a[196:197], v202 offset:8\n"
    "v_mfma_f32_16x16x32_bf16 v[44:47], a[232:235], a[92:95], v[44:47]\n"
    "s_add_u32 m0, s30, 0x20000\n"
    "ds_read_b64_tr_b16 a[198:199], v202 offset:1032\n"
    "v_mfma_f32_16x16x32_bf16 v[48:51], a[236:239], a[80:83], v[48:51]\n"
    "ds_read_b128 a[104:107], v246 offset:4096\n"
    "buffer_load_dwordx4 v206, s[52:55], s56 offen offset:0 lds\n"
    "v_mfma_f32_16x16x32_bf16 v[52:55], a[236:239], a[84:87], v[52:55]\n"
    "ds_read_b64_tr_b16 a[200:201], v203 offset:0\n"
    "v_mfma_f32_16x16x32_bf16 v[56:59], a[236:239], a[88:91], v[56:59]\n"
    "ds_read_b64_tr_b16 a[202:203], v203 offset:1024\n"
    "v_mfma_f32_16x16x32_bf16 v[60:63], a[236:239], a[92:95], v[60:63]\n"
    "ds_read_b128 a[108:111], v246 offset:6144\n"
    "v_mfma_f32_16x16x32_bf16 v[64:67], a[240:243], a[80:83], v[64:67]\n"
    "ds_read_b64_tr_b16 a[204:205], v203 offset:8\n"
    "buffer_load_dwordx4 v207, s[52:55], s56 offen offset:1024 lds\n"
    "v_mfma_f32_16x16x32_bf16 v[68:71], a[240:243], a[84:87], v[68:71]\n"
    "ds_read_b64_tr_b16 a[206:207], v203 offset:1032\n"
    "v_mfma_f32_16x16x32_bf16 v[72:75], a[240:243], a[88:91], v[72:75]\n"
    "ds_read_b128 a[112:115], v247 offset:0\n"
    "ds_read_b64_tr_b16 a[208:209], v204 offset:0\n"
    "v_mfma_f32_16x16x32_bf16 v[76:79], a[240:243], a[92:95], v[76:79]\n"
    "ds_read_b64_tr_b16 a[210:211], v204 offset:1024\n"
    "v_mfma_f32_16x16x32_bf16 v[80:83], a[244:247], a[80:83], v[80:83]\n"
    "ds_read_b128 a[116:119], v247 offset:2048\n"
    "buffer_load_dwordx4 v208, s[52:55], s56 offen offset:2048 lds\n"
    "v_mfma_f32_16x16x32_bf16 v[84:87], a[244:247], a[84:87], v[84:87]\n"
    "ds_read_b64_tr_b16 a[212:213], v204 offset:8\n"
    "v_mfma_f32_16x16x32_bf16 v[88:91], a[244:247], a[88:91], v[88:91]\n"
    "ds_read_b64_tr_b16 a[214:215], v204 offset:1032\n"
    "v_mfma_f32_16x16x32_bf16 v[92:95], a[244:247], a[92:95], v[92:95]\n"
    "ds_read_b128 a[120:123], v247 offset:4096\n"
    "v_mfma_f32_16x16x32_bf16 v[96:99], a[248:251], a[80:83], v[96:99]\n"
    "ds_read_b64_tr_b16 a[216:217], v205 offset:0\n"
    "buffer_load_dwordx4 v209, s[52:55], s56 offen offset:3072 lds\n"
    "v_mfma_f32_16x16x32_bf16 v[100:103], a[248:251], a[84:87], v[100:103]\n"
    "s_add_u32 s98, s98, 1\n"
    "s_and_b32 s98, s98, 7\n"
    "s_lshl_b32 s56, s98, 19\n"
    "s_add_u32 s56, s56, s99\n"
    "ds_read_b64_tr_b16 a[218:219], v205 offset:1024\n"
    "v_mfma_f32_16x16x32_bf16 v[104:107], a[248:251], a[88:91], v[104:107]\n"
    "ds_read_b128 a[124:127], v247 offset:6144\n"
    "v_mfma_f32_16x16x32_bf16 v[108:111], a[248:251], a[92:95], v[108:111]\n"
    "ds_read_b64_tr_b16 a[220:221], v205 offset:8\n"
    "v_mfma_f32_16x16x32_bf16 v[112:115], a[252:255], a[80:83], v[112:115]\n"
    "ds_read_b64_tr_b16 a[222:223], v205 offset:1032\n"
    "v_mfma_f32_16x16x32_bf16 v[116:119], a[252:255], a[84:87], v[116:119]\n"
    "v_mfma_f32_16x16x32_bf16 v[120:123], a[252:255], a[88:91], v[120:123]\n"
    "v_mfma_f32_16x16x32_bf16 v[124:127], a[252:255], a[92:95], v[124:127]\n"
    "s_waitcnt lgkmcnt(2)\n"
    "v_mfma_f32_16x16x32_bf16 v[0:3], a[192:195], a[96:99], v[0:3]\n"
    "v_mfma_f32_16x16x32_bf16 v[4:7], a[192:195], a[100:103], v[4:7]\n"
    "s_waitcnt lgkmcnt(0)\n"
    "ds_read_b64_tr_b16 a[224:225], v202 offset:8192\n"
    "v_mfma_f32_16x16x32_bf16 v[8:11], a[192:195], a[104:107], v[8:11]\n"
    "s_add_u32 m0, s93, 0x0\n"
    "ds_read_b64_tr_b16 a[226:227], v202 offset:9216\n"
    "v_mfma_f32_16x16x32_bf16 v[12:15], a[192:195], a[108:111], v[12:15]\n"
    "buffer_load_dwordx4 v198, s[88:91], s94 offen offset:0 lds\n"
    "v_mfma_f32_16x16x32_bf16 v[16:19], a[196:199], a[96:99], v[16:19]\n"
    "ds_read_b64_tr_b16 a[228:229], v202 offset:8200\n"
    "v_mfma_f32_16x16x32_bf16 v[20:23], a[196:199], a[100:103], v[20:23]\n"
    "v_mfma_f32_16x16x32_bf16 v[24:27], a[196:199], a[104:107], v[24:27]\n"
    "ds_read_b64_tr_b16 a[230:231], v202 offset:9224\n"
    "buffer_load_dwordx4 v199, s[88:91], s94 offen offset:1024 lds\n"
    "s_waitcnt vmcnt(14)\n"
    "v_readfirstlane_b32 s96, v248\n"
    "v_readfirstlane_b32 s97, v249\n"
    "s_nop 2\n"
    "s_add_u32 s96, s96, 1\n"
    "s_addc_u32 s97, s97, 0\n"
    "v_mfma_f32_16x16x32_bf16 v[28:31], a[196:199], a[108:111], v[28:31]\n"
    "v_mfma_f32_16x16x32_bf16 v[32:35], a[200:203], a[96:99], v[32:35]\n"
    "s_and_b32 s38, s49, 7\n"
    "ds_read_b64_tr_b16 a[232:233], v203 offset:8192\n"
    "v_mfma_f32_16x16x32_bf16 v[36:39], a[200:203], a[100:103], v[36:39]\n"
    "s_lshr_b32 s46, s49, 3\n"
    "buffer_load_dwordx4 v200, s[88:91], s94 offen offset:2048 lds\n"
    "v_mfma_f32_16x16x32_bf16 v[40:43], a[200:203], a[104:107], v[40:43]\n"
    "s_and_b32 s39, s46, 3\n"
    "ds_read_b64_tr_b16 a[234:235], v203 offset:9216\n"
    "v_mfma_f32_16x16x32_bf16 v[44:47], a[200:203], a[108:111], v[44:47]\n"
    "s_lshr_b32 s40, s46, 2\n"
    "ds_read_b64_tr_b16 a[236:237], v203 offset:8200\n"
    "v_mfma_f32_16x16x32_bf16 v[48:51], a[204:207], a[96:99], v[48:51]\n"
    "s_lshl_b32 s46, s38, 5\n"
    "buffer_load_dwordx4 v201, s[88:91], s94 offen offset:3072 lds\n"
    "v_mfma_f32_16x16x32_bf16 v[52:55], a[204:207], a[100:103], v[52:55]\n"
    "s_lshl_b32 s47, s48, 3\n"
    "ds_read_b64_tr_b16 a[238:239], v203 offset:9224\n"
    "v_mfma_f32_16x16x32_bf16 v[56:59], a[204:207], a[104:107], v[56:59]\n"
    "s_add_u32 m0, s93, 0x1000\n"
    "v_mfma_f32_16x16x32_bf16 v[60:63], a[204:207], a[108:111], v[60:63]\n"
    "s_add_u32 s46, s46, s47\n"
    "ds_read_b64_tr_b16 a[240:241], v204 offset:8192\n"
    "buffer_load_dwordx4 v198, s[88:91], s95 offen offset:0 lds\n"
    "v_mfma_f32_16x16x32_bf16 v[64:67], a[208:211], a[96:99], v[64:67]\n"
    "s_lshl_b32 s46, s46, 2\n"
    "v_mfma_f32_16x16x32_bf16 v[68:71], a[208:211], a[100:103], v[68:71]\n"
    "s_add_u32 s46, s46, s39\n"
    "ds_read_b64_tr_b16 a[242:243], v204 offset:9216\n"
    "v_mfma_f32_16x16x32_bf16 v[72:75], a[208:211], a[104:107], v[72:75]\n"
    "s_lshl_b32 s46, s46, 14\n"
    "buffer_load_dwordx4 v199, s[88:91], s95 offen offset:1024 lds\n"
    "v_mfma_f32_16x16x32_bf16 v[76:79], a[208:211], a[108:111], v[76:79]\n"
    "s_add_u32 s62, s70, s46\n"
    "s_addc_u32 s63, s71, 0\n"
    "ds_read_b64_tr_b16 a[244:245], v204 offset:8200\n"
    "v_mfma_f32_16x16x32_bf16 v[80:83], a[212:215], a[96:99], v[80:83]\n"
    "s_lshl_b32 s46, s40, 5\n"
    "ds_read_b64_tr_b16 a[246:247], v204 offset:9224\n"
    "v_mfma_f32_16x16x32_bf16 v[84:87], a[212:215], a[100:103], v[84:87]\n"
    "s_lshl_b32 s47, s48, 3\n"
    "buffer_load_dwordx4 v200, s[88:91], s95 offen offset:2048 lds\n"
    "v_mfma_f32_16x16x32_bf16 v[88:91], a[212:215], a[104:107], v[88:91]\n"
    "s_add_u32 s46, s46, s47\n"
    "ds_read_b64_tr_b16 a[248:249], v205 offset:8192\n"
    "v_mfma_f32_16x16x32_bf16 v[92:95], a[212:215], a[108:111], v[92:95]\n"
    "s_lshl_b32 s46, s46, 14\n"
    "v_mfma_f32_16x16x32_bf16 v[96:99], a[216:219], a[96:99], v[96:99]\n"
    "s_add_u32 s46, s46, 0x1000000\n"
    "ds_read_b64_tr_b16 a[250:251], v205 offset:9216\n"
    "buffer_load_dwordx4 v201, s[88:91], s95 offen offset:3072 lds\n"
    "v_mfma_f32_16x16x32_bf16 v[100:103], a[216:219], a[100:103], v[100:103]\n"
    "s_add_u32 s60, s70, s46\n"
    "s_addc_u32 s61, s71, 0\n"
    "v_mfma_f32_16x16x32_bf16 v[104:107], a[216:219], a[104:107], v[104:107]\n"
    "s_add_u32 s94, s94, 128\n"
    "s_and_b32 s94, s94, 1023\n"
    "s_add_u32 s95, s94, 32768\n"
    "ds_read_b64_tr_b16 a[252:253], v205 offset:8200\n"
    "v_mfma_f32_16x16x32_bf16 v[108:111], a[216:219], a[108:111], v[108:111]\n"
    "s_lshl_b32 s46, s38, 2\n"
    "v_mfma_f32_16x16x32_bf16 v[112:115], a[220:223], a[96:99], v[112:115]\n"
    "s_add_u32 s46, s46, s39\n"
    "ds_read_b64_tr_b16 a[254:255], v205 offset:9224\n"
    "v_mfma_f32_16x16x32_bf16 v[116:119], a[220:223], a[100:103], v[116:119]\n"
    "s_lshl_b32 s46, s46, 16\n"
    "v_mfma_f32_16x16x32_bf16 v[120:123], a[220:223], a[104:107], v[120:123]\n"
    "s_lshl_b32 s47, s40, 7\n"
    "v_mfma_f32_16x16x32_bf16 v[124:127], a[220:223], a[108:111], v[124:127]\n"
    "s_waitcnt lgkmcnt(1)\n"
    "s_add_u32 s46, s46, s47\n"
    "v_mfma_f32_16x16x32_bf16 v[0:3], a[224:227], a[112:115], v[0:3]\n"
    "s_add_u32 s68, s68, s46\n"
    "s_addc_u32 s69, s69, 0\n"
    "v_mfma_f32_16x16x32_bf16 v[4:7], a[224:227], a[116:119], v[4:7]\n"
    "v_and_b32 v238, 7, v236\n"
    "v_mfma_f32_16x16x32_bf16 v[8:11], a[224:227], a[120:123], v[8:11]\n"
    "v_lshlrev_b32 v238, 3, v238\n"
    "v_mfma_f32_16x16x32_bf16 v[12:15], a[224:227], a[124:127], v[12:15]\n"
    "s_lshl_b32 s46, s38, 5\n"
    "v_mfma_f32_16x16x32_bf16 v[16:19], a[228:231], a[112:115], v[16:19]\n"
    "s_lshl_b32 s47, s48, 3\n"
    "v_mfma_f32_16x16x32_bf16 v[20:23], a[228:231], a[116:119], v[20:23]\n"
    "s_waitcnt vmcnt(12)\n"
    "s_waitcnt lgkmcnt(0)\n"
    "v_mfma_f32_16x16x32_bf16 v[24:27], a[228:231], a[120:123], v[24:27]\n"
    "s_barrier\n"
    "v_add_u32 v202, 0xffff8000, v202\n"
    "v_add_u32 v203, 0xffff8000, v203\n"
    "v_add_u32 v204, 0xffff8000, v204\n"
    "v_add_u32 v205, 0xffff8000, v205\n"
    "v_mfma_f32_16x16x32_bf16 v[28:31], a[228:231], a[124:127], v[28:31]\n"
    "s_add_u32 s46, s46, s47\n"
    "ds_read_b128 a[64:67], v244 offset:0\n"
    "ds_read_b64_tr_b16 a[192:193], v202 offset:0\n"
    "v_mfma_f32_16x16x32_bf16 v[32:35], a[232:235], a[112:115], v[32:35]\n"
    "s_lshl_b32 s46, s46, 3\n"
    "ds_read_b64_tr_b16 a[194:195], v202 offset:1024\n"
    "v_mfma_f32_16x16x32_bf16 v[36:39], a[232:235], a[116:119], v[36:39]\n"
    "s_add_u32 s46, s46, 4096\n"
    "ds_read_b128 a[68:71], v244 offset:2048\n"
    "v_mfma_f32_16x16x32_bf16 v[40:43], a[232:235], a[120:123], v[40:43]\n"
    "ds_read_b64_tr_b16 a[196:197], v202 offset:8\n"
    "v_add_u32 v239, s46, v238\n"
    "v_mfma_f32_16x16x32_bf16 v[44:47], a[232:235], a[124:127], v[44:47]\n"
    "s_add_u32 m0, s30, 0x24000\n"
    "ds_read_b64_tr_b16 a[198:199], v202 offset:1032\n"
    "v_mfma_f32_16x16x32_bf16 v[48:51], a[236:239], a[112:115], v[48:51]\n"
    "s_lshl_b32 s46, s40, 5\n"
    "ds_read_b128 a[72:75], v244 offset:4096\n"
    "buffer_load_dwordx4 v206, s[52:55], s56 offen offset:0 lds\n"
    "v_mfma_f32_16x16x32_bf16 v[52:55], a[236:239], a[116:119], v[52:55]\n"
    "s_add_u32 s46, s46, s47\n"
    "ds_read_b64_tr_b16 a[200:201], v203 offset:0\n"
    "v_mfma_f32_16x16x32_bf16 v[56:59], a[236:239], a[120:123], v[56:59]\n"
    "s_lshl_b32 s46, s46, 3\n"
    "ds_read_b64_tr_b16 a[202:203], v203 offset:1024\n"
    "v_mfma_f32_16x16x32_bf16 v[60:63], a[236:239], a[124:127], v[60:63]\n"
    "s_add_u32 s46, s46, 8192\n"
    "ds_read_b128 a[76:79], v244 offset:6144\n"
    "v_mfma_f32_16x16x32_bf16 v[64:67], a[240:243], a[112:115], v[64:67]\n"
    "ds_read_b64_tr_b16 a[204:205], v203 offset:8\n"
    "buffer_load_dwordx4 v207, s[52:55], s56 offen offset:1024 lds\n"
    "v_mfma_f32_16x16x32_bf16 v[68:71], a[240:243], a[116:119], v[68:71]\n"
    "ds_read_b64_tr_b16 a[206:207], v203 offset:1032\n"
    "v_add_u32 v238, s46, v238\n"
    "v_mfma_f32_16x16x32_bf16 v[72:75], a[240:243], a[120:123], v[72:75]\n"
    "ds_read_b128 a[80:83], v245 offset:0\n"
    "ds_read_b64_tr_b16 a[208:209], v204 offset:0\n"
    "v_mfma_f32_16x16x32_bf16 v[76:79], a[240:243], a[124:127], v[76:79]\n"
    "ds_read_b64_tr_b16 a[210:211], v204 offset:1024\n"
    "v_mfma_f32_16x16x32_bf16 v[80:83], a[244:247], a[112:115], v[80:83]\n"
    "ds_read_b128 a[84:87], v245 offset:2048\n"
    "buffer_load_dwordx4 v208, s[52:55], s56 offen offset:2048 lds\n"
    "v_mfma_f32_16x16x32_bf16 v[84:87], a[244:247], a[116:119], v[84:87]\n"
    "ds_read_b64_tr_b16 a[212:213], v204 offset:8\n"
    "v_mfma_f32_16x16x32_bf16 v[88:91], a[244:247], a[120:123], v[88:91]\n"
    "ds_read_b64_tr_b16 a[214:215], v204 offset:1032\n"
    "v_mfma_f32_16x16x32_bf16 v[92:95], a[244:247], a[124:127], v[92:95]\n"
    "ds_read_b128 a[88:91], v245 offset:4096\n"
    "v_mfma_f32_16x16x32_bf16 v[96:99], a[248:251], a[112:115], v[96:99]\n"
    "ds_read_b64_tr_b16 a[216:217], v205 offset:0\n"
    "buffer_load_dwordx4 v209, s[52:55], s56 offen offset:3072 lds\n"
    "v_mfma_f32_16x16x32_bf16 v[100:103], a[248:251], a[116:119], v[100:103]\n"
    "s_add_u32 s98, s98, 1\n"
    "s_and_b32 s98, s98, 7\n"
    "s_lshl_b32 s56, s98, 19\n"
    "s_add_u32 s56, s56, s99\n"
    "ds_read_b64_tr_b16 a[218:219], v205 offset:1024\n"
    "v_mfma_f32_16x16x32_bf16 v[104:107], a[248:251], a[120:123], v[104:107]\n"
    "ds_read_b128 a[92:95], v245 offset:6144\n"
    "v_mfma_f32_16x16x32_bf16 v[108:111], a[248:251], a[124:127], v[108:111]\n"
    "ds_read_b64_tr_b16 a[220:221], v205 offset:8\n"
    "v_mfma_f32_16x16x32_bf16 v[112:115], a[252:255], a[112:115], v[112:115]\n"
    "ds_read_b64_tr_b16 a[222:223], v205 offset:1032\n"
    "v_mfma_f32_16x16x32_bf16 v[116:119], a[252:255], a[116:119], v[116:119]\n"
    "v_mfma_f32_16x16x32_bf16 v[120:123], a[252:255], a[120:123], v[120:123]\n"
    "v_mfma_f32_16x16x32_bf16 v[124:127], a[252:255], a[124:127], v[124:127]\n"
    "s_waitcnt lgkmcnt(2)\n"
    "v_mfma_f32_16x16x32_bf16 v[0:3], a[192:195], a[64:67], v[0:3]\n"
    "v_mfma_f32_16x16x32_bf16 v[4:7], a[192:195], a[68:71], v[4:7]\n"
    "s_waitcnt lgkmcnt(0)\n"
    "ds_read_b64_tr_b16 a[224:225], v202 offset:8192\n"
    "v_mfma_f32_16x16x32_bf16 v[8:11], a[192:195], a[72:75], v[8:11]\n"
    "s_add_u32 m0, s92, 0x0\n"
    "ds_read_b64_tr_b16 a[226:227], v202 offset:9216\n"
    "v_mfma_f32_16x16x32_bf16 v[12:15], a[192:195], a[76:79], v[12:15]\n"
    "buffer_load_dwordx4 v198, s[88:91], s94 offen offset:0 lds\n"
    "v_mfma_f32_16x16x32_bf16 v[16:19], a[196:199], a[64:67], v[16:19]\n"
    "ds_read_b64_tr_b16 a[228:229], v202 offset:8200\n"
    "v_mfma_f32_16x16x32_bf16 v[20:23], a[196:199], a[68:71], v[20:23]\n"
    "v_mfma_f32_16x16x32_bf16 v[24:27], a[196:199], a[72:75], v[24:27]\n"
    "ds_read_b64_tr_b16 a[230:231], v202 offset:9224\n"
    "buffer_load_dwordx4 v199, s[88:91], s94 offen offset:1024 lds\n"
    "v_mfma_f32_16x16x32_bf16 v[28:31], a[196:199], a[76:79], v[28:31]\n"
    "v_mfma_f32_16x16x32_bf16 v[32:35], a[200:203], a[64:67], v[32:35]\n"
    "ds_read_b64_tr_b16 a[232:233], v203 offset:8192\n"
    "v_mfma_f32_16x16x32_bf16 v[36:39], a[200:203], a[68:71], v[36:39]\n"
    "buffer_load_dwordx4 v200, s[88:91], s94 offen offset:2048 lds\n"
    "v_mfma_f32_16x16x32_bf16 v[40:43], a[200:203], a[72:75], v[40:43]\n"
    "ds_read_b64_tr_b16 a[234:235], v203 offset:9216\n"
    "v_mfma_f32_16x16x32_bf16 v[44:47], a[200:203], a[76:79], v[44:47]\n"
    "ds_read_b64_tr_b16 a[236:237], v203 offset:8200\n"
    "v_mfma_f32_16x16x32_bf16 v[48:51], a[204:207], a[64:67], v[48:51]\n"
    "buffer_load_dwordx4 v201, s[88:91], s94 offen offset:3072 lds\n"
    "v_mfma_f32_16x16x32_bf16 v[52:55], a[204:207], a[68:71], v[52:55]\n"
    "ds_read_b64_tr_b16 a[238:239], v203 offset:9224\n"
    "v_mfma_f32_16x16x32_bf16 v[56:59], a[204:207], a[72:75], v[56:59]\n"
    "s_add_u32 m0, s92, 0x1000\n"
    "v_mfma_f32_16x16x32_bf16 v[60:63], a[204:207], a[76:79], v[60:63]\n"
    "ds_read_b64_tr_b16 a[240:241], v204 offset:8192\n"
    "buffer_load_dwordx4 v198, s[88:91], s95 offen offset:0 lds\n"
    "v_mfma_f32_16x16x32_bf16 v[64:67], a[208:211], a[64:67], v[64:67]\n"
    "v_mfma_f32_16x16x32_bf16 v[68:71], a[208:211], a[68:71], v[68:71]\n"
    "ds_read_b64_tr_b16 a[242:243], v204 offset:9216\n"
    "v_mfma_f32_16x16x32_bf16 v[72:75], a[208:211], a[72:75], v[72:75]\n"
    "buffer_load_dwordx4 v199, s[88:91], s95 offen offset:1024 lds\n"
    "v_mfma_f32_16x16x32_bf16 v[76:79], a[208:211], a[76:79], v[76:79]\n"
    "ds_read_b64_tr_b16 a[244:245], v204 offset:8200\n"
    "v_mfma_f32_16x16x32_bf16 v[80:83], a[212:215], a[64:67], v[80:83]\n"
    "ds_read_b64_tr_b16 a[246:247], v204 offset:9224\n"
    "v_mfma_f32_16x16x32_bf16 v[84:87], a[212:215], a[68:71], v[84:87]\n"
    "buffer_load_dwordx4 v200, s[88:91], s95 offen offset:2048 lds\n"
    "v_mfma_f32_16x16x32_bf16 v[88:91], a[212:215], a[72:75], v[88:91]\n"
    "ds_read_b64_tr_b16 a[248:249], v205 offset:8192\n"
    "v_mfma_f32_16x16x32_bf16 v[92:95], a[212:215], a[76:79], v[92:95]\n"
    "v_mfma_f32_16x16x32_bf16 v[96:99], a[216:219], a[64:67], v[96:99]\n"
    "ds_read_b64_tr_b16 a[250:251], v205 offset:9216\n"
    "buffer_load_dwordx4 v201, s[88:91], s95 offen offset:3072 lds\n"
    "v_mfma_f32_16x16x32_bf16 v[100:103], a[216:219], a[68:71], v[100:103]\n"
    "v_mfma_f32_16x16x32_bf16 v[104:107], a[216:219], a[72:75], v[104:107]\n"
    "s_add_u32 s94, s94, 128\n"
    "s_and_b32 s94, s94, 1023\n"
    "s_add_u32 s95, s94, 32768\n"
    "ds_read_b64_tr_b16 a[252:253], v205 offset:8200\n"
    "v_mfma_f32_16x16x32_bf16 v[108:111], a[216:219], a[76:79], v[108:111]\n"
    "v_mfma_f32_16x16x32_bf16 v[112:115], a[220:223], a[64:67], v[112:115]\n"
    "ds_read_b64_tr_b16 a[254:255], v205 offset:9224\n"
    "v_mfma_f32_16x16x32_bf16 v[116:119], a[220:223], a[68:71], v[116:119]\n"
    "v_mfma_f32_16x16x32_bf16 v[120:123], a[220:223], a[72:75], v[120:123]\n"
    "v_mfma_f32_16x16x32_bf16 v[124:127], a[220:223], a[76:79], v[124:127]\n"
    "s_waitcnt lgkmcnt(2)\n"
    "v_mfma_f32_16x16x32_bf16 v[0:3], a[224:227], a[80:83], v[0:3]\n"
    "v_mfma_f32_16x16x32_bf16 v[4:7], a[224:227], a[84:87], v[4:7]\n"
    "v_mfma_f32_16x16x32_bf16 v[8:11], a[224:227], a[88:91], v[8:11]\n"
    "v_mfma_f32_16x16x32_bf16 v[12:15], a[224:227], a[92:95], v[12:15]\n"
    "v_mfma_f32_16x16x32_bf16 v[16:19], a[228:231], a[80:83], v[16:19]\n"
    "v_mfma_f32_16x16x32_bf16 v[20:23], a[228:231], a[84:87], v[20:23]\n"
    "s_waitcnt vmcnt(12)\n"
    "s_waitcnt lgkmcnt(0)\n"
    "v_mfma_f32_16x16x32_bf16 v[24:27], a[228:231], a[88:91], v[24:27]\n"
    "s_barrier\n"
    "v_add_u32 v202, 0x4000, v202\n"
    "v_add_u32 v203, 0x4000, v203\n"
    "v_add_u32 v204, 0x4000, v204\n"
    "v_add_u32 v205, 0x4000, v205\n"
    "v_mfma_f32_16x16x32_bf16 v[28:31], a[228:231], a[92:95], v[28:31]\n"
    "ds_read_b128 a[96:99], v246 offset:0\n"
    "ds_read_b64_tr_b16 a[192:193], v202 offset:0\n"
    "v_mfma_f32_16x16x32_bf16 v[32:35], a[232:235], a[80:83], v[32:35]\n"
    "ds_read_b64_tr_b16 a[194:195], v202 offset:1024\n"
    "v_mfma_f32_16x16x32_bf16 v[36:39], a[232:235], a[84:87], v[36:39]\n"
    "ds_read_b128 a[100:103], v246 offset:2048\n"
    "v_mfma_f32_16x16x32_bf16 v[40:43], a[232:235], a[88:91], v[40:43]\n"
    "ds_read_b64_tr_b16 a[196:197], v202 offset:8\n"
    "v_mfma_f32_16x16x32_bf16 v[44:47], a[232:235], a[92:95], v[44:47]\n"
    "s_add_u32 m0, s30, 0x1c000\n"
    "ds_read_b64_tr_b16 a[198:199], v202 offset:1032\n"
    "v_mfma_f32_16x16x32_bf16 v[48:51], a[236:239], a[80:83], v[48:51]\n"
    "ds_read_b128 a[104:107], v246 offset:4096\n"
    "buffer_load_dwordx4 v206, s[52:55], s56 offen offset:0 lds\n"
    "v_mfma_f32_16x16x32_bf16 v[52:55], a[236:239], a[84:87], v[52:55]\n"
    "ds_read_b64_tr_b16 a[200:201], v203 offset:0\n"
    "v_mfma_f32_16x16x32_bf16 v[56:59], a[236:239], a[88:91], v[56:59]\n"
    "ds_read_b64_tr_b16 a[202:203], v203 offset:1024\n"
    "v_mfma_f32_16x16x32_bf16 v[60:63], a[236:239], a[92:95], v[60:63]\n"
    "ds_read_b128 a[108:111], v246 offset:6144\n"
    "v_mfma_f32_16x16x32_bf16 v[64:67], a[240:243], a[80:83], v[64:67]\n"
    "ds_read_b64_tr_b16 a[204:205], v203 offset:8\n"
    "buffer_load_dwordx4 v207, s[52:55], s56 offen offset:1024 lds\n"
    "v_mfma_f32_16x16x32_bf16 v[68:71], a[240:243], a[84:87], v[68:71]\n"
    "ds_read_b64_tr_b16 a[206:207], v203 offset:1032\n"
    "v_mfma_f32_16x16x32_bf16 v[72:75], a[240:243], a[88:91], v[72:75]\n"
    "ds_read_b128 a[112:115], v247 offset:0\n"
    "ds_read_b64_tr_b16 a[208:209], v204 offset:0\n"
    "v_mfma_f32_16x16x32_bf16 v[76:79], a[240:243], a[92:95], v[76:79]\n"
    "ds_read_b64_tr_b16 a[210:211], v204 offset:1024\n"
    "v_mfma_f32_16x16x32_bf16 v[80:83], a[244:247], a[80:83], v[80:83]\n"
    "ds_read_b128 a[116:119], v247 offset:2048\n"
    "buffer_load_dwordx4 v208, s[52:55], s56 offen offset:2048 lds\n"
    "v_mfma_f32_16x16x32_bf16 v[84:87], a[244:247], a[84:87], v[84:87]\n"
    "ds_read_b64_tr_b16 a[212:213], v204 offset:8\n"
    "v_mfma_f32_16x16x32_bf16 v[88:91], a[244:247], a[88:91], v[88:91]\n"
    "ds_read_b64_tr_b16 a[214:215], v204 offset:1032\n"
    "v_mfma_f32_16x16x32_bf16 v[92:95], a[244:247], a[92:95], v[92:95]\n"
    "ds_read_b128 a[120:123], v247 offset:4096\n"
    "v_mfma_f32_16x16x32_bf16 v[96:99], a[248:251], a[80:83], v[96:99]\n"
    "ds_read_b64_tr_b16 a[216:217], v205 offset:0\n"
    "buffer_load_dwordx4 v209, s[52:55], s56 offen offset:3072 lds\n"
    "v_mfma_f32_16x16x32_bf16 v[100:103], a[248:251], a[84:87], v[100:103]\n"
    "s_add_u32 s98, s98, 1\n"
    "s_and_b32 s98, s98, 7\n"
    "s_lshl_b32 s56, s98, 19\n"
    "s_add_u32 s56, s56, s99\n"
    "ds_read_b64_tr_b16 a[218:219], v205 offset:1024\n"
    "v_mfma_f32_16x16x32_bf16 v[104:107], a[248:251], a[88:91], v[104:107]\n"
    "ds_read_b128 a[124:127], v247 offset:6144\n"
    "v_mfma_f32_16x16x32_bf16 v[108:111], a[248:251], a[92:95], v[108:111]\n"
    "ds_read_b64_tr_b16 a[220:221], v205 offset:8\n"
    "v_mfma_f32_16x16x32_bf16 v[112:115], a[252:255], a[80:83], v[112:115]\n"
    "ds_read_b64_tr_b16 a[222:223], v205 offset:1032\n"
    "v_mfma_f32_16x16x32_bf16 v[116:119], a[252:255], a[84:87], v[116:119]\n"
    "v_mfma_f32_16x16x32_bf16 v[120:123], a[252:255], a[88:91], v[120:123]\n"
    "v_mfma_f32_16x16x32_bf16 v[124:127], a[252:255], a[92:95], v[124:127]\n"
    "s_waitcnt lgkmcnt(2)\n"
    "v_mfma_f32_16x16x32_bf16 v[0:3], a[192:195], a[96:99], v[0:3]\n"
    "v_mfma_f32_16x16x32_bf16 v[4:7], a[192:195], a[100:103], v[4:7]\n"
    "s_waitcnt lgkmcnt(0)\n"
    "ds_read_b64_tr_b16 a[224:225], v202 offset:8192\n"
    "v_mfma_f32_16x16x32_bf16 v[8:11], a[192:195], a[104:107], v[8:11]\n"
    "s_add_u32 m0, s93, 0x0\n"
    "ds_read_b64_tr_b16 a[226:227], v202 offset:9216\n"
    "v_mfma_f32_16x16x32_bf16 v[12:15], a[192:195], a[108:111], v[12:15]\n"
    "buffer_load_dwordx4 v198, s[88:91], s94 offen offset:0 lds\n"
    "v_mfma_f32_16x16x32_bf16 v[16:19], a[196:199], a[96:99], v[16:19]\n"
    "ds_read_b64_tr_b16 a[228:229], v202 offset:8200\n"
    "v_mfma_f32_16x16x32_bf16 v[20:23], a[196:199], a[100:103], v[20:23]\n"
    "v_mfma_f32_16x16x32_bf16 v[24:27], a[196:199], a[104:107], v[24:27]\n"
    "ds_read_b64_tr_b16 a[230:231], v202 offset:9224\n"
    "buffer_load_dwordx4 v199, s[88:91], s94 offen offset:1024 lds\n"
    "v_mfma_f32_16x16x32_bf16 v[28:31], a[196:199], a[108:111], v[28:31]\n"
    "v_mfma_f32_16x16x32_bf16 v[32:35], a[200:203], a[96:99], v[32:35]\n"
    "ds_read_b64_tr_b16 a[232:233], v203 offset:8192\n"
    "v_mfma_f32_16x16x32_bf16 v[36:39], a[200:203], a[100:103], v[36:39]\n"
    "buffer_load_dwordx4 v200, s[88:91], s94 offen offset:2048 lds\n"
    "v_mfma_f32_16x16x32_bf16 v[40:43], a[200:203], a[104:107], v[40:43]\n"
    "ds_read_b64_tr_b16 a[234:235], v203 offset:9216\n"
    "v_mfma_f32_16x16x32_bf16 v[44:47], a[200:203], a[108:111], v[44:47]\n"
    "ds_read_b64_tr_b16 a[236:237], v203 offset:8200\n"
    "v_mfma_f32_16x16x32_bf16 v[48:51], a[204:207], a[96:99], v[48:51]\n"
    "buffer_load_dwordx4 v201, s[88:91], s94 offen offset:3072 lds\n"
    "v_mfma_f32_16x16x32_bf16 v[52:55], a[204:207], a[100:103], v[52:55]\n"
    "ds_read_b64_tr_b16 a[238:239], v203 offset:9224\n"
    "v_mfma_f32_16x16x32_bf16 v[56:59], a[204:207], a[104:107], v[56:59]\n"
    "s_add_u32 m0, s93, 0x1000\n"
    "v_mfma_f32_16x16x32_bf16 v[60:63], a[204:207], a[108:111], v[60:63]\n"
    "ds_read_b64_tr_b16 a[240:241], v204 offset:8192\n"
    "buffer_load_dwordx4 v198, s[88:91], s95 offen offset:0 lds\n"
    "v_mfma_f32_16x16x32_bf16 v[64:67], a[208:211], a[96:99], v[64:67]\n"
    "v_mfma_f32_16x16x32_bf16 v[68:71], a[208:211], a[100:103], v[68:71]\n"
    "ds_read_b64_tr_b16 a[242:243], v204 offset:9216\n"
    "v_mfma_f32_16x16x32_bf16 v[72:75], a[208:211], a[104:107], v[72:75]\n"
    "buffer_load_dwordx4 v199, s[88:91], s95 offen offset:1024 lds\n"
    "v_mfma_f32_16x16x32_bf16 v[76:79], a[208:211], a[108:111], v[76:79]\n"
    "ds_read_b64_tr_b16 a[244:245], v204 offset:8200\n"
    "v_mfma_f32_16x16x32_bf16 v[80:83], a[212:215], a[96:99], v[80:83]\n"
    "ds_read_b64_tr_b16 a[246:247], v204 offset:9224\n"
    "v_mfma_f32_16x16x32_bf16 v[84:87], a[212:215], a[100:103], v[84:87]\n"
    "buffer_load_dwordx4 v200, s[88:91], s95 offen offset:2048 lds\n"
    "v_mfma_f32_16x16x32_bf16 v[88:91], a[212:215], a[104:107], v[88:91]\n"
    "ds_read_b64_tr_b16 a[248:249], v205 offset:8192\n"
    "v_mfma_f32_16x16x32_bf16 v[92:95], a[212:215], a[108:111], v[92:95]\n"
    "v_mfma_f32_16x16x32_bf16 v[96:99], a[216:219], a[96:99], v[96:99]\n"
    "ds_read_b64_tr_b16 a[250:251], v205 offset:9216\n"
    "buffer_load_dwordx4 v201, s[88:91], s95 offen offset:3072 lds\n"
    "v_mfma_f32_16x16x32_bf16 v[100:103], a[216:219], a[100:103], v[100:103]\n"
    "v_mfma_f32_16x16x32_bf16 v[104:107], a[216:219], a[104:107], v[104:107]\n"
    "s_add_u32 s94, s94, 128\n"
    "s_and_b32 s94, s94, 1023\n"
    "s_add_u32 s95, s94, 32768\n"
    "ds_read_b64_tr_b16 a[252:253], v205 offset:8200\n"
    "v_mfma_f32_16x16x32_bf16 v[108:111], a[216:219], a[108:111], v[108:111]\n"
    "v_mfma_f32_16x16x32_bf16 v[112:115], a[220:223], a[96:99], v[112:115]\n"
    "ds_read_b64_tr_b16 a[254:255], v205 offset:9224\n"
    "v_mfma_f32_16x16x32_bf16 v[116:119], a[220:223], a[100:103], v[116:119]\n"
    "v_mfma_f32_16x16x32_bf16 v[120:123], a[220:223], a[104:107], v[120:123]\n"
    "v_mfma_f32_16x16x32_bf16 v[124:127], a[220:223], a[108:111], v[124:127]\n"
    "s_waitcnt lgkmcnt(2)\n"
    "v_mfma_f32_16x16x32_bf16 v[0:3], a[224:227], a[112:115], v[0:3]\n"
    "v_mfma_f32_16x16x32_bf16 v[4:7], a[224:227], a[116:119], v[4:7]\n"
    "v_mfma_f32_16x16x32_bf16 v[8:11], a[224:227], a[120:123], v[8:11]\n"
    "v_mfma_f32_16x16x32_bf16 v[12:15], a[224:227], a[124:127], v[12:15]\n"
    "v_mfma_f32_16x16x32_bf16 v[16:19], a[228:231], a[112:115], v[16:19]\n"
    "v_mfma_f32_16x16x32_bf16 v[20:23], a[228:231], a[116:119], v[20:23]\n"
    "s_waitcnt vmcnt(12)\n"
    "s_waitcnt lgkmcnt(0)\n"
    "v_mfma_f32_16x16x32_bf16 v[24:27], a[228:231], a[120:123], v[24:27]\n"
    "s_barrier\n"
    "v_add_u32 v202, 0x4000, v202\n"
    "v_add_u32 v203, 0x4000, v203\n"
    "v_add_u32 v204, 0x4000, v204\n"
    "v_add_u32 v205, 0x4000, v205\n"
    "v_mfma_f32_16x16x32_bf16 v[28:31], a[228:231], a[124:127], v[28:31]\n"
    "ds_read_b128 a[64:67], v244 offset:0\n"
    "ds_read_b64_tr_b16 a[192:193], v202 offset:0\n"
    "v_mfma_f32_16x16x32_bf16 v[32:35], a[232:235], a[112:115], v[32:35]\n"
    "ds_read_b64_tr_b16 a[194:195], v202 offset:1024\n"
    "v_mfma_f32_16x16x32_bf16 v[36:39], a[232:235], a[116:119], v[36:39]\n"
    "ds_read_b128 a[68:71], v244 offset:2048\n"
    "v_mfma_f32_16x16x32_bf16 v[40:43], a[232:235], a[120:123], v[40:43]\n"
    "ds_read_b64_tr_b16 a[196:197], v202 offset:8\n"
    "v_mfma_f32_16x16x32_bf16 v[44:47], a[232:235], a[124:127], v[44:47]\n"
    "s_add_u32 m0, s30, 0x20000\n"
    "ds_read_b64_tr_b16 a[198:199], v202 offset:1032\n"
    "v_mfma_f32_16x16x32_bf16 v[48:51], a[236:239], a[112:115], v[48:51]\n"
    "ds_read_b128 a[72:75], v244 offset:4096\n"
    "buffer_load_dwordx4 v206, s[52:55], s56 offen offset:0 lds\n"
    "v_mfma_f32_16x16x32_bf16 v[52:55], a[236:239], a[116:119], v[52:55]\n"
    "ds_read_b64_tr_b16 a[200:201], v203 offset:0\n"
    "v_mfma_f32_16x16x32_bf16 v[56:59], a[236:239], a[120:123], v[56:59]\n"
    "ds_read_b64_tr_b16 a[202:203], v203 offset:1024\n"
    "v_mfma_f32_16x16x32_bf16 v[60:63], a[236:239], a[124:127], v[60:63]\n"
    "ds_read_b128 a[76:79], v244 offset:6144\n"
    "v_mfma_f32_16x16x32_bf16 v[64:67], a[240:243], a[112:115], v[64:67]\n"
    "ds_read_b64_tr_b16 a[204:205], v203 offset:8\n"
    "buffer_load_dwordx4 v207, s[52:55], s56 offen offset:1024 lds\n"
    "v_mfma_f32_16x16x32_bf16 v[68:71], a[240:243], a[116:119], v[68:71]\n"
    "ds_read_b64_tr_b16 a[206:207], v203 offset:1032\n"
    "v_mfma_f32_16x16x32_bf16 v[72:75], a[240:243], a[120:123], v[72:75]\n"
    "ds_read_b128 a[80:83], v245 offset:0\n"
    "ds_read_b64_tr_b16 a[208:209], v204 offset:0\n"
    "v_mfma_f32_16x16x32_bf16 v[76:79], a[240:243], a[124:127], v[76:79]\n"
    "ds_read_b64_tr_b16 a[210:211], v204 offset:1024\n"
    "v_mfma_f32_16x16x32_bf16 v[80:83], a[244:247], a[112:115], v[80:83]\n"
    "ds_read_b128 a[84:87], v245 offset:2048\n"
    "buffer_load_dwordx4 v208, s[52:55], s56 offen offset:2048 lds\n"
    "v_mfma_f32_16x16x32_bf16 v[84:87], a[244:247], a[116:119], v[84:87]\n"
    "ds_read_b64_tr_b16 a[212:213], v204 offset:8\n"
    "v_mfma_f32_16x16x32_bf16 v[88:91], a[244:247], a[120:123], v[88:91]\n"
    "ds_read_b64_tr_b16 a[214:215], v204 offset:1032\n"
    "v_mfma_f32_16x16x32_bf16 v[92:95], a[244:247], a[124:127], v[92:95]\n"
    "ds_read_b128 a[88:91], v245 offset:4096\n"
    "v_mfma_f32_16x16x32_bf16 v[96:99], a[248:251], a[112:115], v[96:99]\n"
    "ds_read_b64_tr_b16 a[216:217], v205 offset:0\n"
    "buffer_load_dwordx4 v209, s[52:55], s56 offen offset:3072 lds\n"
    "v_mfma_f32_16x16x32_bf16 v[100:103], a[248:251], a[116:119], v[100:103]\n"
    "s_add_u32 s98, s98, 1\n"
    "s_and_b32 s98, s98, 7\n"
    "s_lshl_b32 s56, s98, 19\n"
    "s_add_u32 s56, s56, s99\n"
    "ds_read_b64_tr_b16 a[218:219], v205 offset:1024\n"
    "v_mfma_f32_16x16x32_bf16 v[104:107], a[248:251], a[120:123], v[104:107]\n"
    "ds_read_b128 a[92:95], v245 offset:6144\n"
    "v_mfma_f32_16x16x32_bf16 v[108:111], a[248:251], a[124:127], v[108:111]\n"
    "ds_read_b64_tr_b16 a[220:221], v205 offset:8\n"
    "v_mfma_f32_16x16x32_bf16 v[112:115], a[252:255], a[112:115], v[112:115]\n"
    "ds_read_b64_tr_b16 a[222:223], v205 offset:1032\n"
    "v_mfma_f32_16x16x32_bf16 v[116:119], a[252:255], a[116:119], v[116:119]\n"
    "v_mfma_f32_16x16x32_bf16 v[120:123], a[252:255], a[120:123], v[120:123]\n"
    "v_mfma_f32_16x16x32_bf16 v[124:127], a[252:255], a[124:127], v[124:127]\n"
    "s_waitcnt lgkmcnt(2)\n"
    "v_mfma_f32_16x16x32_bf16 v[0:3], a[192:195], a[64:67], v[0:3]\n"
    "v_mfma_f32_16x16x32_bf16 v[4:7], a[192:195], a[68:71], v[4:7]\n"
    "s_waitcnt lgkmcnt(0)\n"
    "ds_read_b64_tr_b16 a[224:225], v202 offset:8192\n"
    "v_mfma_f32_16x16x32_bf16 v[8:11], a[192:195], a[72:75], v[8:11]\n"
    "s_add_u32 m0, s92, 0x0\n"
    "ds_read_b64_tr_b16 a[226:227], v202 offset:9216\n"
    "v_mfma_f32_16x16x32_bf16 v[12:15], a[192:195], a[76:79], v[12:15]\n"
    "buffer_load_dwordx4 v198, s[88:91], s94 offen offset:0 lds\n"
    "v_mfma_f32_16x16x32_bf16 v[16:19], a[196:199], a[64:67], v[16:19]\n"
    "ds_read_b64_tr_b16 a[228:229], v202 offset:8200\n"
    "v_mfma_f32_16x16x32_bf16 v[20:23], a[196:199], a[68:71], v[20:23]\n"
    "v_mfma_f32_16x16x32_bf16 v[24:27], a[196:199], a[72:75], v[24:27]\n"
    "ds_read_b64_tr_b16 a[230:231], v202 offset:9224\n"
    "buffer_load_dwordx4 v199, s[88:91], s94 offen offset:1024 lds\n"
    "v_mfma_f32_16x16x32_bf16 v[28:31], a[196:199], a[76:79], v[28:31]\n"
    "v_mfma_f32_16x16x32_bf16 v[32:35], a[200:203], a[64:67], v[32:35]\n"
    "ds_read_b64_tr_b16 a[232:233], v203 offset:8192\n"
    "v_mfma_f32_16x16x32_bf16 v[36:39], a[200:203], a[68:71], v[36:39]\n"
    "buffer_load_dwordx4 v200, s[88:91], s94 offen offset:2048 lds\n"
    "v_mfma_f32_16x16x32_bf16 v[40:43], a[200:203], a[72:75], v[40:43]\n"
    "ds_read_b64_tr_b16 a[234:235], v203 offset:9216\n"
    "v_mfma_f32_16x16x32_bf16 v[44:47], a[200:203], a[76:79], v[44:47]\n"
    "ds_read_b64_tr_b16 a[236:237], v203 offset:8200\n"
    "v_mfma_f32_16x16x32_bf16 v[48:51], a[204:207], a[64:67], v[48:51]\n"
    "buffer_load_dwordx4 v201, s[88:91], s94 offen offset:3072 lds\n"
    "v_mfma_f32_16x16x32_bf16 v[52:55], a[204:207], a[68:71], v[52:55]\n"
    "ds_read_b64_tr_b16 a[238:239], v203 offset:9224\n"
    "v_mfma_f32_16x16x32_bf16 v[56:59], a[204:207], a[72:75], v[56:59]\n"
    "s_add_u32 m0, s92, 0x1000\n"
    "v_mfma_f32_16x16x32_bf16 v[60:63], a[204:207], a[76:79], v[60:63]\n"
    "ds_read_b64_tr_b16 a[240:241], v204 offset:8192\n"
    "buffer_load_dwordx4 v198, s[88:91], s95 offen offset:0 lds\n"
    "v_mfma_f32_16x16x32_bf16 v[64:67], a[208:211], a[64:67], v[64:67]\n"
    "v_mfma_f32_16x16x32_bf16 v[68:71], a[208:211], a[68:71], v[68:71]\n"
    "ds_read_b64_tr_b16 a[242:243], v204 offset:9216\n"
    "v_mfma_f32_16x16x32_bf16 v[72:75], a[208:211], a[72:75], v[72:75]\n"
    "buffer_load_dwordx4 v199, s[88:91], s95 offen offset:1024 lds\n"
    "v_mfma_f32_16x16x32_bf16 v[76:79], a[208:211], a[76:79], v[76:79]\n"
    "ds_read_b64_tr_b16 a[244:245], v204 offset:8200\n"
    "v_mfma_f32_16x16x32_bf16 v[80:83], a[212:215], a[64:67], v[80:83]\n"
    "ds_read_b64_tr_b16 a[246:247], v204 offset:9224\n"
    "v_mfma_f32_16x16x32_bf16 v[84:87], a[212:215], a[68:71], v[84:87]\n"
    "buffer_load_dwordx4 v200, s[88:91], s95 offen offset:2048 lds\n"
    "v_mfma_f32_16x16x32_bf16 v[88:91], a[212:215], a[72:75], v[88:91]\n"
    "ds_read_b64_tr_b16 a[248:249], v205 offset:8192\n"
    "v_mfma_f32_16x16x32_bf16 v[92:95], a[212:215], a[76:79], v[92:95]\n"
    "v_mfma_f32_16x16x32_bf16 v[96:99], a[216:219], a[64:67], v[96:99]\n"
    "ds_read_b64_tr_b16 a[250:251], v205 offset:9216\n"
    "buffer_load_dwordx4 v201, s[88:91], s95 offen offset:3072 lds\n"
    "v_mfma_f32_16x16x32_bf16 v[100:103], a[216:219], a[68:71], v[100:103]\n"
    "v_mfma_f32_16x16x32_bf16 v[104:107], a[216:219], a[72:75], v[104:107]\n"
    "s_add_u32 s94, s94, 128\n"
    "s_and_b32 s94, s94, 1023\n"
    "s_add_u32 s95, s94, 32768\n"
    "ds_read_b64_tr_b16 a[252:253], v205 offset:8200\n"
    "v_mfma_f32_16x16x32_bf16 v[108:111], a[216:219], a[76:79], v[108:111]\n"
    "v_mfma_f32_16x16x32_bf16 v[112:115], a[220:223], a[64:67], v[112:115]\n"
    "ds_read_b64_tr_b16 a[254:255], v205 offset:9224\n"
    "v_mfma_f32_16x16x32_bf16 v[116:119], a[220:223], a[68:71], v[116:119]\n"
    "v_mfma_f32_16x16x32_bf16 v[120:123], a[220:223], a[72:75], v[120:123]\n"
    "v_mfma_f32_16x16x32_bf16 v[124:127], a[220:223], a[76:79], v[124:127]\n"
    "s_waitcnt lgkmcnt(2)\n"
    "v_mfma_f32_16x16x32_bf16 v[0:3], a[224:227], a[80:83], v[0:3]\n"
    "v_mfma_f32_16x16x32_bf16 v[4:7], a[224:227], a[84:87], v[4:7]\n"
    "v_mfma_f32_16x16x32_bf16 v[8:11], a[224:227], a[88:91], v[8:11]\n"
    "v_mfma_f32_16x16x32_bf16 v[12:15], a[224:227], a[92:95], v[12:15]\n"
    "v_mfma_f32_16x16x32_bf16 v[16:19], a[228:231], a[80:83], v[16:19]\n"
    "v_mfma_f32_16x16x32_bf16 v[20:23], a[228:231], a[84:87], v[20:23]\n"
    "s_waitcnt vmcnt(12)\n"
    "s_waitcnt lgkmcnt(0)\n"
    "v_mfma_f32_16x16x32_bf16 v[24:27], a[228:231], a[88:91], v[24:27]\n"
    "s_barrier\n"
    "v_add_u32 v202, 0xffff8000, v202\n"
    "v_add_u32 v203, 0xffff8000, v203\n"
    "v_add_u32 v204, 0xffff8000, v204\n"
    "v_add_u32 v205, 0xffff8000, v205\n"
    "v_mfma_f32_16x16x32_bf16 v[28:31], a[228:231], a[92:95], v[28:31]\n"
    "ds_read_b128 a[96:99], v246 offset:0\n"
    "ds_read_b64_tr_b16 a[192:193], v202 offset:0\n"
    "v_mfma_f32_16x16x32_bf16 v[32:35], a[232:235], a[80:83], v[32:35]\n"
    "ds_read_b64_tr_b16 a[194:195], v202 offset:1024\n"
    "v_mfma_f32_16x16x32_bf16 v[36:39], a[232:235], a[84:87], v[36:39]\n"
    "ds_read_b128 a[100:103], v246 offset:2048\n"
    "v_mfma_f32_16x16x32_bf16 v[40:43], a[232:235], a[88:91], v[40:43]\n"
    "ds_read_b64_tr_b16 a[196:197], v202 offset:8\n"
    "v_mfma_f32_16x16x32_bf16 v[44:47], a[232:235], a[92:95], v[44:47]\n"
    "s_add_u32 m0, s30, 0x24000\n"
    "ds_read_b64_tr_b16 a[198:199], v202 offset:1032\n"
    "v_mfma_f32_16x16x32_bf16 v[48:51], a[236:239], a[80:83], v[48:51]\n"
    "ds_read_b128 a[104:107], v246 offset:4096\n"
    "buffer_load_dwordx4 v206, s[52:55], s56 offen offset:0 lds\n"
    "v_mfma_f32_16x16x32_bf16 v[52:55], a[236:239], a[84:87], v[52:55]\n"
    "ds_read_b64_tr_b16 a[200:201], v203 offset:0\n"
    "v_mfma_f32_16x16x32_bf16 v[56:59], a[236:239], a[88:91], v[56:59]\n"
    "ds_read_b64_tr_b16 a[202:203], v203 offset:1024\n"
    "v_mfma_f32_16x16x32_bf16 v[60:63], a[236:239], a[92:95], v[60:63]\n"
    "ds_read_b128 a[108:111], v246 offset:6144\n"
    "v_mfma_f32_16x16x32_bf16 v[64:67], a[240:243], a[80:83], v[64:67]\n"
    "ds_read_b64_tr_b16 a[204:205], v203 offset:8\n"
    "buffer_load_dwordx4 v207, s[52:55], s56 offen offset:1024 lds\n"
    "v_mfma_f32_16x16x32_bf16 v[68:71], a[240:243], a[84:87], v[68:71]\n"
    "ds_read_b64_tr_b16 a[206:207], v203 offset:1032\n"
    "v_mfma_f32_16x16x32_bf16 v[72:75], a[240:243], a[88:91], v[72:75]\n"
    "ds_read_b128 a[112:115], v247 offset:0\n"
    "ds_read_b64_tr_b16 a[208:209], v204 offset:0\n"
    "v_mfma_f32_16x16x32_bf16 v[76:79], a[240:243], a[92:95], v[76:79]\n"
    "ds_read_b64_tr_b16 a[210:211], v204 offset:1024\n"
    "v_mfma_f32_16x16x32_bf16 v[80:83], a[244:247], a[80:83], v[80:83]\n"
    "ds_read_b128 a[116:119], v247 offset:2048\n"
    "buffer_load_dwordx4 v208, s[52:55], s56 offen offset:2048 lds\n"
    "v_mfma_f32_16x16x32_bf16 v[84:87], a[244:247], a[84:87], v[84:87]\n"
    "ds_read_b64_tr_b16 a[212:213], v204 offset:8\n"
    "v_mfma_f32_16x16x32_bf16 v[88:91], a[244:247], a[88:91], v[88:91]\n"
    "ds_read_b64_tr_b16 a[214:215], v204 offset:1032\n"
    "v_mfma_f32_16x16x32_bf16 v[92:95], a[244:247], a[92:95], v[92:95]\n"
    "ds_read_b128 a[120:123], v247 offset:4096\n"
    "v_mfma_f32_16x16x32_bf16 v[96:99], a[248:251], a[80:83], v[96:99]\n"
    "ds_read_b64_tr_b16 a[216:217], v205 offset:0\n"
    "buffer_load_dwordx4 v209, s[52:55], s56 offen offset:3072 lds\n"
    "v_mfma_f32_16x16x32_bf16 v[100:103], a[248:251], a[84:87], v[100:103]\n"
    "s_add_u32 s98, s98, 1\n"
    "s_and_b32 s98, s98, 7\n"
    "s_lshl_b32 s56, s98, 19\n"
    "s_add_u32 s56, s56, s99\n"
    "ds_read_b64_tr_b16 a[218:219], v205 offset:1024\n"
    "v_mfma_f32_16x16x32_bf16 v[104:107], a[248:251], a[88:91], v[104:107]\n"
    "ds_read_b128 a[124:127], v247 offset:6144\n"
    "v_mfma_f32_16x16x32_bf16 v[108:111], a[248:251], a[92:95], v[108:111]\n"
    "ds_read_b64_tr_b16 a[220:221], v205 offset:8\n"
    "v_mfma_f32_16x16x32_bf16 v[112:115], a[252:255], a[80:83], v[112:115]\n"
    "ds_read_b64_tr_b16 a[222:223], v205 offset:1032\n"
    "v_mfma_f32_16x16x32_bf16 v[116:119], a[252:255], a[84:87], v[116:119]\n"
    "v_mfma_f32_16x16x32_bf16 v[120:123], a[252:255], a[88:91], v[120:123]\n"
    "v_mfma_f32_16x16x32_bf16 v[124:127], a[252:255], a[92:95], v[124:127]\n"
    "s_waitcnt lgkmcnt(2)\n"
    "v_mfma_f32_16x16x32_bf16 v[0:3], a[192:195], a[96:99], v[0:3]\n"
    "v_mfma_f32_16x16x32_bf16 v[4:7], a[192:195], a[100:103], v[4:7]\n"
    "s_waitcnt lgkmcnt(0)\n"
    "ds_read_b64_tr_b16 a[224:225], v202 offset:8192\n"
    "v_mfma_f32_16x16x32_bf16 v[8:11], a[192:195], a[104:107], v[8:11]\n"
    "s_add_u32 m0, s93, 0x0\n"
    "ds_read_b64_tr_b16 a[226:227], v202 offset:9216\n"
    "ds_read_b64_tr_b16 v[128:129], v224 offset:16384\n"
    "v_mfma_f32_16x16x32_bf16 v[12:15], a[192:195], a[108:111], v[12:15]\n"
    "buffer_load_dwordx4 v198, s[88:91], s94 offen offset:0 lds\n"
    "ds_read_b64_tr_b16 v[130:131], v224 offset:18432\n"
    "v_mfma_f32_16x16x32_bf16 v[16:19], a[196:199], a[96:99], v[16:19]\n"
    "ds_read_b64_tr_b16 a[228:229], v202 offset:8200\n"
    "ds_read_b64_tr_b16 v[132:133], v224 offset:16392\n"
    "v_mfma_f32_16x16x32_bf16 v[20:23], a[196:199], a[100:103], v[20:23]\n"
    "ds_read_b64_tr_b16 v[134:135], v224 offset:18440\n"
    "v_mfma_f32_16x16x32_bf16 v[24:27], a[196:199], a[104:107], v[24:27]\n"
    "ds_read_b64_tr_b16 a[230:231], v202 offset:9224\n"
    "buffer_load_dwordx4 v199, s[88:91], s94 offen offset:1024 lds\n"
    "ds_read_b64_tr_b16 v[136:137], v224 offset:16400\n"
    "v_mfma_f32_16x16x32_bf16 v[28:31], a[196:199], a[108:111], v[28:31]\n"
    "ds_read_b64_tr_b16 v[138:139], v224 offset:18448\n"
    "v_mfma_f32_16x16x32_bf16 v[32:35], a[200:203], a[96:99], v[32:35]\n"
    "ds_read_b64_tr_b16 a[232:233], v203 offset:8192\n"
    "ds_read_b64_tr_b16 v[140:141], v224 offset:16408\n"
    "v_mfma_f32_16x16x32_bf16 v[36:39], a[200:203], a[100:103], v[36:39]\n"
    "buffer_load_dwordx4 v200, s[88:91], s94 offen offset:2048 lds\n"
    "ds_read_b64_tr_b16 v[142:143], v224 offset:18456\n"
    "v_mfma_f32_16x16x32_bf16 v[40:43], a[200:203], a[104:107], v[40:43]\n"
    "ds_read_b64_tr_b16 a[234:235], v203 offset:9216\n"
    "v_mfma_f32_16x16x32_bf16 v[44:47], a[200:203], a[108:111], v[44:47]\n"
    "ds_read_b64_tr_b16 a[236:237], v203 offset:8200\n"
    "v_mfma_f32_16x16x32_bf16 v[48:51], a[204:207], a[96:99], v[48:51]\n"
    "buffer_load_dwordx4 v201, s[88:91], s94 offen offset:3072 lds\n"
    "v_mfma_f32_16x16x32_bf16 v[52:55], a[204:207], a[100:103], v[52:55]\n"
    "ds_read_b64_tr_b16 a[238:239], v203 offset:9224\n"
    "v_mfma_f32_16x16x32_bf16 v[56:59], a[204:207], a[104:107], v[56:59]\n"
    "s_add_u32 m0, s93, 0x1000\n"
    "s_waitcnt lgkmcnt(2)\n"
    "global_store_dwordx4 v237, v[128:131], s[64:65] offset:0 sc1\n"
    "v_mfma_f32_16x16x32_bf16 v[60:63], a[204:207], a[108:111], v[60:63]\n"
    "ds_read_b64_tr_b16 a[240:241], v204 offset:8192\n"
    "buffer_load_dwordx4 v198, s[88:91], s95 offen offset:0 lds\n"
    "global_store_dwordx4 v237, v[132:135], s[64:65] offset:1024 sc1\n"
    "v_mfma_f32_16x16x32_bf16 v[64:67], a[208:211], a[96:99], v[64:67]\n"
    "global_store_dwordx4 v237, v[136:139], s[64:65] offset:2048 sc1\n"
    "v_mfma_f32_16x16x32_bf16 v[68:71], a[208:211], a[100:103], v[68:71]\n"
    "ds_read_b64_tr_b16 a[242:243], v204 offset:9216\n"
    "global_store_dwordx4 v237, v[140:143], s[64:65] offset:3072 sc1\n"
    "v_mfma_f32_16x16x32_bf16 v[72:75], a[208:211], a[104:107], v[72:75]\n"
    "buffer_load_dwordx4 v199, s[88:91], s95 offen offset:1024 lds\n"
    "v_mfma_f32_16x16x32_bf16 v[76:79], a[208:211], a[108:111], v[76:79]\n"
    "ds_read_b64_tr_b16 a[244:245], v204 offset:8200\n"
    "v_mfma_f32_16x16x32_bf16 v[80:83], a[212:215], a[96:99], v[80:83]\n"
    "ds_read_b64_tr_b16 a[246:247], v204 offset:9224\n"
    "v_mfma_f32_16x16x32_bf16 v[84:87], a[212:215], a[100:103], v[84:87]\n"
    "buffer_load_dwordx4 v200, s[88:91], s95 offen offset:2048 lds\n"
    "v_mfma_f32_16x16x32_bf16 v[88:91], a[212:215], a[104:107], v[88:91]\n"
    "ds_read_b64_tr_b16 a[248:249], v205 offset:8192\n"
    "v_mfma_f32_16x16x32_bf16 v[92:95], a[212:215], a[108:111], v[92:95]\n"
    "v_mfma_f32_16x16x32_bf16 v[96:99], a[216:219], a[96:99], v[96:99]\n"
    "ds_read_b64_tr_b16 a[250:251], v205 offset:9216\n"
    "buffer_load_dwordx4 v201, s[88:91], s95 offen offset:3072 lds\n"
    "v_mfma_f32_16x16x32_bf16 v[100:103], a[216:219], a[100:103], v[100:103]\n"
    "v_mfma_f32_16x16x32_bf16 v[104:107], a[216:219], a[104:107], v[104:107]\n"
    "s_add_u32 s94, s94, 128\n"
    "s_and_b32 s94, s94, 1023\n"
    "s_add_u32 s95, s94, 32768\n"
    "ds_read_b64_tr_b16 a[252:253], v205 offset:8200\n"
    "v_mfma_f32_16x16x32_bf16 v[108:111], a[216:219], a[108:111], v[108:111]\n"
    "v_mfma_f32_16x16x32_bf16 v[112:115], a[220:223], a[96:99], v[112:115]\n"
    "ds_read_b64_tr_b16 a[254:255], v205 offset:9224\n"
    "v_mfma_f32_16x16x32_bf16 v[116:119], a[220:223], a[100:103], v[116:119]\n"
    "v_mfma_f32_16x16x32_bf16 v[120:123], a[220:223], a[104:107], v[120:123]\n"
    "v_mfma_f32_16x16x32_bf16 v[124:127], a[220:223], a[108:111], v[124:127]\n"
    "v_mfma_f32_16x16x32_bf16 v[0:3], a[224:227], a[112:115], v[0:3]\n"
    "v_mfma_f32_16x16x32_bf16 v[4:7], a[224:227], a[116:119], v[4:7]\n"
    "v_mfma_f32_16x16x32_bf16 v[8:11], a[224:227], a[120:123], v[8:11]\n"
    "v_mfma_f32_16x16x32_bf16 v[12:15], a[224:227], a[124:127], v[12:15]\n"
    "v_mfma_f32_16x16x32_bf16 v[16:19], a[228:231], a[112:115], v[16:19]\n"
    "s_add_u32 m0, s30, 0x10000\n"
    "v_mfma_f32_16x16x32_bf16 v[20:23], a[228:231], a[116:119], v[20:23]\n"
    "s_waitcnt vmcnt(16)\n"
    "s_waitcnt lgkmcnt(0)\n"
    "buffer_load_dwordx4 v196, s[24:27], s29 offen offset:0 nt lds\n"
    "v_mfma_f32_16x16x32_bf16 v[24:27], a[228:231], a[120:123], v[24:27]\n"
    "s_barrier\n"
    "v_add_u32 v202, 0x4000, v202\n"
    "v_add_u32 v203, 0x4000, v203\n"
    "v_add_u32 v204, 0x4000, v204\n"
    "v_add_u32 v205, 0x4000, v205\n"
    "v_mfma_f32_16x16x32_bf16 v[28:31], a[228:231], a[124:127], v[28:31]\n"
    "ds_read_b128 a[64:67], v244 offset:0\n"
    "ds_read_b64_tr_b16 a[192:193], v202 offset:0\n"
    "buffer_load_dwordx4 v197, s[24:27], s29 offen offset:1024 nt lds\n"
    "v_mfma_f32_16x16x32_bf16 v[32:35], a[232:235], a[112:115], v[32:35]\n"
    "ds_read_b64_tr_b16 a[194:195], v202 offset:1024\n"
    "v_mfma_f32_16x16x32_bf16 v[36:39], a[232:235], a[116:119], v[36:39]\n"
    "ds_read_b128 a[68:71], v244 offset:2048\n"
    "buffer_load_dwordx4 v196, s[24:27], s29 offen offset:2048 nt lds\n"
    "v_mfma_f32_16x16x32_bf16 v[40:43], a[232:235], a[120:123], v[40:43]\n"
    "ds_read_b64_tr_b16 a[196:197], v202 offset:8\n"
    "v_mfma_f32_16x16x32_bf16 v[44:47], a[232:235], a[124:127], v[44:47]\n"
    "ds_read_b64_tr_b16 a[198:199], v202 offset:1032\n"
    "buffer_load_dwordx4 v197, s[24:27], s29 offen offset:3072 nt lds\n"
    "v_mfma_f32_16x16x32_bf16 v[48:51], a[236:239], a[112:115], v[48:51]\n"
    "s_add_u32 s29, s29, 16384\n"
    "s_and_b32 s29, s29, 0x7ffff\n"
    "ds_read_b128 a[72:75], v244 offset:4096\n"
    "v_mfma_f32_16x16x32_bf16 v[52:55], a[236:239], a[116:119], v[52:55]\n"
    "ds_read_b64_tr_b16 a[200:201], v203 offset:0\n"
    "v_mfma_f32_16x16x32_bf16 v[56:59], a[236:239], a[120:123], v[56:59]\n"
    "ds_read_b64_tr_b16 a[202:203], v203 offset:1024\n"
    "v_mfma_f32_16x16x32_bf16 v[60:63], a[236:239], a[124:127], v[60:63]\n"
    "ds_read_b128 a[76:79], v244 offset:6144\n"
    "v_mfma_f32_16x16x32_bf16 v[64:67], a[240:243], a[112:115], v[64:67]\n"
    "s_add_u32 m0, s30, 0x4000\n"
    "ds_read_b64_tr_b16 a[204:205], v203 offset:8\n"
    "v_mfma_f32_16x16x32_bf16 v[68:71], a[240:243], a[116:119], v[68:71]\n"
    "ds_read_b64_tr_b16 a[206:207], v203 offset:1032\n"
    "buffer_load_dwordx4 v192, s[20:23], s28 offen offset:0 nt lds\n"
    "v_mfma_f32_16x16x32_bf16 v[72:75], a[240:243], a[120:123], v[72:75]\n"
    "ds_read_b128 a[80:83], v245 offset:0\n"
    "ds_read_b64_tr_b16 a[208:209], v204 offset:0\n"
    "v_mfma_f32_16x16x32_bf16 v[76:79], a[240:243], a[124:127], v[76:79]\n"
    "ds_read_b64_tr_b16 a[210:211], v204 offset:1024\n"
    "buffer_load_dwordx4 v193, s[20:23], s28 offen offset:1024 nt lds\n"
    "v_mfma_f32_16x16x32_bf16 v[80:83], a[244:247], a[112:115], v[80:83]\n"
    "ds_read_b128 a[84:87], v245 offset:2048\n"
    "v_mfma_f32_16x16x32_bf16 v[84:87], a[244:247], a[116:119], v[84:87]\n"
    "ds_read_b64_tr_b16 a[212:213], v204 offset:8\n"
    "buffer_load_dwordx4 v194, s[20:23], s28 offen offset:2048 nt lds\n"
    "v_mfma_f32_16x16x32_bf16 v[88:91], a[244:247], a[120:123], v[88:91]\n"
    "ds_read_b64_tr_b16 a[214:215], v204 offset:1032\n"
    "v_mfma_f32_16x16x32_bf16 v[92:95], a[244:247], a[124:127], v[92:95]\n"
    "ds_read_b128 a[88:91], v245 offset:4096\n"
    "buffer_load_dwordx4 v195, s[20:23], s28 offen offset:3072 nt lds\n"
    "v_mfma_f32_16x16x32_bf16 v[96:99], a[248:251], a[112:115], v[96:99]\n"
    "s_add_u32 s28, s28, 16384\n"
    "s_and_b32 s28, s28, 0x7ffff\n"
    "ds_read_b64_tr_b16 a[216:217], v205 offset:0\n"
    "v_mfma_f32_16x16x32_bf16 v[100:103], a[248:251], a[116:119], v[100:103]\n"
    "ds_read_b64_tr_b16 a[218:219], v205 offset:1024\n"
    "v_mfma_f32_16x16x32_bf16 v[104:107], a[248:251], a[120:123], v[104:107]\n"
    "ds_read_b128 a[92:95], v245 offset:6144\n"
    "v_mfma_f32_16x16x32_bf16 v[108:111], a[248:251], a[124:127], v[108:111]\n"
    "ds_read_b64_tr_b16 a[220:221], v205 offset:8\n"
    "v_mfma_f32_16x16x32_bf16 v[112:115], a[252:255], a[112:115], v[112:115]\n"
    "ds_read_b64_tr_b16 a[222:223], v205 offset:1032\n"
    "v_mfma_f32_16x16x32_bf16 v[116:119], a[252:255], a[116:119], v[116:119]\n"
    "v_mfma_f32_16x16x32_bf16 v[120:123], a[252:255], a[120:123], v[120:123]\n"
    "v_mfma_f32_16x16x32_bf16 v[124:127], a[252:255], a[124:127], v[124:127]\n"
    "s_waitcnt lgkmcnt(2)\n"
    "v_mfma_f32_16x16x32_bf16 v[0:3], a[192:195], a[64:67], v[0:3]\n"
    "v_mfma_f32_16x16x32_bf16 v[4:7], a[192:195], a[68:71], v[4:7]\n"
    "ds_read_b64_tr_b16 a[224:225], v202 offset:8192\n"
    "v_mfma_f32_16x16x32_bf16 v[8:11], a[192:195], a[72:75], v[8:11]\n"
    "ds_read_b64_tr_b16 a[226:227], v202 offset:9216\n"
    "v_mfma_f32_16x16x32_bf16 v[12:15], a[192:195], a[76:79], v[12:15]\n"
    "v_mfma_f32_16x16x32_bf16 v[16:19], a[196:199], a[64:67], v[16:19]\n"
    "ds_read_b64_tr_b16 a[228:229], v202 offset:8200\n"
    "v_mfma_f32_16x16x32_bf16 v[20:23], a[196:199], a[68:71], v[20:23]\n"
    "v_mfma_f32_16x16x32_bf16 v[24:27], a[196:199], a[72:75], v[24:27]\n"
    "ds_read_b64_tr_b16 a[230:231], v202 offset:9224\n"
    "v_mfma_f32_16x16x32_bf16 v[28:31], a[196:199], a[76:79], v[28:31]\n"
    "v_mfma_f32_16x16x32_bf16 v[32:35], a[200:203], a[64:67], v[32:35]\n"
    "ds_read_b64_tr_b16 a[232:233], v203 offset:8192\n"
    "v_mfma_f32_16x16x32_bf16 v[36:39], a[200:203], a[68:71], v[36:39]\n"
    "v_mfma_f32_16x16x32_bf16 v[40:43], a[200:203], a[72:75], v[40:43]\n"
    "ds_read_b64_tr_b16 a[234:235], v203 offset:9216\n"
    "v_mfma_f32_16x16x32_bf16 v[44:47], a[200:203], a[76:79], v[44:47]\n"
    "ds_read_b64_tr_b16 a[236:237], v203 offset:8200\n"
    "v_mfma_f32_16x16x32_bf16 v[48:51], a[204:207], a[64:67], v[48:51]\n"
    "v_mfma_f32_16x16x32_bf16 v[52:55], a[204:207], a[68:71], v[52:55]\n"
    "ds_read_b64_tr_b16 a[238:239], v203 offset:9224\n"
    "v_mfma_f32_16x16x32_bf16 v[56:59], a[204:207], a[72:75], v[56:59]\n"
    "v_mfma_f32_16x16x32_bf16 v[60:63], a[204:207], a[76:79], v[60:63]\n"
    "ds_read_b64_tr_b16 a[240:241], v204 offset:8192\n"
    "v_mfma_f32_16x16x32_bf16 v[64:67], a[208:211], a[64:67], v[64:67]\n"
    "v_mfma_f32_16x16x32_bf16 v[68:71], a[208:211], a[68:71], v[68:71]\n"
    "ds_read_b64_tr_b16 a[242:243], v204 offset:9216\n"
    "v_mfma_f32_16x16x32_bf16 v[72:75], a[208:211], a[72:75], v[72:75]\n"
    "v_mfma_f32_16x16x32_bf16 v[76:79], a[208:211], a[76:79], v[76:79]\n"
    "ds_read_b64_tr_b16 a[244:245], v204 offset:8200\n"
    "v_mfma_f32_16x16x32_bf16 v[80:83], a[212:215], a[64:67], v[80:83]\n"
    "ds_read_b64_tr_b16 a[246:247], v204 offset:9224\n"
    "v_mfma_f32_16x16x32_bf16 v[84:87], a[212:215], a[68:71], v[84:87]\n"
    "v_mfma_f32_16x16x32_bf16 v[88:91], a[212:215], a[72:75], v[88:91]\n"
    "ds_read_b64_tr_b16 a[248:249], v205 offset:8192\n"
    "v_mfma_f32_16x16x32_bf16 v[92:95], a[212:215], a[76:79], v[92:95]\n"
    "v_mfma_f32_16x16x32_bf16 v[96:99], a[216:219], a[64:67], v[96:99]\n"
    "ds_read_b64_tr_b16 a[250:251], v205 offset:9216\n"
    "v_mfma_f32_16x16x32_bf16 v[100:103], a[216:219], a[68:71], v[100:103]\n"
    "v_mfma_f32_16x16x32_bf16 v[104:107], a[216:219], a[72:75], v[104:107]\n"
    "ds_read_b64_tr_b16 a[252:253], v205 offset:8200\n"
    "v_mfma_f32_16x16x32_bf16 v[108:111], a[216:219], a[76:79], v[108:111]\n"
    "s_waitcnt lgkmcnt(2)\n"
    "v_mfma_f32_16x16x32_bf16 v[112:115], a[220:223], a[64:67], v[112:115]\n"
    "ds_read_b64_tr_b16 a[254:255], v205 offset:9224\n"
    "v_mfma_f32_16x16x32_bf16 v[116:119], a[220:223], a[68:71], v[116:119]\n"
    "v_mfma_f32_16x16x32_bf16 v[120:123], a[220:223], a[72:75], v[120:123]\n"
    "v_mfma_f32_16x16x32_bf16 v[124:127], a[220:223], a[76:79], v[124:127]\n"
    "v_mfma_f32_16x16x32_bf16 v[0:3], a[224:227], a[80:83], v[0:3]\n"
    "v_mfma_f32_16x16x32_bf16 v[4:7], a[224:227], a[84:87], v[4:7]\n"
    "v_mfma_f32_16x16x32_bf16 v[8:11], a[224:227], a[88:91], v[8:11]\n"
    "v_mfma_f32_16x16x32_bf16 v[12:15], a[224:227], a[92:95], v[12:15]\n"
    "v_mfma_f32_16x16x32_bf16 v[16:19], a[228:231], a[80:83], v[16:19]\n"
    "v_mfma_f32_16x16x32_bf16 v[20:23], a[228:231], a[84:87], v[20:23]\n"
    "s_waitcnt vmcnt(8)\n"
    "s_waitcnt lgkmcnt(0)\n"
    "v_mfma_f32_16x16x32_bf16 v[24:27], a[228:231], a[88:91], v[24:27]\n"
    "s_barrier\n"
    "v_add_u32 v202, 0x4000, v202\n"
    "v_add_u32 v203, 0x4000, v203\n"
    "v_add_u32 v204, 0x4000, v204\n"
    "v_add_u32 v205, 0x4000, v205\n"
    "v_mfma_f32_16x16x32_bf16 v[28:31], a[228:231], a[92:95], v[28:31]\n"
    "ds_read_b128 a[96:99], v246 offset:0\n"
    "ds_read_b64_tr_b16 a[192:193], v202 offset:0\n"
    "v_mfma_f32_16x16x32_bf16 v[32:35], a[232:235], a[80:83], v[32:35]\n"
    "s_add_u32 m0, s30, 0x8000\n"
    "ds_read_b64_tr_b16 a[194:195], v202 offset:1024\n"
    "v_mfma_f32_16x16x32_bf16 v[36:39], a[232:235], a[84:87], v[36:39]\n"
    "ds_read_b128 a[100:103], v246 offset:2048\n"
    "buffer_load_dwordx4 v192, s[20:23], s28 offen offset:0 nt lds\n"
    "v_mfma_f32_16x16x32_bf16 v[40:43], a[232:235], a[88:91], v[40:43]\n"
    "ds_read_b64_tr_b16 a[196:197], v202 offset:8\n"
    "v_mfma_f32_16x16x32_bf16 v[44:47], a[232:235], a[92:95], v[44:47]\n"
    "ds_read_b64_tr_b16 a[198:199], v202 offset:1032\n"
    "buffer_load_dwordx4 v193, s[20:23], s28 offen offset:1024 nt lds\n"
    "v_mfma_f32_16x16x32_bf16 v[48:51], a[236:239], a[80:83], v[48:51]\n"
    "ds_read_b128 a[104:107], v246 offset:4096\n"
    "v_mfma_f32_16x16x32_bf16 v[52:55], a[236:239], a[84:87], v[52:55]\n"
    "ds_read_b64_tr_b16 a[200:201], v203 offset:0\n"
    "buffer_load_dwordx4 v194, s[20:23], s28 offen offset:2048 nt lds\n"
    "v_mfma_f32_16x16x32_bf16 v[56:59], a[236:239], a[88:91], v[56:59]\n"
    "ds_read_b64_tr_b16 a[202:203], v203 offset:1024\n"
    "v_mfma_f32_16x16x32_bf16 v[60:63], a[236:239], a[92:95], v[60:63]\n"
    "ds_read_b128 a[108:111], v246 offset:6144\n"
    "buffer_load_dwordx4 v195, s[20:23], s28 offen offset:3072 nt lds\n"
    "v_mfma_f32_16x16x32_bf16 v[64:67], a[240:243], a[80:83], v[64:67]\n"
    "s_add_u32 s28, s28, 16384\n"
    "s_and_b32 s28, s28, 0x7ffff\n"
    "ds_read_b64_tr_b16 a[204:205], v203 offset:8\n"
    "v_mfma_f32_16x16x32_bf16 v[68:71], a[240:243], a[84:87], v[68:71]\n"
    "ds_read_b64_tr_b16 a[206:207], v203 offset:1032\n"
    "v_mfma_f32_16x16x32_bf16 v[72:75], a[240:243], a[88:91], v[72:75]\n"
    "ds_read_b128 a[112:115], v247 offset:0\n"
    "ds_read_b64_tr_b16 a[208:209], v204 offset:0\n"
    "v_mfma_f32_16x16x32_bf16 v[76:79], a[240:243], a[92:95], v[76:79]\n"
    "ds_read_b64_tr_b16 a[210:211], v204 offset:1024\n"
    "v_mfma_f32_16x16x32_bf16 v[80:83], a[244:247], a[80:83], v[80:83]\n"
    "s_add_u32 m0, s30, 0x14000\n"
    "ds_read_b128 a[116:119], v247 offset:2048\n"
    "v_mfma_f32_16x16x32_bf16 v[84:87], a[244:247], a[84:87], v[84:87]\n"
    "ds_read_b64_tr_b16 a[212:213], v204 offset:8\n"
    "buffer_load_dwordx4 v196, s[24:27], s29 offen offset:0 nt lds\n"
    "v_mfma_f32_16x16x32_bf16 v[88:91], a[244:247], a[88:91], v[88:91]\n"
    "ds_read_b64_tr_b16 a[214:215], v204 offset:1032\n"
    "v_mfma_f32_16x16x32_bf16 v[92:95], a[244:247], a[92:95], v[92:95]\n"
    "ds_read_b128 a[120:123], v247 offset:4096\n"
    "buffer_load_dwordx4 v197, s[24:27], s29 offen offset:1024 nt lds\n"
    "v_mfma_f32_16x16x32_bf16 v[96:99], a[248:251], a[80:83], v[96:99]\n"
    "ds_read_b64_tr_b16 a[216:217], v205 offset:0\n"
    "v_mfma_f32_16x16x32_bf16 v[100:103], a[248:251], a[84:87], v[100:103]\n"
    "ds_read_b64_tr_b16 a[218:219], v205 offset:1024\n"
    "buffer_load_dwordx4 v196, s[24:27], s29 offen offset:2048 nt lds\n"
    "v_mfma_f32_16x16x32_bf16 v[104:107], a[248:251], a[88:91], v[104:107]\n"
    "ds_read_b128 a[124:127], v247 offset:6144\n"
    "v_mfma_f32_16x16x32_bf16 v[108:111], a[248:251], a[92:95], v[108:111]\n"
    "ds_read_b64_tr_b16 a[220:221], v205 offset:8\n"
    "buffer_load_dwordx4 v197, s[24:27], s29 offen offset:3072 nt lds\n"
    "v_mfma_f32_16x16x32_bf16 v[112:115], a[252:255], a[80:83], v[112:115]\n"
    "s_add_u32 s29, s29, 16384\n"
    "s_and_b32 s29, s29, 0x7ffff\n"
    "ds_read_b64_tr_b16 a[222:223], v205 offset:1032\n"
    "v_mfma_f32_16x16x32_bf16 v[116:119], a[252:255], a[84:87], v[116:119]\n"
    "v_mfma_f32_16x16x32_bf16 v[120:123], a[252:255], a[88:91], v[120:123]\n"
    "v_mfma_f32_16x16x32_bf16 v[124:127], a[252:255], a[92:95], v[124:127]\n"
    "s_waitcnt lgkmcnt(1)\n"
    "v_mfma_f32_16x16x32_bf16 v[0:3], a[192:195], a[96:99], v[0:3]\n"
    "v_mfma_f32_16x16x32_bf16 v[4:7], a[192:195], a[100:103], v[4:7]\n"
    "ds_read_b64_tr_b16 a[224:225], v202 offset:8192\n"
    "v_mfma_f32_16x16x32_bf16 v[8:11], a[192:195], a[104:107], v[8:11]\n"
    "ds_read_b64_tr_b16 a[226:227], v202 offset:9216\n"
    "ds_read_b128 a[64:67], v180 offset:0\n"
    "v_mfma_f32_16x16x32_bf16 v[12:15], a[192:195], a[108:111], v[12:15]\n"
    "ds_read_b64_tr_b16 a[228:229], v202 offset:8200\n"
    "v_mfma_f32_16x16x32_bf16 v[16:19], a[196:199], a[96:99], v[16:19]\n"
    "v_mfma_f32_16x16x32_bf16 v[20:23], a[196:199], a[100:103], v[20:23]\n"
    "ds_read_b64_tr_b16 a[230:231], v202 offset:9224\n"
    "ds_read_b128 a[68:71], v181 offset:0\n"
    "v_mfma_f32_16x16x32_bf16 v[24:27], a[196:199], a[104:107], v[24:27]\n"
    "ds_read_b64_tr_b16 a[232:233], v203 offset:8192\n"
    "v_mfma_f32_16x16x32_bf16 v[28:31], a[196:199], a[108:111], v[28:31]\n"
    "ds_read_b64_tr_b16 a[234:235], v203 offset:9216\n"
    "v_mfma_f32_16x16x32_bf16 v[32:35], a[200:203], a[96:99], v[32:35]\n"
    "ds_read_b128 a[72:75], v182 offset:0\n"
    "v_mfma_f32_16x16x32_bf16 v[36:39], a[200:203], a[100:103], v[36:39]\n"
    "ds_read_b64_tr_b16 a[236:237], v203 offset:8200\n"
    "v_mfma_f32_16x16x32_bf16 v[40:43], a[200:203], a[104:107], v[40:43]\n"
    "ds_read_b64_tr_b16 a[238:239], v203 offset:9224\n"
    "v_mfma_f32_16x16x32_bf16 v[44:47], a[200:203], a[108:111], v[44:47]\n"
    "ds_read_b128 a[76:79], v183 offset:0\n"
    "v_mfma_f32_16x16x32_bf16 v[48:51], a[204:207], a[96:99], v[48:51]\n"
    "ds_read_b64_tr_b16 a[240:241], v204 offset:8192\n"
    "v_mfma_f32_16x16x32_bf16 v[52:55], a[204:207], a[100:103], v[52:55]\n"
    "ds_read_b64_tr_b16 a[242:243], v204 offset:9216\n"
    "v_mfma_f32_16x16x32_bf16 v[56:59], a[204:207], a[104:107], v[56:59]\n"
    "ds_read_b64_tr_b16 a[244:245], v204 offset:8200\n"
    "ds_read_b128 a[80:83], v180 offset:4096\n"
    "v_mfma_f32_16x16x32_bf16 v[60:63], a[204:207], a[108:111], v[60:63]\n"
    "v_mfma_f32_16x16x32_bf16 v[64:67], a[208:211], a[96:99], v[64:67]\n"
    "ds_read_b64_tr_b16 a[246:247], v204 offset:9224\n"
    "v_mfma_f32_16x16x32_bf16 v[68:71], a[208:211], a[100:103], v[68:71]\n"
    "ds_read_b64_tr_b16 a[248:249], v205 offset:8192\n"
    "ds_read_b128 a[84:87], v181 offset:4096\n"
    "v_mfma_f32_16x16x32_bf16 v[72:75], a[208:211], a[104:107], v[72:75]\n"
    "ds_read_b64_tr_b16 a[250:251], v205 offset:9216\n"
    "v_mfma_f32_16x16x32_bf16 v[76:79], a[208:211], a[108:111], v[76:79]\n"
    "v_mfma_f32_16x16x32_bf16 v[80:83], a[212:215], a[96:99], v[80:83]\n"
    "ds_read_b64_tr_b16 a[252:253], v205 offset:8200\n"
    "ds_read_b128 a[88:91], v182 offset:4096\n"
    "v_mfma_f32_16x16x32_bf16 v[84:87], a[212:215], a[100:103], v[84:87]\n"
    "ds_read_b64_tr_b16 a[254:255], v205 offset:9224\n"
    "v_mfma_f32_16x16x32_bf16 v[88:91], a[212:215], a[104:107], v[88:91]\n"
    "v_mfma_f32_16x16x32_bf16 v[92:95], a[212:215], a[108:111], v[92:95]\n"
    "ds_read_b128 a[92:95], v183 offset:4096\n"
    "v_mfma_f32_16x16x32_bf16 v[96:99], a[216:219], a[96:99], v[96:99]\n"
    "v_mfma_f32_16x16x32_bf16 v[100:103], a[216:219], a[100:103], v[100:103]\n"
    "v_mfma_f32_16x16x32_bf16 v[104:107], a[216:219], a[104:107], v[104:107]\n"
    "v_mfma_f32_16x16x32_bf16 v[108:111], a[216:219], a[108:111], v[108:111]\n"
    "s_waitcnt lgkmcnt(1)\n"
    "v_mfma_f32_16x16x32_bf16 v[112:115], a[220:223], a[96:99], v[112:115]\n"
    "v_mfma_f32_16x16x32_bf16 v[116:119], a[220:223], a[100:103], v[116:119]\n"
    "v_mfma_f32_16x16x32_bf16 v[120:123], a[220:223], a[104:107], v[120:123]\n"
    "v_mfma_f32_16x16x32_bf16 v[124:127], a[220:223], a[108:111], v[124:127]\n"
    "v_mfma_f32_16x16x32_bf16 v[0:3], a[224:227], a[112:115], v[0:3]\n"
    "v_mfma_f32_16x16x32_bf16 v[16:19], a[228:231], a[112:115], v[16:19]\n"
    "v_mfma_f32_16x16x32_bf16 v[32:35], a[232:235], a[112:115], v[32:35]\n"
    "v_mfma_f32_16x16x32_bf16 v[48:51], a[236:239], a[112:115], v[48:51]\n"
    "v_mfma_f32_16x16x32_bf16 v[64:67], a[240:243], a[112:115], v[64:67]\n"
    "v_mfma_f32_16x16x32_bf16 v[80:83], a[244:247], a[112:115], v[80:83]\n"
    "v_mfma_f32_16x16x32_bf16 v[96:99], a[248:251], a[112:115], v[96:99]\n"
    "v_mfma_f32_16x16x32_bf16 v[112:115], a[252:255], a[112:115], v[112:115]\n"
    "v_mfma_f32_16x16x32_bf16 v[4:7], a[224:227], a[116:119], v[4:7]\n"
    "v_mfma_f32_16x16x32_bf16 v[20:23], a[228:231], a[116:119], v[20:23]\n"
    "v_mfma_f32_16x16x32_bf16 v[36:39], a[232:235], a[116:119], v[36:39]\n"
    "v_pk_mul_f32 v[0:1], v[0:1], s[66:67]\n"
    "v_pk_mul_f32 v[2:3], v[2:3], s[66:67]\n"
    "v_mfma_f32_16x16x32_bf16 v[52:55], a[236:239], a[116:119], v[52:55]\n"
    "v_pk_mul_f32 v[16:17], v[16:17], s[66:67]\n"
    "v_pk_mul_f32 v[18:19], v[18:19], s[66:67]\n"
    "v_mfma_f32_16x16x32_bf16 v[68:71], a[240:243], a[116:119], v[68:71]\n"
    "v_cvt_pk_bf16_f32 v212, v0, v1\n"
    "v_cvt_pk_bf16_f32 v213, v2, v3\n"
    "v_mfma_f32_16x16x32_bf16 v[84:87], a[244:247], a[116:119], v[84:87]\n"
    "v_cvt_pk_bf16_f32 v214, v16, v17\n"
    "v_cvt_pk_bf16_f32 v215, v18, v19\n"
    "v_mfma_f32_16x16x32_bf16 v[100:103], a[248:251], a[116:119], v[100:103]\n"
    "v_accvgpr_write_b32 a0, v212\n"
    "v_accvgpr_write_b32 a1, v213\n"
    "v_mfma_f32_16x16x32_bf16 v[116:119], a[252:255], a[116:119], v[116:119]\n"
    "v_accvgpr_write_b32 a2, v214\n"
    "v_accvgpr_write_b32 a3, v215\n"
    "v_mfma_f32_16x16x32_bf16 v[8:11], a[224:227], a[120:123], v[8:11]\n"
    "v_pk_mul_f32 v[32:33], v[32:33], s[66:67]\n"
    "v_pk_mul_f32 v[34:35], v[34:35], s[66:67]\n"
    "v_mfma_f32_16x16x32_bf16 v[24:27], a[228:231], a[120:123], v[24:27]\n"
    "v_pk_mul_f32 v[48:49], v[48:49], s[66:67]\n"
    "v_pk_mul_f32 v[50:51], v[50:51], s[66:67]\n"
    "v_mfma_f32_16x16x32_bf16 v[40:43], a[232:235], a[120:123], v[40:43]\n"
    "v_cvt_pk_bf16_f32 v216, v32, v33\n"
    "v_cvt_pk_bf16_f32 v217, v34, v35\n"
    "v_mfma_f32_16x16x32_bf16 v[56:59], a[236:239], a[120:123], v[56:59]\n"
    "v_cvt_pk_bf16_f32 v218, v48, v49\n"
    "v_cvt_pk_bf16_f32 v219, v50, v51\n"
    "v_mfma_f32_16x16x32_bf16 v[72:75], a[240:243], a[120:123], v[72:75]\n"
    "v_accvgpr_write_b32 a4, v216\n"
    "v_accvgpr_write_b32 a5, v217\n"
    "v_mfma_f32_16x16x32_bf16 v[88:91], a[244:247], a[120:123], v[88:91]\n"
    "v_accvgpr_write_b32 a6, v218\n"
    "v_accvgpr_write_b32 a7, v219\n"
    "v_mfma_f32_16x16x32_bf16 v[104:107], a[248:251], a[120:123], v[104:107]\n"
    "v_pk_mul_f32 v[64:65], v[64:65], s[66:67]\n"
    "v_pk_mul_f32 v[66:67], v[66:67], s[66:67]\n"
    "v_mfma_f32_16x16x32_bf16 v[120:123], a[252:255], a[120:123], v[120:123]\n"
    "v_pk_mul_f32 v[80:81], v[80:81], s[66:67]\n"
    "v_pk_mul_f32 v[82:83], v[82:83], s[66:67]\n"
    "v_mfma_f32_16x16x32_bf16 v[12:15], a[224:227], a[124:127], v[12:15]\n"
    "v_cvt_pk_bf16_f32 v212, v64, v65\n"
    "v_cvt_pk_bf16_f32 v213, v66, v67\n"
    "v_mfma_f32_16x16x32_bf16 v[28:31], a[228:231], a[124:127], v[28:31]\n"
    "v_cvt_pk_bf16_f32 v214, v80, v81\n"
    "v_cvt_pk_bf16_f32 v215, v82, v83\n"
    "v_mfma_f32_16x16x32_bf16 v[44:47], a[232:235], a[124:127], v[44:47]\n"
    "v_accvgpr_write_b32 a8, v212\n"
    "v_accvgpr_write_b32 a9, v213\n"
    "v_mfma_f32_16x16x32_bf16 v[60:63], a[236:239], a[124:127], v[60:63]\n"
    "v_accvgpr_write_b32 a10, v214\n"
    "v_accvgpr_write_b32 a11, v215\n"
    "v_mfma_f32_16x16x32_bf16 v[76:79], a[240:243], a[124:127], v[76:79]\n"
    "v_pk_mul_f32 v[96:97], v[96:97], s[66:67]\n"
    "v_pk_mul_f32 v[98:99], v[98:99], s[66:67]\n"
    "v_mfma_f32_16x16x32_bf16 v[92:95], a[244:247], a[124:127], v[92:95]\n"
    "v_pk_mul_f32 v[112:113], v[112:113], s[66:67]\n"
    "v_pk_mul_f32 v[114:115], v[114:115], s[66:67]\n"
    "v_mfma_f32_16x16x32_bf16 v[108:111], a[248:251], a[124:127], v[108:111]\n"
    "v_cvt_pk_bf16_f32 v216, v96, v97\n"
    "v_cvt_pk_bf16_f32 v217, v98, v99\n"
    "v_mfma_f32_16x16x32_bf16 v[124:127], a[252:255], a[124:127], v[124:127]\n"
    "v_cvt_pk_bf16_f32 v218, v112, v113\n"
    "v_cvt_pk_bf16_f32 v219, v114, v115\n"
    "v_accvgpr_write_b32 a12, v216\n"
    "v_accvgpr_write_b32 a13, v217\n"
    "v_accvgpr_write_b32 a14, v218\n"
    "v_accvgpr_write_b32 a15, v219\n"
    "v_pk_mul_f32 v[4:5], v[4:5], s[66:67]\n"
    "v_pk_mul_f32 v[6:7], v[6:7], s[66:67]\n"
    "v_pk_mul_f32 v[20:21], v[20:21], s[66:67]\n"
    "v_pk_mul_f32 v[22:23], v[22:23], s[66:67]\n"
    "v_cvt_pk_bf16_f32 v212, v4, v5\n"
    "v_cvt_pk_bf16_f32 v213, v6, v7\n"
    "v_cvt_pk_bf16_f32 v214, v20, v21\n"
    "v_cvt_pk_bf16_f32 v215, v22, v23\n"
    "v_accvgpr_write_b32 a16, v212\n"
    "v_accvgpr_write_b32 a17, v213\n"
    "v_accvgpr_write_b32 a18, v214\n"
    "v_accvgpr_write_b32 a19, v215\n"
    "v_pk_mul_f32 v[36:37], v[36:37], s[66:67]\n"
    "v_pk_mul_f32 v[38:39], v[38:39], s[66:67]\n"
    "v_pk_mul_f32 v[52:53], v[52:53], s[66:67]\n"
    "v_pk_mul_f32 v[54:55], v[54:55], s[66:67]\n"
    "v_cvt_pk_bf16_f32 v216, v36, v37\n"
    "v_cvt_pk_bf16_f32 v217, v38, v39\n"
    "v_cvt_pk_bf16_f32 v218, v52, v53\n"
    "v_cvt_pk_bf16_f32 v219, v54, v55\n"
    "v_accvgpr_write_b32 a20, v216\n"
    "v_accvgpr_write_b32 a21, v217\n"
    "v_accvgpr_write_b32 a22, v218\n"
    "v_accvgpr_write_b32 a23, v219\n"
    "v_pk_mul_f32 v[68:69], v[68:69], s[66:67]\n"
    "v_pk_mul_f32 v[70:71], v[70:71], s[66:67]\n"
    "v_pk_mul_f32 v[84:85], v[84:85], s[66:67]\n"
    "v_pk_mul_f32 v[86:87], v[86:87], s[66:67]\n"
    "v_cvt_pk_bf16_f32 v212, v68, v69\n"
    "v_cvt_pk_bf16_f32 v213, v70, v71\n"
    "v_cvt_pk_bf16_f32 v214, v84, v85\n"
    "v_cvt_pk_bf16_f32 v215, v86, v87\n"
    "v_accvgpr_write_b32 a24, v212\n"
    "v_accvgpr_write_b32 a25, v213\n"
    "v_accvgpr_write_b32 a26, v214\n"
    "v_accvgpr_write_b32 a27, v215\n"
    "v_pk_mul_f32 v[100:101], v[100:101], s[66:67]\n"
    "v_pk_mul_f32 v[102:103], v[102:103], s[66:67]\n"
    "v_pk_mul_f32 v[116:117], v[116:117], s[66:67]\n"
    "v_pk_mul_f32 v[118:119], v[118:119], s[66:67]\n"
    "v_cvt_pk_bf16_f32 v216, v100, v101\n"
    "v_cvt_pk_bf16_f32 v217, v102, v103\n"
    "v_cvt_pk_bf16_f32 v218, v116, v117\n"
    "v_cvt_pk_bf16_f32 v219, v118, v119\n"
    "v_accvgpr_write_b32 a28, v216\n"
    "v_accvgpr_write_b32 a29, v217\n"
    "v_accvgpr_write_b32 a30, v218\n"
    "v_accvgpr_write_b32 a31, v219\n"
    "v_pk_mul_f32 v[8:9], v[8:9], s[66:67]\n"
    "v_pk_mul_f32 v[10:11], v[10:11], s[66:67]\n"
    "v_pk_mul_f32 v[24:25], v[24:25], s[66:67]\n"
    "v_pk_mul_f32 v[26:27], v[26:27], s[66:67]\n"
    "v_cvt_pk_bf16_f32 v212, v8, v9\n"
    "v_cvt_pk_bf16_f32 v213, v10, v11\n"
    "v_cvt_pk_bf16_f32 v214, v24, v25\n"
    "v_cvt_pk_bf16_f32 v215, v26, v27\n"
    "v_accvgpr_write_b32 a32, v212\n"
    "v_accvgpr_write_b32 a33, v213\n"
    "v_accvgpr_write_b32 a34, v214\n"
    "v_accvgpr_write_b32 a35, v215\n"
    "v_pk_mul_f32 v[40:41], v[40:41], s[66:67]\n"
    "v_pk_mul_f32 v[42:43], v[42:43], s[66:67]\n"
    "v_pk_mul_f32 v[56:57], v[56:57], s[66:67]\n"
    "v_pk_mul_f32 v[58:59], v[58:59], s[66:67]\n"
    "v_cvt_pk_bf16_f32 v216, v40, v41\n"
    "v_cvt_pk_bf16_f32 v217, v42, v43\n"
    "v_cvt_pk_bf16_f32 v218, v56, v57\n"
    "v_cvt_pk_bf16_f32 v219, v58, v59\n"
    "v_accvgpr_write_b32 a36, v216\n"
    "v_accvgpr_write_b32 a37, v217\n"
    "v_accvgpr_write_b32 a38, v218\n"
    "v_accvgpr_write_b32 a39, v219\n"
    "v_pk_mul_f32 v[72:73], v[72:73], s[66:67]\n"
    "v_pk_mul_f32 v[74:75], v[74:75], s[66:67]\n"
    "v_pk_mul_f32 v[88:89], v[88:89], s[66:67]\n"
    "v_pk_mul_f32 v[90:91], v[90:91], s[66:67]\n"
    "v_cvt_pk_bf16_f32 v212, v72, v73\n"
    "v_cvt_pk_bf16_f32 v213, v74, v75\n"
    "v_cvt_pk_bf16_f32 v214, v88, v89\n"
    "v_cvt_pk_bf16_f32 v215, v90, v91\n"
    "v_accvgpr_write_b32 a40, v212\n"
    "v_accvgpr_write_b32 a41, v213\n"
    "v_accvgpr_write_b32 a42, v214\n"
    "v_accvgpr_write_b32 a43, v215\n"
    "v_pk_mul_f32 v[104:105], v[104:105], s[66:67]\n"
    "v_pk_mul_f32 v[106:107], v[106:107], s[66:67]\n"
    "v_pk_mul_f32 v[120:121], v[120:121], s[66:67]\n"
    "v_pk_mul_f32 v[122:123], v[122:123], s[66:67]\n"
    "v_cvt_pk_bf16_f32 v216, v104, v105\n"
    "v_cvt_pk_bf16_f32 v217, v106, v107\n"
    "v_cvt_pk_bf16_f32 v218, v120, v121\n"
    "v_cvt_pk_bf16_f32 v219, v122, v123\n"
    "v_accvgpr_write_b32 a44, v216\n"
    "v_accvgpr_write_b32 a45, v217\n"
    "v_accvgpr_write_b32 a46, v218\n"
    "v_accvgpr_write_b32 a47, v219\n"
    "v_pk_mul_f32 v[12:13], v[12:13], s[66:67]\n"
    "v_pk_mul_f32 v[14:15], v[14:15], s[66:67]\n"
    "v_pk_mul_f32 v[28:29], v[28:29], s[66:67]\n"
    "v_pk_mul_f32 v[30:31], v[30:31], s[66:67]\n"
    "v_cvt_pk_bf16_f32 v212, v12, v13\n"
    "v_cvt_pk_bf16_f32 v213, v14, v15\n"
    "v_cvt_pk_bf16_f32 v214, v28, v29\n"
    "v_cvt_pk_bf16_f32 v215, v30, v31\n"
    "v_accvgpr_write_b32 a48, v212\n"
    "v_accvgpr_write_b32 a49, v213\n"
    "v_accvgpr_write_b32 a50, v214\n"
    "v_accvgpr_write_b32 a51, v215\n"
    "v_pk_mul_f32 v[44:45], v[44:45], s[66:67]\n"
    "v_pk_mul_f32 v[46:47], v[46:47], s[66:67]\n"
    "v_pk_mul_f32 v[60:61], v[60:61], s[66:67]\n"
    "v_pk_mul_f32 v[62:63], v[62:63], s[66:67]\n"
    "v_cvt_pk_bf16_f32 v216, v44, v45\n"
    "v_cvt_pk_bf16_f32 v217, v46, v47\n"
    "v_cvt_pk_bf16_f32 v218, v60, v61\n"
    "v_cvt_pk_bf16_f32 v219, v62, v63\n"
    "v_accvgpr_write_b32 a52, v216\n"
    "v_accvgpr_write_b32 a53, v217\n"
    "v_accvgpr_write_b32 a54, v218\n"
    "v_accvgpr_write_b32 a55, v219\n"
    "v_pk_mul_f32 v[76:77], v[76:77], s[66:67]\n"
    "v_pk_mul_f32 v[78:79], v[78:79], s[66:67]\n"
    "v_pk_mul_f32 v[92:93], v[92:93], s[66:67]\n"
    "v_pk_mul_f32 v[94:95], v[94:95], s[66:67]\n"
    "v_cvt_pk_bf16_f32 v212, v76, v77\n"
    "v_cvt_pk_bf16_f32 v213, v78, v79\n"
    "v_cvt_pk_bf16_f32 v214, v92, v93\n"
    "v_cvt_pk_bf16_f32 v215, v94, v95\n"
    "v_accvgpr_write_b32 a56, v212\n"
    "v_accvgpr_write_b32 a57, v213\n"
    "v_accvgpr_write_b32 a58, v214\n"
    "v_accvgpr_write_b32 a59, v215\n"
    "v_pk_mul_f32 v[108:109], v[108:109], s[66:67]\n"
    "v_pk_mul_f32 v[110:111], v[110:111], s[66:67]\n"
    "v_pk_mul_f32 v[124:125], v[124:125], s[66:67]\n"
    "v_pk_mul_f32 v[126:127], v[126:127], s[66:67]\n"
    "v_cvt_pk_bf16_f32 v216, v108, v109\n"
    "v_cvt_pk_bf16_f32 v217, v110, v111\n"
    "v_cvt_pk_bf16_f32 v218, v124, v125\n"
    "v_cvt_pk_bf16_f32 v219, v126, v127\n"
    "v_accvgpr_write_b32 a60, v216\n"
    "v_accvgpr_write_b32 a61, v217\n"
    "v_accvgpr_write_b32 a62, v218\n"
    "v_accvgpr_write_b32 a63, v219\n"
    "; ---- wo fragments published; reference max m from the first 16 keys (TM: true max on the second pass)\n"
    "s_waitcnt vmcnt(8)\n"
    "s_waitcnt lgkmcnt(0)\n"
    "s_barrier\n"
    "s_cmp_lg_u32 s48, 0\n"
    "s_cbranch_scc1 WFL%=\n"
    "s_lshl_b32 s46, s57, 5\n"
    "s_add_u32 s46, s46, s58\n"
    "s_lshl_b32 s46, s46, 3\n"
    "s_add_u32 s46, s46, 8192\n"
    "v_mov_b32 v252, s46\n"
    "v_mov_b32 v254, s96\n"
    "v_mov_b32 v255, s97\n"
    "global_store_dwordx2 v252, v[254:255], s[32:33] sc1\n"
    "WFL%=:\n"
    "s_mov_b64 s[52:53], s[60:61]\n"
    "s_and_b32 s53, s53, 0xffff\n"
    "s_mov_b32 s54, 131072\n"
    "s_mov_b32 s55, 0x00020000\n"
    "s_lshr_b32 s89, s49, 5\n"
    "s_mov_b64 s[90:91], s[60:61]\n"
    "s_mov_b64 s[92:93], s[62:63]\n"
    "s_cmp_lg_u32 s51, 0\n"
    "s_cbranch_scc1 TM%=\n"
    "v_mfma_f32_16x16x32_bf16 v[128:131], a[64:67], a[0:3], 0\n"
    "v_mfma_f32_16x16x32_bf16 v[132:135], a[64:67], a[16:19], 0\n"
    "v_mfma_f32_16x16x32_bf16 v[136:139], a[64:67], a[32:35], 0\n"
    "v_mfma_f32_16x16x32_bf16 v[140:143], a[64:67], a[48:51], 0\n"
    "v_mfma_f32_16x16x32_bf16 v[128:131], a[68:71], a[4:7], v[128:131]\n"
    "v_mfma_f32_16x16x32_bf16 v[132:135], a[68:71], a[20:23], v[132:135]\n"
    "v_mfma_f32_16x16x32_bf16 v[136:139], a[68:71], a[36:39], v[136:139]\n"
    "v_mfma_f32_16x16x32_bf16 v[140:143], a[68:71], a[52:55], v[140:143]\n"
    "v_mfma_f32_16x16x32_bf16 v[128:131], a[72:75], a[8:11], v[128:131]\n"
    "v_mfma_f32_16x16x32_bf16 v[132:135], a[72:75], a[24:27], v[132:135]\n"
    "v_mfma_f32_16x16x32_bf16 v[136:139], a[72:75], a[40:43], v[136:139]\n"
    "v_mfma_f32_16x16x32_bf16 v[140:143], a[72:75], a[56:59], v[140:143]\n"
    "v_mfma_f32_16x16x32_bf16 v[128:131], a[76:79], a[12:15], v[128:131]\n"
    "v_mfma_f32_16x16x32_bf16 v[132:135], a[76:79], a[28:31], v[132:135]\n"
    "v_mfma_f32_16x16x32_bf16 v[136:139], a[76:79], a[44:47], v[136:139]\n"
    "v_mfma_f32_16x16x32_bf16 v[140:143], a[76:79], a[60:63], v[140:143]\n"
    "s_nop 4\n"
    "v_max3_f32 v212, v128, v129, v130\n"
    "v_max3_f32 v214, v132, v133, v134\n"
    "v_max3_f32 v216, v136, v137, v138\n"
    "v_max3_f32 v218, v140, v141, v142\n"
    "v_max_f32 v212, v212, v131\n"
    "v_max_f32 v214, v214, v135\n"
    "v_max_f32 v216, v216, v139\n"
    "v_max_f32 v218, v218, v143\n"
    "v_mov_b32 v213, v212\n"
    "v_mov_b32 v215, v214\n"
    "v_mov_b32 v217, v216\n"
    "v_mov_b32 v219, v218\n"
    "v_permlane32_swap_b32 v212, v213\n"
    "v_permlane32_swap_b32 v214, v215\n"
    "v_permlane32_swap_b32 v216, v217\n"
    "v_permlane32_swap_b32 v218, v219\n"
    "v_max_f32 v212, v212, v213\n"
    "v_max_f32 v214, v214, v215\n"
    "v_max_f32 v216, v216, v217\n"
    "v_max_f32 v218, v218, v219\n"
    "v_mov_b32 v213, v212\n"
    "v_mov_b32 v215, v214\n"
    "v_mov_b32 v217, v216\n"
    "v_mov_b32 v219, v218\n"
    "v_permlane16_swap_b32 v212, v213\n"
    "v_permlane16_swap_b32 v214, v215\n"
    "v_permlane16_swap_b32 v216, v217\n"
    "v_permlane16_swap_b32 v218, v219\n"
    "v_max_f32 v212, v212, v213\n"
    "v_max_f32 v214, v214, v215\n"
    "v_max_f32 v216, v216, v217\n"
    "v_max_f32 v218, v218, v219\n"
    "v_sub_f32 v160, 0, v212\n"
    "v_sub_f32 v164, 0, v214\n"
    "v_sub_f32 v168, 0, v216\n"
    "v_sub_f32 v172, 0, v218\n"
    "v_sub_f32 v161, 0, v212\n"
    "v_sub_f32 v165, 0, v214\n"
    "v_sub_f32 v169, 0, v216\n"
    "v_sub_f32 v173, 0, v218\n"
    "v_sub_f32 v162, 0, v212\n"
    "v_sub_f32 v166, 0, v214\n"
    "v_sub_f32 v170, 0, v216\n"
    "v_sub_f32 v174, 0, v218\n"
    "v_sub_f32 v163, 0, v212\n"
    "v_sub_f32 v167, 0, v214\n"
    "v_sub_f32 v171, 0, v216\n"
    "v_sub_f32 v175, 0, v218\n"
    "MAXD%=:\n"
    "s_waitcnt lgkmcnt(0)\n"
    "s_memrealtime s[64:65]\n"
    "s_waitcnt lgkmcnt(0)\n"
    "s_sub_u32 s59, s64, 19\n"
    "s_mov_b32 s56, 148\n"
    "s_mov_b32 s67, 0\n"
    "s_mov_b32 s66, 0\n"
    "s_mov_b32 s45, 136\n"
    "s_waitcnt lgkmcnt(0)\n"
    "; ---- attention, tile 0\n"
    "v_mfma_f32_16x16x32_bf16 v[128:131], a[64:67], a[0:3], v[160:163]\n"
    "v_mfma_f32_16x16x32_bf16 v[128:131], a[68:71], a[4:7], v[128:131]\n"
    "s_barrier\n"
    "v_mfma_f32_16x16x32_bf16 v[128:131], a[72:75], a[8:11], v[128:131]\n"
    "global_load_dwordx2 v[248:249], v238, s[32:33] sc1\n"
    "v_mfma_f32_16x16x32_bf16 v[128:131], a[76:79], a[12:15], v[128:131]\n"
    "s_add_u32 m0, s30, 0xc000\n"
    "ds_read_b64_tr_b16 a[128:129], v184 offset:0\n"
    "v_mfma_f32_16x16x32_bf16 v[132:135], a[80:83], a[0:3], v[160:163]\n"
    "ds_read_b64_tr_b16 a[130:131], v184 offset:4096\n"
    "v_mfma_f32_16x16x32_bf16 v[132:135], a[84:87], a[4:7], v[132:135]\n"
    "buffer_load_dwordx4 v192, s[20:23], s28 offen offset:0 nt lds\n"
    "v_mfma_f32_16x16x32_bf16 v[132:135], a[88:91], a[8:11], v[132:135]\n"
    "ds_read_b64_tr_b16 a[132:133], v185 offset:0\n"
    "v_mfma_f32_16x16x32_bf16 v[132:135], a[92:95], a[12:15], v[132:135]\n"
    "v_exp_f32 v128, v128\n"
    "ds_read_b64_tr_b16 a[134:135], v185 offset:4096\n"
    "v_exp_f32 v129, v129\n"
    "buffer_load_dwordx4 v193, s[20:23], s28 offen offset:1024 nt lds\n"
    "v_exp_f32 v130, v130\n"
    "ds_read_b64_tr_b16 a[136:137], v186 offset:0\n"
    "v_cvt_pk_bf16_f32 v144, v128, v129\n"
    "v_exp_f32 v131, v131\n"
    "buffer_load_dwordx4 v194, s[20:23], s28 offen offset:2048 nt lds\n"
    "v_cvt_pk_bf16_f32 v145, v130, v131\n"
    "v_exp_f32 v132, v132\n"
    "ds_read_b64_tr_b16 a[138:139], v186 offset:4096\n"
    "ds_read_b64_tr_b16 a[140:141], v187 offset:0\n"
    "v_mfma_f32_16x16x32_bf16 v[136:139], a[64:67], a[16:19], v[164:167]\n"
    "ds_read_b64_tr_b16 a[142:143], v187 offset:4096\n"
    "v_exp_f32 v133, v133\n"
    "v_mfma_f32_16x16x32_bf16 v[136:139], a[68:71], a[20:23], v[136:139]\n"
    "buffer_load_dwordx4 v195, s[20:23], s28 offen offset:3072 nt lds\n"
    "v_cvt_pk_bf16_f32 v146, v132, v133\n"
    "v_mfma_f32_16x16x32_bf16 v[136:139], a[72:75], a[24:27], v[136:139]\n"
    "v_exp_f32 v134, v134\n"
    "v_mfma_f32_16x16x32_bf16 v[136:139], a[76:79], a[28:31], v[136:139]\n"
    "s_add_u32 s28, s28, 16384\n"
    "s_and_b32 s28, s28, 0x7ffff\n"
    "s_add_u32 m0, s30, 0x18000\n"
    "v_mfma_f32_16x16x32_bf16 v[140:143], a[80:83], a[16:19], v[164:167]\n"
    "v_exp_f32 v135, v135\n"
    "v_mfma_f32_16x16x32_bf16 v[140:143], a[84:87], a[20:23], v[140:143]\n"
    "buffer_load_dwordx4 v196, s[24:27], s29 offen offset:0 nt lds\n"
    "v_mfma_f32_16x16x32_bf16 v[140:143], a[88:91], a[24:27], v[140:143]\n"
    "ds_read_b64_tr_b16 a[144:145], v188 offset:0\n"
    "ds_read_b64_tr_b16 a[146:147], v188 offset:4096\n"
    "v_mfma_f32_16x16x32_bf16 v[140:143], a[92:95], a[28:31], v[140:143]\n"
    "v_exp_f32 v136, v136\n"
    "ds_read_b64_tr_b16 a[148:149], v189 offset:0\n"
    "v_cvt_pk_bf16_f32 v147, v134, v135\n"
    "v_exp_f32 v137, v137\n"
    "buffer_load_dwordx4 v197, s[24:27], s29 offen offset:1024 nt lds\n"
    "v_cvt_pk_bf16_f32 v148, v136, v137\n"
    "v_exp_f32 v138, v138\n"
    "ds_read_b64_tr_b16 a[150:151], v189 offset:4096\n"
    "ds_read_b64_tr_b16 a[152:153], v190 offset:0\n"
    "v_exp_f32 v139, v139\n"
    "buffer_load_dwordx4 v196, s[24:27], s29 offen offset:2048 nt lds\n"
    "v_cvt_pk_bf16_f32 v149, v138, v139\n"
    "v_exp_f32 v140, v140\n"
    "ds_read_b64_tr_b16 a[154:155], v190 offset:4096\n"
    "ds_read_b64_tr_b16 a[156:157], v191 offset:0\n"
    "v_mfma_f32_16x16x32_bf16 v[128:131], a[64:67], a[32:35], v[168:171]\n"
    "v_exp_f32 v141, v141\n"
    "v_mfma_f32_16x16x32_bf16 v[128:131], a[68:71], a[36:39], v[128:131]\n"
    "buffer_load_dwordx4 v197, s[24:27], s29 offen offset:3072 nt lds\n"
    "v_mfma_f32_16x16x32_bf16 v[128:131], a[72:75], a[40:43], v[128:131]\n"
    "v_exp_f32 v142, v142\n"
    "v_mfma_f32_16x16x32_bf16 v[128:131], a[76:79], a[44:47], v[128:131]\n"
    "s_add_u32 s29, s29, 16384\n"
    "s_and_b32 s29, s29, 0x7ffff\n"
    "ds_read_b64_tr_b16 a[158:159], v191 offset:4096\n"
    "v_mfma_f32_16x16x32_bf16 v[132:135], a[80:83], a[32:35], v[168:171]\n"
    "v_exp_f32 v143, v143\n"
    "v_mfma_f32_16x16x32_bf16 v[132:135], a[84:87], a[36:39], v[132:135]\n"
    "v_cvt_pk_bf16_f32 v150, v140, v141\n"
    "v_cvt_pk_bf16_f32 v151, v142, v143\n"
    "v_mfma_f32_16x16x32_bf16 v[132:135], a[88:91], a[40:43], v[132:135]\n"
    "v_mfma_f32_16x16x32_bf16 v[132:135], a[92:95], a[44:47], v[132:135]\n"
    "s_waitcnt lgkmcnt(0)\n"
    "v_exp_f32 v128, v128\n"
    "v_mfma_f32_16x16x32_bf16 v[0:3], a[128:131], v[144:147], 0\n"
    "ds_read_b128 a[96:99], v180 offset:8192\n"
    "v_mfma_f32_16x16x32_bf16 v[16:19], a[132:135], v[144:147], 0\n"
    "v_exp_f32 v129, v129\n"
    "v_mfma_f32_16x16x32_bf16 v[32:35], a[136:139], v[144:147], 0\n"
    "ds_read_b128 a[100:103], v181 offset:8192\n"
    "v_cvt_pk_bf16_f32 v152, v128, v129\n"
    "v_mfma_f32_16x16x32_bf16 v[48:51], a[140:143], v[144:147], 0\n"
    "v_exp_f32 v130, v130\n"
    "v_mfma_f32_16x16x32_bf16 v[64:67], a[144:147], v[144:147], 0\n"
    "ds_read_b128 a[104:107], v182 offset:8192\n"
    "v_mfma_f32_16x16x32_bf16 v[80:83], a[148:151], v[144:147], 0\n"
    "v_exp_f32 v131, v131\n"
    "v_mfma_f32_16x16x32_bf16 v[96:99], a[152:155], v[144:147], 0\n"
    "ds_read_b128 a[108:111], v183 offset:8192\n"
    "v_cvt_pk_bf16_f32 v153, v130, v131\n"
    "v_mfma_f32_16x16x32_bf16 v[112:115], a[156:159], v[144:147], 0\n"
    "v_exp_f32 v132, v132\n"
    "v_mfma_f32_16x16x32_bf16 a[192:195], v[176:179], v[144:147], 0\n"
    "ds_read_b128 a[112:115], v180 offset:12288\n"
    "v_mfma_f32_16x16x32_bf16 v[136:139], a[64:67], a[48:51], v[172:175]\n"
    "v_exp_f32 v133, v133\n"
    "v_mfma_f32_16x16x32_bf16 v[136:139], a[68:71], a[52:55], v[136:139]\n"
    "ds_read_b128 a[116:119], v181 offset:12288\n"
    "v_cvt_pk_bf16_f32 v154, v132, v133\n"
    "v_mfma_f32_16x16x32_bf16 v[136:139], a[72:75], a[56:59], v[136:139]\n"
    "v_exp_f32 v134, v134\n"
    "v_mfma_f32_16x16x32_bf16 v[136:139], a[76:79], a[60:63], v[136:139]\n"
    "v_mfma_f32_16x16x32_bf16 v[140:143], a[80:83], a[48:51], v[172:175]\n"
    "v_exp_f32 v135, v135\n"
    "v_mfma_f32_16x16x32_bf16 v[140:143], a[84:87], a[52:55], v[140:143]\n"
    "ds_read_b128 a[120:123], v182 offset:12288\n"
    "v_cvt_pk_bf16_f32 v155, v134, v135\n"
    "v_mfma_f32_16x16x32_bf16 v[140:143], a[88:91], a[56:59], v[140:143]\n"
    "ds_read_b128 a[124:127], v183 offset:12288\n"
    "v_mfma_f32_16x16x32_bf16 v[140:143], a[92:95], a[60:63], v[140:143]\n"
    "v_exp_f32 v136, v136\n"
    "v_mfma_f32_16x16x32_bf16 v[4:7], a[128:131], v[148:151], 0\n"
    "v_mfma_f32_16x16x32_bf16 v[20:23], a[132:135], v[148:151], 0\n"
    "v_exp_f32 v137, v137\n"
    "v_mfma_f32_16x16x32_bf16 v[36:39], a[136:139], v[148:151], 0\n"
    "v_cvt_pk_bf16_f32 v156, v136, v137\n"
    "v_mfma_f32_16x16x32_bf16 v[52:55], a[140:143], v[148:151], 0\n"
    "v_exp_f32 v138, v138\n"
    "v_mfma_f32_16x16x32_bf16 v[68:71], a[144:147], v[148:151], 0\n"
    "ds_read_b64_tr_b16 a[160:161], v184 offset:8192\n"
    "v_mfma_f32_16x16x32_bf16 v[84:87], a[148:151], v[148:151], 0\n"
    "v_exp_f32 v139, v139\n"
    "v_mfma_f32_16x16x32_bf16 v[100:103], a[152:155], v[148:151], 0\n"
    "ds_read_b64_tr_b16 a[162:163], v184 offset:12288\n"
    "v_cvt_pk_bf16_f32 v157, v138, v139\n"
    "v_mfma_f32_16x16x32_bf16 v[116:119], a[156:159], v[148:151], 0\n"
    "v_exp_f32 v140, v140\n"
    "v_mfma_f32_16x16x32_bf16 a[196:199], v[176:179], v[148:151], 0\n"
    "s_waitcnt lgkmcnt(1)\n"
    "ds_read_b64_tr_b16 a[164:165], v185 offset:8192\n"
    "v_mfma_f32_16x16x32_bf16 v[128:131], a[96:99], a[0:3], v[160:163]\n"
    "v_exp_f32 v141, v141\n"
    "v_mfma_f32_16x16x32_bf16 v[128:131], a[100:103], a[4:7], v[128:131]\n"
    "v_cvt_pk_bf16_f32 v158, v140, v141\n"
    "v_mfma_f32_16x16x32_bf16 v[128:131], a[104:107], a[8:11], v[128:131]\n"
    "v_exp_f32 v142, v142\n"
    "v_mfma_f32_16x16x32_bf16 v[128:131], a[108:111], a[12:15], v[128:131]\n"
    "ds_read_b64_tr_b16 a[166:167], v185 offset:12288\n"
    "v_mfma_f32_16x16x32_bf16 v[132:135], a[112:115], a[0:3], v[160:163]\n"
    "v_exp_f32 v143, v143\n"
    "v_mfma_f32_16x16x32_bf16 v[132:135], a[116:119], a[4:7], v[132:135]\n"
    "ds_read_b64_tr_b16 a[168:169], v186 offset:8192\n"
    "v_cvt_pk_bf16_f32 v159, v142, v143\n"
    "v_mfma_f32_16x16x32_bf16 v[132:135], a[120:123], a[8:11], v[132:135]\n"
    "v_mfma_f32_16x16x32_bf16 v[132:135], a[124:127], a[12:15], v[132:135]\n"
    "v_exp_f32 v128, v128\n"
    "v_mfma_f32_16x16x32_bf16 v[8:11], a[128:131], v[152:155], 0\n"
    "ds_read_b64_tr_b16 a[170:171], v186 offset:12288\n"
    "v_mfma_f32_16x16x32_bf16 v[24:27], a[132:135], v[152:155], 0\n"
    "v_exp_f32 v129, v129\n"
    "v_mfma_f32_16x16x32_bf16 v[40:43], a[136:139], v[152:155], 0\n"
    "ds_read_b64_tr_b16 a[172:173], v187 offset:8192\n"
    "v_cvt_pk_bf16_f32 v144, v128, v129\n"
    "v_mfma_f32_16x16x32_bf16 v[56:59], a[140:143], v[152:155], 0\n"
    "v_exp_f32 v130, v130\n"
    "v_mfma_f32_16x16x32_bf16 v[72:75], a[144:147], v[152:155], 0\n"
    "ds_read_b64_tr_b16 a[174:175], v187 offset:12288\n"
    "v_mfma_f32_16x16x32_bf16 v[88:91], a[148:151], v[152:155], 0\n"
    "v_exp_f32 v131, v131\n"
    "v_mfma_f32_16x16x32_bf16 v[104:107], a[152:155], v[152:155], 0\n"
    "ds_read_b64_tr_b16 a[176:177], v188 offset:8192\n"
    "v_cvt_pk_bf16_f32 v145, v130, v131\n"
    "v_mfma_f32_16x16x32_bf16 v[120:123], a[156:159], v[152:155], 0\n"
    "v_exp_f32 v132, v132\n"
    "v_mfma_f32_16x16x32_bf16 a[200:203], v[176:179], v[152:155], 0\n"
    "ds_read_b64_tr_b16 a[178:179], v188 offset:12288\n"
    "v_mfma_f32_16x16x32_bf16 v[136:139], a[96:99], a[16:19], v[164:167]\n"
    "v_exp_f32 v133, v133\n"
    "v_mfma_f32_16x16x32_bf16 v[136:139], a[100:103], a[20:23], v[136:139]\n"
    "v_cvt_pk_bf16_f32 v146, v132, v133\n"
    "v_mfma_f32_16x16x32_bf16 v[136:139], a[104:107], a[24:27], v[136:139]\n"
    "v_exp_f32 v134, v134\n"
    "v_mfma_f32_16x16x32_bf16 v[136:139], a[108:111], a[28:31], v[136:139]\n"
    "ds_read_b64_tr_b16 a[180:181], v189 offset:8192\n"
    "v_mfma_f32_16x16x32_bf16 v[140:143], a[112:115], a[16:19], v[164:167]\n"
    "v_exp_f32 v135, v135\n"
    "v_mfma_f32_16x16x32_bf16 v[140:143], a[116:119], a[20:23], v[140:143]\n"
    "ds_read_b64_tr_b16 a[182:183], v189 offset:12288\n"
    "v_cvt_pk_bf16_f32 v147, v134, v135\n"
    "v_mfma_f32_16x16x32_bf16 v[140:143], a[120:123], a[24:27], v[140:143]\n"
    "v_mfma_f32_16x16x32_bf16 v[140:143], a[124:127], a[28:31], v[140:143]\n"
    "v_exp_f32 v136, v136\n"
    "v_mfma_f32_16x16x32_bf16 v[12:15], a[128:131], v[156:159], 0\n"
    "ds_read_b64_tr_b16 a[184:185], v190 offset:8192\n"
    "v_mfma_f32_16x16x32_bf16 v[28:31], a[132:135], v[156:159], 0\n"
    "v_exp_f32 v137, v137\n"
    "v_mfma_f32_16x16x32_bf16 v[44:47], a[136:139], v[156:159], 0\n"
    "ds_read_b64_tr_b16 a[186:187], v190 offset:12288\n"
    "v_cvt_pk_bf16_f32 v148, v136, v137\n"
    "v_mfma_f32_16x16x32_bf16 v[60:63], a[140:143], v[156:159], 0\n"
    "v_exp_f32 v138, v138\n"
    "v_mfma_f32_16x16x32_bf16 v[76:79], a[144:147], v[156:159], 0\n"
    "ds_read_b64_tr_b16 a[188:189], v191 offset:8192\n"
    "v_mfma_f32_16x16x32_bf16 v[92:95], a[148:151], v[156:159], 0\n"
    "v_exp_f32 v139, v139\n"
    "v_mfma_f32_16x16x32_bf16 v[108:111], a[152:155], v[156:159], 0\n"
    "ds_read_b64_tr_b16 a[190:191], v191 offset:12288\n"
    "v_cvt_pk_bf16_f32 v149, v138, v139\n"
    "v_mfma_f32_16x16x32_bf16 v[124:127], a[156:159], v[156:159], 0\n"
    "v_exp_f32 v140, v140\n"
    "v_mfma_f32_16x16x32_bf16 a[204:207], v[176:179], v[156:159], 0\n"
    "v_mfma_f32_16x16x32_bf16 v[128:131], a[96:99], a[32:35], v[168:171]\n"
    "v_exp_f32 v141, v141\n"
    "v_mfma_f32_16x16x32_bf16 v[128:131], a[100:103], a[36:39], v[128:131]\n"
    "v_cvt_pk_bf16_f32 v150, v140, v141\n"
    "v_mfma_f32_16x16x32_bf16 v[128:131], a[104:107], a[40:43], v[128:131]\n"
    "v_exp_f32 v142, v142\n"
    "v_mfma_f32_16x16x32_bf16 v[128:131], a[108:111], a[44:47], v[128:131]\n"
    "ds_read_b128 a[64:67], v180 offset:16384\n"
    "v_mfma_f32_16x16x32_bf16 v[132:135], a[112:115], a[32:35], v[168:171]\n"
    "v_exp_f32 v143, v143\n"
    "v_mfma_f32_16x16x32_bf16 v[132:135], a[116:119], a[36:39], v[132:135]\n"
    "ds_read_b128 a[68:71], v181 offset:16384\n"
    "v_cvt_pk_bf16_f32 v151, v142, v143\n"
    "v_mfma_f32_16x16x32_bf16 v[132:135], a[120:123], a[40:43], v[132:135]\n"
    "v_mfma_f32_16x16x32_bf16 v[132:135], a[124:127], a[44:47], v[132:135]\n"
    "s_waitcnt lgkmcnt(1)\n"
    "v_exp_f32 v128, v128\n"
    "v_mfma_f32_16x16x32_bf16 v[0:3], a[160:163], v[144:147], v[0:3]\n"
    "ds_read_b128 a[72:75], v182 offset:16384\n"
    "v_mfma_f32_16x16x32_bf16 v[16:19], a[164:167], v[144:147], v[16:19]\n"
    "v_exp_f32 v129, v129\n"
    "v_mfma_f32_16x16x32_bf16 v[32:35], a[168:171], v[144:147], v[32:35]\n"
    "v_cvt_pk_bf16_f32 v152, v128, v129\n"
    "v_mfma_f32_16x16x32_bf16 v[48:51], a[172:175], v[144:147], v[48:51]\n"
    "v_exp_f32 v130, v130\n"
    "v_mfma_f32_16x16x32_bf16 v[64:67], a[176:179], v[144:147], v[64:67]\n"
    "ds_read_b128 a[76:79], v183 offset:16384\n"
    "v_mfma_f32_16x16x32_bf16 v[80:83], a[180:183], v[144:147], v[80:83]\n"
    "v_exp_f32 v131, v131\n"
    "v_mfma_f32_16x16x32_bf16 v[96:99], a[184:187], v[144:147], v[96:99]\n"
    "ds_read_b128 a[80:83], v180 offset:20480\n"
    "v_cvt_pk_bf16_f32 v153, v130, v131\n"
    "v_mfma_f32_16x16x32_bf16 v[112:115], a[188:191], v[144:147], v[112:115]\n"
    "v_exp_f32 v132, v132\n"
    "v_mfma_f32_16x16x32_bf16 a[192:195], v[176:179], v[144:147], a[192:195]\n"
    "s_memrealtime s[64:65]\n"
    "v_mfma_f32_16x16x32_bf16 v[136:139], a[96:99], a[48:51], v[172:175]\n"
    "v_exp_f32 v133, v133\n"
    "v_mfma_f32_16x16x32_bf16 v[136:139], a[100:103], a[52:55], v[136:139]\n"
    "ds_read_b128 a[84:87], v181 offset:20480\n"
    "v_cvt_pk_bf16_f32 v154, v132, v133\n"
    "v_mfma_f32_16x16x32_bf16 v[136:139], a[104:107], a[56:59], v[136:139]\n"
    "v_exp_f32 v134, v134\n"
    "v_mfma_f32_16x16x32_bf16 v[136:139], a[108:111], a[60:63], v[136:139]\n"
    "ds_read_b128 a[88:91], v182 offset:20480\n"
    "v_mfma_f32_16x16x32_bf16 v[140:143], a[112:115], a[48:51], v[172:175]\n"
    "v_exp_f32 v135, v135\n"
    "v_mfma_f32_16x16x32_bf16 v[140:143], a[116:119], a[52:55], v[140:143]\n"
    "v_cvt_pk_bf16_f32 v155, v134, v135\n"
    "v_mfma_f32_16x16x32_bf16 v[140:143], a[120:123], a[56:59], v[140:143]\n"
    "ds_read_b128 a[92:95], v183 offset:20480\n"
    "v_mfma_f32_16x16x32_bf16 v[140:143], a[124:127], a[60:63], v[140:143]\n"
    "v_exp_f32 v136, v136\n"
    "v_mfma_f32_16x16x32_bf16 v[4:7], a[160:163], v[148:151], v[4:7]\n"
    "v_mfma_f32_16x16x32_bf16 v[20:23], a[164:167], v[148:151], v[20:23]\n"
    "v_exp_f32 v137, v137\n"
    "v_mfma_f32_16x16x32_bf16 v[36:39], a[168:171], v[148:151], v[36:39]\n"
    "v_cvt_pk_bf16_f32 v156, v136, v137\n"
    "v_mfma_f32_16x16x32_bf16 v[52:55], a[172:175], v[148:151], v[52:55]\n"
    "v_exp_f32 v138, v138\n"
    "v_mfma_f32_16x16x32_bf16 v[68:71], a[176:179], v[148:151], v[68:71]\n"
    "v_mfma_f32_16x16x32_bf16 v[84:87], a[180:183], v[148:151], v[84:87]\n"
    "v_exp_f32 v139, v139\n"
    "v_mfma_f32_16x16x32_bf16 v[100:103], a[184:187], v[148:151], v[100:103]\n"
    "v_cvt_pk_bf16_f32 v157, v138, v139\n"
    "v_mfma_f32_16x16x32_bf16 v[116:119], a[188:191], v[148:151], v[116:119]\n"
    "v_exp_f32 v140, v140\n"
    "v_mfma_f32_16x16x32_bf16 a[196:199], v[176:179], v[148:151], a[196:199]\n"
    "s_waitcnt lgkmcnt(0)\n"
    "s_mov_b32 s31, 7\n"
    "; ---- attention, tiles 1..28 (4 tiles per iteration)\n"
    "LOOP%=:\n"
    "v_mfma_f32_16x16x32_bf16 v[128:131], a[64:67], a[0:3], v[160:163]\n"
    "s_waitcnt vmcnt(9)\n"
    "s_waitcnt lgkmcnt(0)\n"
    "v_exp_f32 v141, v141\n"
    "v_mfma_f32_16x16x32_bf16 v[128:131], a[68:71], a[4:7], v[128:131]\n"
    "s_barrier\n"
    "v_cvt_pk_bf16_f32 v158, v140, v141\n"
    "v_mfma_f32_16x16x32_bf16 v[128:131], a[72:75], a[8:11], v[128:131]\n"
    "s_add_u32 s59, s59, s56\n"
    "v_exp_f32 v142, v142\n"
    "v_mfma_f32_16x16x32_bf16 v[128:131], a[76:79], a[12:15], v[128:131]\n"
    "s_sub_u32 s65, s59, s64\n"
    "s_add_u32 m0, s30, 0x0\n"
    "ds_read_b64_tr_b16 a[128:129], v184 offset:16384\n"
    "v_mfma_f32_16x16x32_bf16 v[132:135], a[80:83], a[0:3], v[160:163]\n"
    "v_exp_f32 v143, v143\n"
    "s_cmp_gt_i32 s65, 0\n"
    "s_cselect_b32 s59, s59, s64\n"
    "s_cbranch_scc1 PC0%=\n"
    "PC0D%=:\n"
    "v_mfma_f32_16x16x32_bf16 v[132:135], a[84:87], a[4:7], v[132:135]\n"
    "buffer_load_dwordx4 v192, s[20:23], s28 offen offset:0 nt lds\n"
    "v_mfma_f32_16x16x32_bf16 v[132:135], a[88:91], a[8:11], v[132:135]\n"
    "ds_read_b64_tr_b16 a[130:131], v184 offset:20480\n"
    "ds_read_b64_tr_b16 a[132:133], v185 offset:16384\n"
    "v_mfma_f32_16x16x32_bf16 v[132:135], a[92:95], a[12:15], v[132:135]\n"
    "v_exp_f32 v128, v128\n"
    "s_cmp_gt_u32 s56, 148\n"
    "s_cselect_b32 s42, 2, 1\n"
    "v_mfma_f32_16x16x32_bf16 v[8:11], a[160:163], v[152:155], v[8:11]\n"
    "ds_read_b64_tr_b16 a[134:135], v185 offset:20480\n"
    "v_cvt_pk_bf16_f32 v159, v142, v143\n"
    "v_mfma_f32_16x16x32_bf16 v[24:27], a[164:167], v[152:155], v[24:27]\n"
    "s_sub_u32 s56, s56, s42\n"
    "v_exp_f32 v129, v129\n"
    "v_mfma_f32_16x16x32_bf16 v[40:43], a[168:171], v[152:155], v[40:43]\n"
    "buffer_load_dwordx4 v193, s[20:23], s28 offen offset:1024 nt lds\n"
    "v_mfma_f32_16x16x32_bf16 v[56:59], a[172:175], v[152:155], v[56:59]\n"
    "s_max_u32 s56, s56, s45\n"
    "v_exp_f32 v130, v130\n"
    "v_mfma_f32_16x16x32_bf16 v[72:75], a[176:179], v[152:155], v[72:75]\n"
    "ds_read_b64_tr_b16 a[136:137], v186 offset:16384\n"
    "v_cvt_pk_bf16_f32 v144, v128, v129\n"
    "v_mfma_f32_16x16x32_bf16 v[88:91], a[180:183], v[152:155], v[88:91]\n"
    "v_exp_f32 v131, v131\n"
    "s_cmp_lt_i32 s65, -25\n"
    "s_cselect_b32 s66, 1, 0\n"
    "v_mfma_f32_16x16x32_bf16 v[104:107], a[184:187], v[152:155], v[104:107]\n"
    "buffer_load_dwordx4 v194, s[20:23], s28 offen offset:2048 nt lds\n"
    "v_cvt_pk_bf16_f32 v145, v130, v131\n"
    "v_mfma_f32_16x16x32_bf16 v[120:123], a[188:191], v[152:155], v[120:123]\n"
    "v_exp_f32 v132, v132\n"
    "s_cmp_eq_u32 s31, 7\n"
    "s_cselect_b32 s66, 0, s66\n"
    "v_mfma_f32_16x16x32_bf16 a[200:203], v[176:179], v[152:155], a[200:203]\n"
    "ds_read_b64_tr_b16 a[138:139], v186 offset:20480\n"
    "ds_read_b64_tr_b16 a[140:141], v187 offset:16384\n"
    "v_mfma_f32_16x16x32_bf16 v[136:139], a[64:67], a[16:19], v[164:167]\n"
    "s_and_b32 s67, s67, s66\n"
    "ds_read_b64_tr_b16 a[142:143], v187 offset:20480\n"
    "v_exp_f32 v133, v133\n"
    "v_mfma_f32_16x16x32_bf16 v[136:139], a[68:71], a[20:23], v[136:139]\n"
    "buffer_load_dwordx4 v195, s[20:23], s28 offen offset:3072 nt lds\n"
    "v_cvt_pk_bf16_f32 v146, v132, v133\n"
    "v_mfma_f32_16x16x32_bf16 v[136:139], a[72:75], a[24:27], v[136:139]\n"
    "v_exp_f32 v134, v134\n"
    "s_cmp_lg_u32 s67, 0\n"
    "s_cselect_b32 s56, 160, s56\n"
    "s_cselect_b32 s45, 148, s45\n"
    "v_mfma_f32_16x16x32_bf16 v[136:139], a[76:79], a[28:31], v[136:139]\n"
    "s_add_u32 s28, s28, 16384\n"
    "s_and_b32 s28, s28, 0x7ffff\n"
    "s_add_u32 m0, s30, 0x1c000\n"
    "v_mfma_f32_16x16x32_bf16 v[140:143], a[80:83], a[16:19], v[164:167]\n"
    "s_mov_b32 s67, s66\n"
    "v_exp_f32 v135, v135\n"
    "v_mfma_f32_16x16x32_bf16 v[140:143], a[84:87], a[20:23], v[140:143]\n"
    "buffer_load_dwordx4 v196, s[24:27], s29 offen offset:0 nt lds\n"
    "v_mfma_f32_16x16x32_bf16 v[140:143], a[88:91], a[24:27], v[140:143]\n"
    "ds_read_b64_tr_b16 a[144:145], v188 offset:16384\n"
    "ds_read_b64_tr_b16 a[146:147], v188 offset:20480\n"
    "v_mfma_f32_16x16x32_bf16 v[140:143], a[92:95], a[28:31], v[140:143]\n"
    "v_exp_f32 v136, v136\n"
    "v_mfma_f32_16x16x32_bf16 v[12:15], a[160:163], v[156:159], v[12:15]\n"
    "ds_read_b64_tr_b16 a[148:149], v189 offset:16384\n"
    "v_cvt_pk_bf16_f32 v147, v134, v135\n"
    "v_mfma_f32_16x16x32_bf16 v[28:31], a[164:167], v[156:159], v[28:31]\n"
    "v_exp_f32 v137, v137\n"
    "v_mfma_f32_16x16x32_bf16 v[44:47], a[168:171], v[156:159], v[44:47]\n"
    "buffer_load_dwordx4 v197, s[24:27], s29 offen offset:1024 nt lds\n"
    "v_cvt_pk_bf16_f32 v148, v136, v137\n"
    "v_mfma_f32_16x16x32_bf16 v[60:63], a[172:175], v[156:159], v[60:63]\n"
    "v_exp_f32 v138, v138\n"
    "v_mfma_f32_16x16x32_bf16 v[76:79], a[176:179], v[156:159], v[76:79]\n"
    "ds_read_b64_tr_b16 a[150:151], v189 offset:20480\n"
    "ds_read_b64_tr_b16 a[152:153], v190 offset:16384\n"
    "v_mfma_f32_16x16x32_bf16 v[92:95], a[180:183], v[156:159], v[92:95]\n"
    "v_exp_f32 v139, v139\n"
    "v_mfma_f32_16x16x32_bf16 v[108:111], a[184:187], v[156:159], v[108:111]\n"
    "buffer_load_dwordx4 v196, s[24:27], s29 offen offset:2048 nt lds\n"
    "v_cvt_pk_bf16_f32 v149, v138, v139\n"
    "v_mfma_f32_16x16x32_bf16 v[124:127], a[188:191], v[156:159], v[124:127]\n"
    "v_exp_f32 v140, v140\n"
    "v_mfma_f32_16x16x32_bf16 a[204:207], v[176:179], v[156:159], a[204:207]\n"
    "ds_read_b64_tr_b16 a[154:155], v190 offset:20480\n"
    "ds_read_b64_tr_b16 a[156:157], v191 offset:16384\n"
    "v_mfma_f32_16x16x32_bf16 v[128:131], a[64:67], a[32:35], v[168:171]\n"
    "v_exp_f32 v141, v141\n"
    "v_mfma_f32_16x16x32_bf16 v[128:131], a[68:71], a[36:39], v[128:131]\n"
    "buffer_load_dwordx4 v197, s[24:27], s29 offen offset:3072 nt lds\n"
    "v_mfma_f32_16x16x32_bf16 v[128:131], a[72:75], a[40:43], v[128:131]\n"
    "v_exp_f32 v142, v142\n"
    "v_mfma_f32_16x16x32_bf16 v[128:131], a[76:79], a[44:47], v[128:131]\n"
    "s_add_u32 s29, s29, 16384\n"
    "s_and_b32 s29, s29, 0x7ffff\n"
    "ds_read_b64_tr_b16 a[158:159], v191 offset:20480\n"
    "v_mfma_f32_16x16x32_bf16 v[132:135], a[80:83], a[32:35], v[168:171]\n"
    "v_exp_f32 v143, v143\n"
    "v_mfma_f32_16x16x32_bf16 v[132:135], a[84:87], a[36:39], v[132:135]\n"
    "v_cvt_pk_bf16_f32 v150, v140, v141\n"
    "v_cvt_pk_bf16_f32 v151, v142, v143\n"
    "v_mfma_f32_16x16x32_bf16 v[132:135], a[88:91], a[40:43], v[132:135]\n"
    "v_mfma_f32_16x16x32_bf16 v[132:135], a[92:95], a[44:47], v[132:135]\n"
    "s_waitcnt lgkmcnt(0)\n"
    "v_exp_f32 v128, v128\n"
    "v_mfma_f32_16x16x32_bf16 v[0:3], a[128:131], v[144:147], v[0:3]\n"
    "ds_read_b128 a[96:99], v180 offset:24576\n"
    "v_mfma_f32_16x16x32_bf16 v[16:19], a[132:135], v[144:147], v[16:19]\n"
    "v_exp_f32 v129, v129\n"
    "v_mfma_f32_16x16x32_bf16 v[32:35], a[136:139], v[144:147], v[32:35]\n"
    "ds_read_b128 a[100:103], v181 offset:24576\n"
    "v_cvt_pk_bf16_f32 v152, v128, v129\n"
    "v_mfma_f32_16x16x32_bf16 v[48:51], a[140:143], v[144:147], v[48:51]\n"
    "v_exp_f32 v130, v130\n"
    "v_mfma_f32_16x16x32_bf16 v[64:67], a[144:147], v[144:147], v[64:67]\n"
    "ds_read_b128 a[104:107], v182 offset:24576\n"
    "v_mfma_f32_16x16x32_bf16 v[80:83], a[148:151], v[144:147], v[80:83]\n"
    "v_exp_f32 v131, v131\n"
    "v_mfma_f32_16x16x32_bf16 v[96:99], a[152:155], v[144:147], v[96:99]\n"
    "ds_read_b128 a[108:111], v183 offset:24576\n"
    "v_cvt_pk_bf16_f32 v153, v130, v131\n"
    "v_mfma_f32_16x16x32_bf16 v[112:115], a[156:159], v[144:147], v[112:115]\n"
    "v_exp_f32 v132, v132\n"
    "v_mfma_f32_16x16x32_bf16 a[192:195], v[176:179], v[144:147], a[192:195]\n"
    "ds_read_b128 a[112:115], v180 offset:28672\n"
    "v_mfma_f32_16x16x32_bf16 v[136:139], a[64:67], a[48:51], v[172:175]\n"
    "v_exp_f32 v133, v133\n"
    "v_mfma_f32_16x16x32_bf16 v[136:139], a[68:71], a[52:55], v[136:139]\n"
    "ds_read_b128 a[116:119], v181 offset:28672\n"
    "v_cvt_pk_bf16_f32 v154, v132, v133\n"
    "v_mfma_f32_16x16x32_bf16 v[136:139], a[72:75], a[56:59], v[136:139]\n"
    "v_exp_f32 v134, v134\n"
    "v_mfma_f32_16x16x32_bf16 v[136:139], a[76:79], a[60:63], v[136:139]\n"
    "v_mfma_f32_16x16x32_bf16 v[140:143], a[80:83], a[48:51], v[172:175]\n"
    "v_exp_f32 v135, v135\n"
    "v_mfma_f32_16x16x32_bf16 v[140:143], a[84:87], a[52:55], v[140:143]\n"
    "ds_read_b128 a[120:123], v182 offset:28672\n"
    "v_cvt_pk_bf16_f32 v155, v134, v135\n"
    "v_mfma_f32_16x16x32_bf16 v[140:143], a[88:91], a[56:59], v[140:143]\n"
    "ds_read_b128 a[124:127], v183 offset:28672\n"
    "v_mfma_f32_16x16x32_bf16 v[140:143], a[92:95], a[60:63], v[140:143]\n"
    "v_exp_f32 v136, v136\n"
    "v_mfma_f32_16x16x32_bf16 v[4:7], a[128:131], v[148:151], v[4:7]\n"
    "v_mfma_f32_16x16x32_bf16 v[20:23], a[132:135], v[148:151], v[20:23]\n"
    "v_exp_f32 v137, v137\n"
    "v_mfma_f32_16x16x32_bf16 v[36:39], a[136:139], v[148:151], v[36:39]\n"
    "v_cvt_pk_bf16_f32 v156, v136, v137\n"
    "v_mfma_f32_16x16x32_bf16 v[52:55], a[140:143], v[148:151], v[52:55]\n"
    "v_exp_f32 v138, v138\n"
    "v_mfma_f32_16x16x32_bf16 v[68:71], a[144:147], v[148:151], v[68:71]\n"
    "ds_read_b64_tr_b16 a[160:161], v184 offset:24576\n"
    "v_mfma_f32_16x16x32_bf16 v[84:87], a[148:151], v[148:151], v[84:87]\n"
    "v_exp_f32 v139, v139\n"
    "v_mfma_f32_16x16x32_bf16 v[100:103], a[152:155], v[148:151], v[100:103]\n"
    "ds_read_b64_tr_b16 a[162:163], v184 offset:28672\n"
    "v_cvt_pk_bf16_f32 v157, v138, v139\n"
    "v_mfma_f32_16x16x32_bf16 v[116:119], a[156:159], v[148:151], v[116:119]\n"
    "v_exp_f32 v140, v140\n"
    "v_mfma_f32_16x16x32_bf16 a[196:199], v[176:179], v[148:151], a[196:199]\n"
    "s_waitcnt lgkmcnt(1)\n"
    "ds_read_b64_tr_b16 a[164:165], v185 offset:24576\n"
    "v_mfma_f32_16x16x32_bf16 v[128:131], a[96:99], a[0:3], v[160:163]\n"
    "v_exp_f32 v141, v141\n"
    "v_mfma_f32_16x16x32_bf16 v[128:131], a[100:103], a[4:7], v[128:131]\n"
    "v_cvt_pk_bf16_f32 v158, v140, v141\n"
    "v_mfma_f32_16x16x32_bf16 v[128:131], a[104:107], a[8:11], v[128:131]\n"
    "v_exp_f32 v142, v142\n"
    "v_mfma_f32_16x16x32_bf16 v[128:131], a[108:111], a[12:15], v[128:131]\n"
    "ds_read_b64_tr_b16 a[166:167], v185 offset:28672\n"
    "v_mfma_f32_16x16x32_bf16 v[132:135], a[112:115], a[0:3], v[160:163]\n"
    "v_exp_f32 v143, v143\n"
    "v_mfma_f32_16x16x32_bf16 v[132:135], a[116:119], a[4:7], v[132:135]\n"
    "ds_read_b64_tr_b16 a[168:169], v186 offset:24576\n"
    "v_cvt_pk_bf16_f32 v159, v142, v143\n"
    "v_mfma_f32_16x16x32_bf16 v[132:135], a[120:123], a[8:11], v[132:135]\n"
    "v_mfma_f32_16x16x32_bf16 v[132:135], a[124:127], a[12:15], v[132:135]\n"
    "v_exp_f32 v128, v128\n"
    "v_mfma_f32_16x16x32_bf16 v[8:11], a[128:131], v[152:155], v[8:11]\n"
    "ds_read_b64_tr_b16 a[170:171], v186 offset:28672\n"
    "v_mfma_f32_16x16x32_bf16 v[24:27], a[132:135], v[152:155], v[24:27]\n"
    "v_exp_f32 v129, v129\n"
    "v_mfma_f32_16x16x32_bf16 v[40:43], a[136:139], v[152:155], v[40:43]\n"
    "ds_read_b64_tr_b16 a[172:173], v187 offset:24576\n"
    "v_cvt_pk_bf16_f32 v144, v128, v129\n"
    "v_mfma_f32_16x16x32_bf16 v[56:59], a[140:143], v[152:155], v[56:59]\n"
    "v_exp_f32 v130, v130\n"
    "v_mfma_f32_16x16x32_bf16 v[72:75], a[144:147], v[152:155], v[72:75]\n"
    "ds_read_b64_tr_b16 a[174:175], v187 offset:28672\n"
    "v_mfma_f32_16x16x32_bf16 v[88:91], a[148:151], v[152:155], v[88:91]\n"
    "v_exp_f32 v131, v131\n"
    "v_mfma_f32_16x16x32_bf16 v[104:107], a[152:155], v[152:155], v[104:107]\n"
    "ds_read_b64_tr_b16 a[176:177], v188 offset:24576\n"
    "v_cvt_pk_bf16_f32 v145, v130, v131\n"
    "v_mfma_f32_16x16x32_bf16 v[120:123], a[156:159], v[152:155], v[120:123]\n"
    "v_exp_f32 v132, v132\n"
    "v_mfma_f32_16x16x32_bf16 a[200:203], v[176:179], v[152:155], a[200:203]\n"
    "ds_read_b64_tr_b16 a[178:179], v188 offset:28672\n"
    "v_mfma_f32_16x16x32_bf16 v[136:139], a[96:99], a[16:19], v[164:167]\n"
    "v_exp_f32 v133, v133\n"
    "v_mfma_f32_16x16x32_bf16 v[136:139], a[100:103], a[20:23], v[136:139]\n"
    "v_cvt_pk_bf16_f32 v146, v132, v133\n"
    "v_mfma_f32_16x16x32_bf16 v[136:139], a[104:107], a[24:27], v[136:139]\n"
    "v_exp_f32 v134, v134\n"
    "v_mfma_f32_16x16x32_bf16 v[136:139], a[108:111], a[28:31], v[136:139]\n"
    "ds_read_b64_tr_b16 a[180:181], v189 offset:24576\n"
    "v_mfma_f32_16x16x32_bf16 v[140:143], a[112:115], a[16:19], v[164:167]\n"
    "v_exp_f32 v135, v135\n"
    "v_mfma_f32_16x16x32_bf16 v[140:143], a[116:119], a[20:23], v[140:143]\n"
    "ds_read_b64_tr_b16 a[182:183], v189 offset:28672\n"
    "v_cvt_pk_bf16_f32 v147, v134, v135\n"
    "v_mfma_f32_16x16x32_bf16 v[140:143], a[120:123], a[24:27], v[140:143]\n"
    "v_mfma_f32_16x16x32_bf16 v[140:143], a[124:127], a[28:31], v[140:143]\n"
    "v_exp_f32 v136, v136\n"
    "v_mfma_f32_16x16x32_bf16 v[12:15], a[128:131], v[156:159], v[12:15]\n"
    "ds_read_b64_tr_b16 a[184:185], v190 offset:24576\n"
    "v_mfma_f32_16x16x32_bf16 v[28:31], a[132:135], v[156:159], v[28:31]\n"
    "v_exp_f32 v137, v137\n"
    "v_mfma_f32_16x16x32_bf16 v[44:47], a[136:139], v[156:159], v[44:47]\n"
    "ds_read_b64_tr_b16 a[186:187], v190 offset:28672\n"
    "v_cvt_pk_bf16_f32 v148, v136, v137\n"
    "v_mfma_f32_16x16x32_bf16 v[60:63], a[140:143], v[156:159], v[60:63]\n"
    "v_exp_f32 v138, v138\n"
    "v_mfma_f32_16x16x32_bf16 v[76:79], a[144:147], v[156:159], v[76:79]\n"
    "ds_read_b64_tr_b16 a[188:189], v191 offset:24576\n"
    "v_mfma_f32_16x16x32_bf16 v[92:95], a[148:151], v[156:159], v[92:95]\n"
    "v_exp_f32 v139, v139\n"
    "v_mfma_f32_16x16x32_bf16 v[108:111], a[152:155], v[156:159], v[108:111]\n"
    "ds_read_b64_tr_b16 a[190:191], v191 offset:28672\n"
    "v_cvt_pk_bf16_f32 v149, v138, v139\n"
    "v_mfma_f32_16x16x32_bf16 v[124:127], a[156:159], v[156:159], v[124:127]\n"
    "v_exp_f32 v140, v140\n"
    "v_mfma_f32_16x16x32_bf16 a[204:207], v[176:179], v[156:159], a[204:207]\n"
    "v_mfma_f32_16x16x32_bf16 v[128:131], a[96:99], a[32:35], v[168:171]\n"
    "v_exp_f32 v141, v141\n"
    "v_mfma_f32_16x16x32_bf16 v[128:131], a[100:103], a[36:39], v[128:131]\n"
    "v_cvt_pk_bf16_f32 v150, v140, v141\n"
    "v_mfma_f32_16x16x32_bf16 v[128:131], a[104:107], a[40:43], v[128:131]\n"
    "v_exp_f32 v142, v142\n"
    "v_mfma_f32_16x16x32_bf16 v[128:131], a[108:111], a[44:47], v[128:131]\n"
    "ds_read_b128 a[64:67], v180 offset:32768\n"
    "v_mfma_f32_16x16x32_bf16 v[132:135], a[112:115], a[32:35], v[168:171]\n"
    "v_exp_f32 v143, v143\n"
    "v_mfma_f32_16x16x32_bf16 v[132:135], a[116:119], a[36:39], v[132:135]\n"
    "ds_read_b128 a[68:71], v181 offset:32768\n"
    "v_cvt_pk_bf16_f32 v151, v142, v143\n"
    "v_mfma_f32_16x16x32_bf16 v[132:135], a[120:123], a[40:43], v[132:135]\n"
    "v_mfma_f32_16x16x32_bf16 v[132:135], a[124:127], a[44:47], v[132:135]\n"
    "s_waitcnt lgkmcnt(1)\n"
    "v_exp_f32 v128, v128\n"
    "v_mfma_f32_16x16x32_bf16 v[0:3], a[160:163], v[144:147], v[0:3]\n"
    "ds_read_b128 a[72:75], v182 offset:32768\n"
    "v_mfma_f32_16x16x32_bf16 v[16:19], a[164:167], v[144:147], v[16:19]\n"
    "v_exp_f32 v129, v129\n"
    "v_mfma_f32_16x16x32_bf16 v[32:35], a[168:171], v[144:147], v[32:35]\n"
    "v_cvt_pk_bf16_f32 v152, v128, v129\n"
    "v_mfma_f32_16x16x32_bf16 v[48:51], a[172:175], v[144:147], v[48:51]\n"
    "v_exp_f32 v130, v130\n"
    "v_mfma_f32_16x16x32_bf16 v[64:67], a[176:179], v[144:147], v[64:67]\n"
    "ds_read_b128 a[76:79], v183 offset:32768\n"
    "v_mfma_f32_16x16x32_bf16 v[80:83], a[180:183], v[144:147], v[80:83]\n"
    "v_exp_f32 v131, v131\n"
    "v_mfma_f32_16x16x32_bf16 v[96:99], a[184:187], v[144:147], v[96:99]\n"
    "ds_read_b128 a[80:83], v180 offset:36864\n"
    "v_cvt_pk_bf16_f32 v153, v130, v131\n"
    "v_mfma_f32_16x16x32_bf16 v[112:115], a[188:191], v[144:147], v[112:115]\n"
    "v_exp_f32 v132, v132\n"
    "v_mfma_f32_16x16x32_bf16 a[192:195], v[176:179], v[144:147], a[192:195]\n"
    "s_memrealtime s[64:65]\n"
    "v_mfma_f32_16x16x32_bf16 v[136:139], a[96:99], a[48:51], v[172:175]\n"
    "v_exp_f32 v133, v133\n"
    "v_mfma_f32_16x16x32_bf16 v[136:139], a[100:103], a[52:55], v[136:139]\n"
    "ds_read_b128 a[84:87], v181 offset:36864\n"
    "v_cvt_pk_bf16_f32 v154, v132, v133\n"
    "v_mfma_f32_16x16x32_bf16 v[136:139], a[104:107], a[56:59], v[136:139]\n"
    "v_exp_f32 v134, v134\n"
    "v_mfma_f32_16x16x32_bf16 v[136:139], a[108:111], a[60:63], v[136:139]\n"
    "ds_read_b128 a[88:91], v182 offset:36864\n"
    "v_mfma_f32_16x16x32_bf16 v[140:143], a[112:115], a[48:51], v[172:175]\n"
    "v_exp_f32 v135, v135\n"
    "v_mfma_f32_16x16x32_bf16 v[140:143], a[116:119], a[52:55], v[140:143]\n"
    "v_cvt_pk_bf16_f32 v155, v134, v135\n"
    "v_mfma_f32_16x16x32_bf16 v[140:143], a[120:123], a[56:59], v[140:143]\n"
    "ds_read_b128 a[92:95], v183 offset:36864\n"
    "v_mfma_f32_16x16x32_bf16 v[140:143], a[124:127], a[60:63], v[140:143]\n"
    "v_exp_f32 v136, v136\n"
    "v_mfma_f32_16x16x32_bf16 v[4:7], a[160:163], v[148:151], v[4:7]\n"
    "v_mfma_f32_16x16x32_bf16 v[20:23], a[164:167], v[148:151], v[20:23]\n"
    "v_exp_f32 v137, v137\n"
    "v_mfma_f32_16x16x32_bf16 v[36:39], a[168:171], v[148:151], v[36:39]\n"
    "v_cvt_pk_bf16_f32 v156, v136, v137\n"
    "v_mfma_f32_16x16x32_bf16 v[52:55], a[172:175], v[148:151], v[52:55]\n"
    "v_exp_f32 v138, v138\n"
    "v_mfma_f32_16x16x32_bf16 v[68:71], a[176:179], v[148:151], v[68:71]\n"
    "v_mfma_f32_16x16x32_bf16 v[84:87], a[180:183], v[148:151], v[84:87]\n"
    "v_exp_f32 v139, v139\n"
    "v_mfma_f32_16x16x32_bf16 v[100:103], a[184:187], v[148:151], v[100:103]\n"
    "v_cvt_pk_bf16_f32 v157, v138, v139\n"
    "v_mfma_f32_16x16x32_bf16 v[116:119], a[188:191], v[148:151], v[116:119]\n"
    "v_exp_f32 v140, v140\n"
    "v_mfma_f32_16x16x32_bf16 a[196:199], v[176:179], v[148:151], a[196:199]\n"
    "v_mfma_f32_16x16x32_bf16 v[128:131], a[64:67], a[0:3], v[160:163]\n"
    "s_waitcnt vmcnt(8)\n"
    "s_waitcnt lgkmcnt(0)\n"
    "v_exp_f32 v141, v141\n"
    "v_mfma_f32_16x16x32_bf16 v[128:131], a[68:71], a[4:7], v[128:131]\n"
    "s_barrier\n"
    "v_cvt_pk_bf16_f32 v158, v140, v141\n"
    "v_mfma_f32_16x16x32_bf16 v[128:131], a[72:75], a[8:11], v[128:131]\n"
    "s_add_u32 s59, s59, s56\n"
    "v_exp_f32 v142, v142\n"
    "v_mfma_f32_16x16x32_bf16 v[128:131], a[76:79], a[12:15], v[128:131]\n"
    "s_sub_u32 s65, s59, s64\n"
    "s_add_u32 m0, s30, 0x4000\n"
    "ds_read_b64_tr_b16 a[128:129], v184 offset:32768\n"
    "v_mfma_f32_16x16x32_bf16 v[132:135], a[80:83], a[0:3], v[160:163]\n"
    "v_exp_f32 v143, v143\n"
    "s_cmp_gt_i32 s65, 0\n"
    "s_cselect_b32 s59, s59, s64\n"
    "s_cbranch_scc1 PC1%=\n"
    "PC1D%=:\n"
    "v_mfma_f32_16x16x32_bf16 v[132:135], a[84:87], a[4:7], v[132:135]\n"
    "buffer_load_dwordx4 v192, s[20:23], s28 offen offset:0 nt lds\n"
    "v_mfma_f32_16x16x32_bf16 v[132:135], a[88:91], a[8:11], v[132:135]\n"
    "ds_read_b64_tr_b16 a[130:131], v184 offset:36864\n"
    "ds_read_b64_tr_b16 a[132:133], v185 offset:32768\n"
    "v_mfma_f32_16x16x32_bf16 v[132:135], a[92:95], a[12:15], v[132:135]\n"
    "v_exp_f32 v128, v128\n"
    "s_cmp_gt_u32 s56, 148\n"
    "s_cselect_b32 s42, 2, 1\n"
    "v_mfma_f32_16x16x32_bf16 v[8:11], a[160:163], v[152:155], v[8:11]\n"
    "ds_read_b64_tr_b16 a[134:135], v185 offset:36864\n"
    "v_cvt_pk_bf16_f32 v159, v142, v143\n"
    "v_mfma_f32_16x16x32_bf16 v[24:27], a[164:167], v[152:155], v[24:27]\n"
    "s_sub_u32 s56, s56, s42\n"
    "v_exp_f32 v129, v129\n"
    "v_mfma_f32_16x16x32_bf16 v[40:43], a[168:171], v[152:155], v[40:43]\n"
    "buffer_load_dwordx4 v193, s[20:23], s28 offen offset:1024 nt lds\n"
    "v_mfma_f32_16x16x32_bf16 v[56:59], a[172:175], v[152:155], v[56:59]\n"
    "s_max_u32 s56, s56, s45\n"
    "v_exp_f32 v130, v130\n"
    "v_mfma_f32_16x16x32_bf16 v[72:75], a[176:179], v[152:155], v[72:75]\n"
    "ds_read_b64_tr_b16 a[136:137], v186 offset:32768\n"
    "v_cvt_pk_bf16_f32 v144, v128, v129\n"
    "v_mfma_f32_16x16x32_bf16 v[88:91], a[180:183], v[152:155], v[88:91]\n"
    "v_exp_f32 v131, v131\n"
    "s_cmp_lt_i32 s65, -25\n"
    "s_cselect_b32 s66, 1, 0\n"
    "v_mfma_f32_16x16x32_bf16 v[104:107], a[184:187], v[152:155], v[104:107]\n"
    "buffer_load_dwordx4 v194, s[20:23], s28 offen offset:2048 nt lds\n"
    "v_cvt_pk_bf16_f32 v145, v130, v131\n"
    "v_mfma_f32_16x16x32_bf16 v[120:123], a[188:191], v[152:155], v[120:123]\n"
    "v_exp_f32 v132, v132\n"
    "s_cmp_eq_u32 s31, 7\n"
    "s_cselect_b32 s66, 0, s66\n"
    "v_mfma_f32_16x16x32_bf16 a[200:203], v[176:179], v[152:155], a[200:203]\n"
    "ds_read_b64_tr_b16 a[138:139], v186 offset:36864\n"
    "ds_read_b64_tr_b16 a[140:141], v187 offset:32768\n"
    "v_mfma_f32_16x16x32_bf16 v[136:139], a[64:67], a[16:19], v[164:167]\n"
    "s_and_b32 s67, s67, s66\n"
    "ds_read_b64_tr_b16 a[142:143], v187 offset:36864\n"
    "v_exp_f32 v133, v133\n"
    "v_mfma_f32_16x16x32_bf16 v[136:139], a[68:71], a[20:23], v[136:139]\n"
    "buffer_load_dwordx4 v195, s[20:23], s28 offen offset:3072 nt lds\n"
    "v_cvt_pk_bf16_f32 v146, v132, v133\n"
    "v_mfma_f32_16x16x32_bf16 v[136:139], a[72:75], a[24:27], v[136:139]\n"
    "v_exp_f32 v134, v134\n"
    "s_cmp_lg_u32 s67, 0\n"
    "s_cselect_b32 s56, 160, s56\n"
    "s_cselect_b32 s45, 148, s45\n"
    "v_mfma_f32_16x16x32_bf16 v[136:139], a[76:79], a[28:31], v[136:139]\n"
    "s_add_u32 s28, s28, 16384\n"
    "s_and_b32 s28, s28, 0x7ffff\n"
    "s_add_u32 m0, s30, 0x10000\n"
    "v_mfma_f32_16x16x32_bf16 v[140:143], a[80:83], a[16:19], v[164:167]\n"
    "s_mov_b32 s67, s66\n"
    "v_exp_f32 v135, v135\n"
    "v_mfma_f32_16x16x32_bf16 v[140:143], a[84:87], a[20:23], v[140:143]\n"
    "buffer_load_dwordx4 v196, s[24:27], s29 offen offset:0 nt lds\n"
    "v_mfma_f32_16x16x32_bf16 v[140:143], a[88:91], a[24:27], v[140:143]\n"
    "ds_read_b64_tr_b16 a[144:145], v188 offset:32768\n"
    "ds_read_b64_tr_b16 a[146:147], v188 offset:36864\n"
    "v_mfma_f32_16x16x32_bf16 v[140:143], a[92:95], a[28:31], v[140:143]\n"
    "v_exp_f32 v136, v136\n"
    "v_mfma_f32_16x16x32_bf16 v[12:15], a[160:163], v[156:159], v[12:15]\n"
    "ds_read_b64_tr_b16 a[148:149], v189 offset:32768\n"
    "v_cvt_pk_bf16_f32 v147, v134, v135\n"
    "v_mfma_f32_16x16x32_bf16 v[28:31], a[164:167], v[156:159], v[28:31]\n"
    "v_exp_f32 v137, v137\n"
    "v_mfma_f32_16x16x32_bf16 v[44:47], a[168:171], v[156:159], v[44:47]\n"
    "buffer_load_dwordx4 v197, s[24:27], s29 offen offset:1024 nt lds\n"
    "v_cvt_pk_bf16_f32 v148, v136, v137\n"
    "v_mfma_f32_16x16x32_bf16 v[60:63], a[172:175], v[156:159], v[60:63]\n"
    "v_exp_f32 v138, v138\n"
    "v_mfma_f32_16x16x32_bf16 v[76:79], a[176:179], v[156:159], v[76:79]\n"
    "ds_read_b64_tr_b16 a[150:151], v189 offset:36864\n"
    "ds_read_b64_tr_b16 a[152:153], v190 offset:32768\n"
    "v_mfma_f32_16x16x32_bf16 v[92:95], a[180:183], v[156:159], v[92:95]\n"
    "v_exp_f32 v139, v139\n"
    "v_mfma_f32_16x16x32_bf16 v[108:111], a[184:187], v[156:159], v[108:111]\n"
    "buffer_load_dwordx4 v196, s[24:27], s29 offen offset:2048 nt lds\n"
    "v_cvt_pk_bf16_f32 v149, v138, v139\n"
    "v_mfma_f32_16x16x32_bf16 v[124:127], a[188:191], v[156:159], v[124:127]\n"
    "v_exp_f32 v140, v140\n"
    "v_mfma_f32_16x16x32_bf16 a[204:207], v[176:179], v[156:159], a[204:207]\n"
    "ds_read_b64_tr_b16 a[154:155], v190 offset:36864\n"
    "ds_read_b64_tr_b16 a[156:157], v191 offset:32768\n"
    "v_mfma_f32_16x16x32_bf16 v[128:131], a[64:67], a[32:35], v[168:171]\n"
    "v_exp_f32 v141, v141\n"
    "v_mfma_f32_16x16x32_bf16 v[128:131], a[68:71], a[36:39], v[128:131]\n"
    "buffer_load_dwordx4 v197, s[24:27], s29 offen offset:3072 nt lds\n"
    "v_mfma_f32_16x16x32_bf16 v[128:131], a[72:75], a[40:43], v[128:131]\n"
    "v_exp_f32 v142, v142\n"
    "v_mfma_f32_16x16x32_bf16 v[128:131], a[76:79], a[44:47], v[128:131]\n"
    "s_add_u32 s29, s29, 16384\n"
    "s_and_b32 s29, s29, 0x7ffff\n"
    "ds_read_b64_tr_b16 a[158:159], v191 offset:36864\n"
    "v_mfma_f32_16x16x32_bf16 v[132:135], a[80:83], a[32:35], v[168:171]\n"
    "v_exp_f32 v143, v143\n"
    "v_mfma_f32_16x16x32_bf16 v[132:135], a[84:87], a[36:39], v[132:135]\n"
    "v_cvt_pk_bf16_f32 v150, v140, v141\n"
    "v_cvt_pk_bf16_f32 v151, v142, v143\n"
    "v_mfma_f32_16x16x32_bf16 v[132:135], a[88:91], a[40:43], v[132:135]\n"
    "v_mfma_f32_16x16x32_bf16 v[132:135], a[92:95], a[44:47], v[132:135]\n"
    "s_waitcnt lgkmcnt(0)\n"
    "v_exp_f32 v128, v128\n"
    "v_mfma_f32_16x16x32_bf16 v[0:3], a[128:131], v[144:147], v[0:3]\n"
    "ds_read_b128 a[96:99], v180 offset:40960\n"
    "v_mfma_f32_16x16x32_bf16 v[16:19], a[132:135], v[144:147], v[16:19]\n"
    "v_exp_f32 v129, v129\n"
    "v_mfma_f32_16x16x32_bf16 v[32:35], a[136:139], v[144:147], v[32:35]\n"
    "ds_read_b128 a[100:103], v181 offset:40960\n"
    "v_cvt_pk_bf16_f32 v152, v128, v129\n"
    "v_mfma_f32_16x16x32_bf16 v[48:51], a[140:143], v[144:147], v[48:51]\n"
    "v_exp_f32 v130, v130\n"
    "v_mfma_f32_16x16x32_bf16 v[64:67], a[144:147], v[144:147], v[64:67]\n"
    "ds_read_b128 a[104:107], v182 offset:40960\n"
    "v_mfma_f32_16x16x32_bf16 v[80:83], a[148:151], v[144:147], v[80:83]\n"
    "v_exp_f32 v131, v131\n"
    "v_mfma_f32_16x16x32_bf16 v[96:99], a[152:155], v[144:147], v[96:99]\n"
    "ds_read_b128 a[108:111], v183 offset:40960\n"
    "v_cvt_pk_bf16_f32 v153, v130, v131\n"
    "v_mfma_f32_16x16x32_bf16 v[112:115], a[156:159], v[144:147], v[112:115]\n"
    "v_exp_f32 v132, v132\n"
    "v_mfma_f32_16x16x32_bf16 a[192:195], v[176:179], v[144:147], a[192:195]\n"
    "ds_read_b128 a[112:115], v180 offset:45056\n"
    "v_mfma_f32_16x16x32_bf16 v[136:139], a[64:67], a[48:51], v[172:175]\n"
    "v_exp_f32 v133, v133\n"
    "v_mfma_f32_16x16x32_bf16 v[136:139], a[68:71], a[52:55], v[136:139]\n"
    "ds_read_b128 a[116:119], v181 offset:45056\n"
    "v_cvt_pk_bf16_f32 v154, v132, v133\n"
    "v_mfma_f32_16x16x32_bf16 v[136:139], a[72:75], a[56:59], v[136:139]\n"
    "v_exp_f32 v134, v134\n"
    "v_mfma_f32_16x16x32_bf16 v[136:139], a[76:79], a[60:63], v[136:139]\n"
    "v_mfma_f32_16x16x32_bf16 v[140:143], a[80:83], a[48:51], v[172:175]\n"
    "v_exp_f32 v135, v135\n"
    "v_mfma_f32_16x16x32_bf16 v[140:143], a[84:87], a[52:55], v[140:143]\n"
    "ds_read_b128 a[120:123], v182 offset:45056\n"
    "v_cvt_pk_bf16_f32 v155, v134, v135\n"
    "v_mfma_f32_16x16x32_bf16 v[140:143], a[88:91], a[56:59], v[140:143]\n"
    "ds_read_b128 a[124:127], v183 offset:45056\n"
    "v_mfma_f32_16x16x32_bf16 v[140:143], a[92:95], a[60:63], v[140:143]\n"
    "v_exp_f32 v136, v136\n"
    "v_mfma_f32_16x16x32_bf16 v[4:7], a[128:131], v[148:151], v[4:7]\n"
    "v_mfma_f32_16x16x32_bf16 v[20:23], a[132:135], v[148:151], v[20:23]\n"
    "v_exp_f32 v137, v137\n"
    "v_mfma_f32_16x16x32_bf16 v[36:39], a[136:139], v[148:151], v[36:39]\n"
    "v_cvt_pk_bf16_f32 v156, v136, v137\n"
    "v_mfma_f32_16x16x32_bf16 v[52:55], a[140:143], v[148:151], v[52:55]\n"
    "v_exp_f32 v138, v138\n"
    "v_mfma_f32_16x16x32_bf16 v[68:71], a[144:147], v[148:151], v[68:71]\n"
    "ds_read_b64_tr_b16 a[160:161], v184 offset:40960\n"
    "v_mfma_f32_16x16x32_bf16 v[84:87], a[148:151], v[148:151], v[84:87]\n"
    "v_exp_f32 v139, v139\n"
    "v_mfma_f32_16x16x32_bf16 v[100:103], a[152:155], v[148:151], v[100:103]\n"
    "ds_read_b64_tr_b16 a[162:163], v184 offset:45056\n"
    "v_cvt_pk_bf16_f32 v157, v138, v139\n"
    "v_mfma_f32_16x16x32_bf16 v[116:119], a[156:159], v[148:151], v[116:119]\n"
    "v_exp_f32 v140, v140\n"
    "v_mfma_f32_16x16x32_bf16 a[196:199], v[176:179], v[148:151], a[196:199]\n"
    "s_waitcnt lgkmcnt(1)\n"
    "ds_read_b64_tr_b16 a[164:165], v185 offset:40960\n"
    "v_mfma_f32_16x16x32_bf16 v[128:131], a[96:99], a[0:3], v[160:163]\n"
    "v_exp_f32 v141, v141\n"
    "v_mfma_f32_16x16x32_bf16 v[128:131], a[100:103], a[4:7], v[128:131]\n"
    "v_cvt_pk_bf16_f32 v158, v140, v141\n"
    "v_mfma_f32_16x16x32_bf16 v[128:131], a[104:107], a[8:11], v[128:131]\n"
    "v_exp_f32 v142, v142\n"
    "v_mfma_f32_16x16x32_bf16 v[128:131], a[108:111], a[12:15], v[128:131]\n"
    "ds_read_b64_tr_b16 a[166:167], v185 offset:45056\n"
    "v_mfma_f32_16x16x32_bf16 v[132:135], a[112:115], a[0:3], v[160:163]\n"
    "v_exp_f32 v143, v143\n"
    "v_mfma_f32_16x16x32_bf16 v[132:135], a[116:119], a[4:7], v[132:135]\n"
    "ds_read_b64_tr_b16 a[168:169], v186 offset:40960\n"
    "v_cvt_pk_bf16_f32 v159, v142, v143\n"
    "v_mfma_f32_16x16x32_bf16 v[132:135], a[120:123], a[8:11], v[132:135]\n"
    "v_mfma_f32_16x16x32_bf16 v[132:135], a[124:127], a[12:15], v[132:135]\n"
    "v_exp_f32 v128, v128\n"
    "v_mfma_f32_16x16x32_bf16 v[8:11], a[128:131], v[152:155], v[8:11]\n"
    "ds_read_b64_tr_b16 a[170:171], v186 offset:45056\n"
    "v_mfma_f32_16x16x32_bf16 v[24:27], a[132:135], v[152:155], v[24:27]\n"
    "v_exp_f32 v129, v129\n"
    "v_mfma_f32_16x16x32_bf16 v[40:43], a[136:139], v[152:155], v[40:43]\n"
    "ds_read_b64_tr_b16 a[172:173], v187 offset:40960\n"
    "v_cvt_pk_bf16_f32 v144, v128, v129\n"
    "v_mfma_f32_16x16x32_bf16 v[56:59], a[140:143], v[152:155], v[56:59]\n"
    "v_exp_f32 v130, v130\n"
    "v_mfma_f32_16x16x32_bf16 v[72:75], a[144:147], v[152:155], v[72:75]\n"
    "ds_read_b64_tr_b16 a[174:175], v187 offset:45056\n"
    "v_mfma_f32_16x16x32_bf16 v[88:91], a[148:151], v[152:155], v[88:91]\n"
    "v_exp_f32 v131, v131\n"
    "v_mfma_f32_16x16x32_bf16 v[104:107], a[152:155], v[152:155], v[104:107]\n"
    "ds_read_b64_tr_b16 a[176:177], v188 offset:40960\n"
    "v_cvt_pk_bf16_f32 v145, v130, v131\n"
    "v_mfma_f32_16x16x32_bf16 v[120:123], a[156:159], v[152:155], v[120:123]\n"
    "v_exp_f32 v132, v132\n"
    "v_mfma_f32_16x16x32_bf16 a[200:203], v[176:179], v[152:155], a[200:203]\n"
    "ds_read_b64_tr_b16 a[178:179], v188 offset:45056\n"
    "v_mfma_f32_16x16x32_bf16 v[136:139], a[96:99], a[16:19], v[164:167]\n"
    "v_exp_f32 v133, v133\n"
    "v_mfma_f32_16x16x32_bf16 v[136:139], a[100:103], a[20:23], v[136:139]\n"
    "v_cvt_pk_bf16_f32 v146, v132, v133\n"
    "v_mfma_f32_16x16x32_bf16 v[136:139], a[104:107], a[24:27], v[136:139]\n"
    "v_exp_f32 v134, v134\n"
    "v_mfma_f32_16x16x32_bf16 v[136:139], a[108:111], a[28:31], v[136:139]\n"
    "ds_read_b64_tr_b16 a[180:181], v189 offset:40960\n"
    "v_mfma_f32_16x16x32_bf16 v[140:143], a[112:115], a[16:19], v[164:167]\n"
    "v_exp_f32 v135, v135\n"
    "v_mfma_f32_16x16x32_bf16 v[140:143], a[116:119], a[20:23], v[140:143]\n"
    "ds_read_b64_tr_b16 a[182:183], v189 offset:45056\n"
    "v_cvt_pk_bf16_f32 v147, v134, v135\n"
    "v_mfma_f32_16x16x32_bf16 v[140:143], a[120:123], a[24:27], v[140:143]\n"
    "v_mfma_f32_16x16x32_bf16 v[140:143], a[124:127], a[28:31], v[140:143]\n"
    "v_exp_f32 v136, v136\n"
    "v_mfma_f32_16x16x32_bf16 v[12:15], a[128:131], v[156:159], v[12:15]\n"
    "ds_read_b64_tr_b16 a[184:185], v190 offset:40960\n"
    "v_mfma_f32_16x16x32_bf16 v[28:31], a[132:135], v[156:159], v[28:31]\n"
    "v_exp_f32 v137, v137\n"
    "v_mfma_f32_16x16x32_bf16 v[44:47], a[136:139], v[156:159], v[44:47]\n"
    "ds_read_b64_tr_b16 a[186:187], v190 offset:45056\n"
    "v_cvt_pk_bf16_f32 v148, v136, v137\n"
    "v_mfma_f32_16x16x32_bf16 v[60:63], a[140:143], v[156:159], v[60:63]\n"
    "v_exp_f32 v138, v138\n"
    "v_mfma_f32_16x16x32_bf16 v[76:79], a[144:147], v[156:159], v[76:79]\n"
    "ds_read_b64_tr_b16 a[188:189], v191 offset:40960\n"
    "v_mfma_f32_16x16x32_bf16 v[92:95], a[148:151], v[156:159], v[92:95]\n"
    "v_exp_f32 v139, v139\n"
    "v_mfma_f32_16x16x32_bf16 v[108:111], a[152:155], v[156:159], v[108:111]\n"
    "ds_read_b64_tr_b16 a[190:191], v191 offset:45056\n"
    "v_cvt_pk_bf16_f32 v149, v138, v139\n"
    "v_mfma_f32_16x16x32_bf16 v[124:127], a[156:159], v[156:159], v[124:127]\n"
    "v_exp_f32 v140, v140\n"
    "v_mfma_f32_16x16x32_bf16 a[204:207], v[176:179], v[156:159], a[204:207]\n"
    "v_mfma_f32_16x16x32_bf16 v[128:131], a[96:99], a[32:35], v[168:171]\n"
    "v_exp_f32 v141, v141\n"
    "v_mfma_f32_16x16x32_bf16 v[128:131], a[100:103], a[36:39], v[128:131]\n"
    "v_cvt_pk_bf16_f32 v150, v140, v141\n"
    "v_mfma_f32_16x16x32_bf16 v[128:131], a[104:107], a[40:43], v[128:131]\n"
    "v_exp_f32 v142, v142\n"
    "v_mfma_f32_16x16x32_bf16 v[128:131], a[108:111], a[44:47], v[128:131]\n"
    "ds_read_b128 a[64:67], v180 offset:49152\n"
    "v_mfma_f32_16x16x32_bf16 v[132:135], a[112:115], a[32:35], v[168:171]\n"
    "v_exp_f32 v143, v143\n"
    "v_mfma_f32_16x16x32_bf16 v[132:135], a[116:119], a[36:39], v[132:135]\n"
    "ds_read_b128 a[68:71], v181 offset:49152\n"
    "v_cvt_pk_bf16_f32 v151, v142, v143\n"
    "v_mfma_f32_16x16x32_bf16 v[132:135], a[120:123], a[40:43], v[132:135]\n"
    "v_mfma_f32_16x16x32_bf16 v[132:135], a[124:127], a[44:47], v[132:135]\n"
    "s_waitcnt lgkmcnt(1)\n"
    "v_exp_f32 v128, v128\n"
    "v_mfma_f32_16x16x32_bf16 v[0:3], a[160:163], v[144:147], v[0:3]\n"
    "ds_read_b128 a[72:75], v182 offset:49152\n"
    "v_mfma_f32_16x16x32_bf16 v[16:19], a[164:167], v[144:147], v[16:19]\n"
    "v_exp_f32 v129, v129\n"
    "v_mfma_f32_16x16x32_bf16 v[32:35], a[168:171], v[144:147], v[32:35]\n"
    "v_cvt_pk_bf16_f32 v152, v128, v129\n"
    "v_mfma_f32_16x16x32_bf16 v[48:51], a[172:175], v[144:147], v[48:51]\n"
    "v_exp_f32 v130, v130\n"
    "v_mfma_f32_16x16x32_bf16 v[64:67], a[176:179], v[144:147], v[64:67]\n"
    "ds_read_b128 a[76:79], v183 offset:49152\n"
    "v_mfma_f32_16x16x32_bf16 v[80:83], a[180:183], v[144:147], v[80:83]\n"
    "v_exp_f32 v131, v131\n"
    "v_mfma_f32_16x16x32_bf16 v[96:99], a[184:187], v[144:147], v[96:99]\n"
    "ds_read_b128 a[80:83], v180 offset:53248\n"
    "v_cvt_pk_bf16_f32 v153, v130, v131\n"
    "v_mfma_f32_16x16x32_bf16 v[112:115], a[188:191], v[144:147], v[112:115]\n"
    "v_exp_f32 v132, v132\n"
    "v_mfma_f32_16x16x32_bf16 a[192:195], v[176:179], v[144:147], a[192:195]\n"
    "s_memrealtime s[64:65]\n"
    "v_mfma_f32_16x16x32_bf16 v[136:139], a[96:99], a[48:51], v[172:175]\n"
    "v_exp_f32 v133, v133\n"
    "v_mfma_f32_16x16x32_bf16 v[136:139], a[100:103], a[52:55], v[136:139]\n"
    "ds_read_b128 a[84:87], v181 offset:53248\n"
    "v_cvt_pk_bf16_f32 v154, v132, v133\n"
    "v_mfma_f32_16x16x32_bf16 v[136:139], a[104:107], a[56:59], v[136:139]\n"
    "v_exp_f32 v134, v134\n"
    "v_mfma_f32_16x16x32_bf16 v[136:139], a[108:111], a[60:63], v[136:139]\n"
    "ds_read_b128 a[88:91], v182 offset:53248\n"
    "v_mfma_f32_16x16x32_bf16 v[140:143], a[112:115], a[48:51], v[172:175]\n"
    "v_exp_f32 v135, v135\n"
    "v_mfma_f32_16x16x32_bf16 v[140:143], a[116:119], a[52:55], v[140:143]\n"
    "v_cvt_pk_bf16_f32 v155, v134, v135\n"
    "v_mfma_f32_16x16x32_bf16 v[140:143], a[120:123], a[56:59], v[140:143]\n"
    "ds_read_b128 a[92:95], v183 offset:53248\n"
    "v_mfma_f32_16x16x32_bf16 v[140:143], a[124:127], a[60:63], v[140:143]\n"
    "v_exp_f32 v136, v136\n"
    "v_mfma_f32_16x16x32_bf16 v[4:7], a[160:163], v[148:151], v[4:7]\n"
    "v_mfma_f32_16x16x32_bf16 v[20:23], a[164:167], v[148:151], v[20:23]\n"
    "v_exp_f32 v137, v137\n"
    "v_mfma_f32_16x16x32_bf16 v[36:39], a[168:171], v[148:151], v[36:39]\n"
    "v_cvt_pk_bf16_f32 v156, v136, v137\n"
    "v_mfma_f32_16x16x32_bf16 v[52:55], a[172:175], v[148:151], v[52:55]\n"
    "v_exp_f32 v138, v138\n"
    "v_mfma_f32_16x16x32_bf16 v[68:71], a[176:179], v[148:151], v[68:71]\n"
    "v_mfma_f32_16x16x32_bf16 v[84:87], a[180:183], v[148:151], v[84:87]\n"
    "v_exp_f32 v139, v139\n"
    "v_mfma_f32_16x16x32_bf16 v[100:103], a[184:187], v[148:151], v[100:103]\n"
    "v_cvt_pk_bf16_f32 v157, v138, v139\n"
    "v_mfma_f32_16x16x32_bf16 v[116:119], a[188:191], v[148:151], v[116:119]\n"
    "v_exp_f32 v140, v140\n"
    "v_mfma_f32_16x16x32_bf16 a[196:199], v[176:179], v[148:151], a[196:199]\n"
    "v_mfma_f32_16x16x32_bf16 v[128:131], a[64:67], a[0:3], v[160:163]\n"
    "s_waitcnt vmcnt(8)\n"
    "s_waitcnt lgkmcnt(0)\n"
    "v_exp_f32 v141, v141\n"
    "v_mfma_f32_16x16x32_bf16 v[128:131], a[68:71], a[4:7], v[128:131]\n"
    "s_barrier\n"
    "v_cvt_pk_bf16_f32 v158, v140, v141\n"
    "v_mfma_f32_16x16x32_bf16 v[128:131], a[72:75], a[8:11], v[128:131]\n"
    "s_add_u32 s59, s59, s56\n"
    "v_exp_f32 v142, v142\n"
    "v_mfma_f32_16x16x32_bf16 v[128:131], a[76:79], a[12:15], v[128:131]\n"
    "s_sub_u32 s65, s59, s64\n"
    "s_add_u32 m0, s30, 0x8000\n"
    "ds_read_b64_tr_b16 a[128:129], v184 offset:49152\n"
    "v_mfma_f32_16x16x32_bf16 v[132:135], a[80:83], a[0:3], v[160:163]\n"
    "v_exp_f32 v143, v143\n"
    "s_cmp_gt_i32 s65, 0\n"
    "s_cselect_b32 s59, s59, s64\n"
    "s_cbranch_scc1 PC2%=\n"
    "PC2D%=:\n"
    "v_mfma_f32_16x16x32_bf16 v[132:135], a[84:87], a[4:7], v[132:135]\n"
    "buffer_load_dwordx4 v192, s[20:23], s28 offen offset:0 nt lds\n"
    "v_mfma_f32_16x16x32_bf16 v[132:135], a[88:91], a[8:11], v[132:135]\n"
    "ds_read_b64_tr_b16 a[130:131], v184 offset:53248\n"
    "ds_read_b64_tr_b16 a[132:133], v185 offset:49152\n"
    "v_mfma_f32_16x16x32_bf16 v[132:135], a[92:95], a[12:15], v[132:135]\n"
    "v_exp_f32 v128, v128\n"
    "s_cmp_gt_u32 s56, 148\n"
    "s_cselect_b32 s42, 2, 1\n"
    "v_mfma_f32_16x16x32_bf16 v[8:11], a[160:163], v[152:155], v[8:11]\n"
    "ds_read_b64_tr_b16 a[134:135], v185 offset:53248\n"
    "v_cvt_pk_bf16_f32 v159, v142, v143\n"
    "v_mfma_f32_16x16x32_bf16 v[24:27], a[164:167], v[152:155], v[24:27]\n"
    "s_sub_u32 s56, s56, s42\n"
    "v_exp_f32 v129, v129\n"
    "v_mfma_f32_16x16x32_bf16 v[40:43], a[168:171], v[152:155], v[40:43]\n"
    "buffer_load_dwordx4 v193, s[20:23], s28 offen offset:1024 nt lds\n"
    "v_mfma_f32_16x16x32_bf16 v[56:59], a[172:175], v[152:155], v[56:59]\n"
    "s_max_u32 s56, s56, s45\n"
    "v_exp_f32 v130, v130\n"
    "v_mfma_f32_16x16x32_bf16 v[72:75], a[176:179], v[152:155], v[72:75]\n"
    "ds_read_b64_tr_b16 a[136:137], v186 offset:49152\n"
    "v_cvt_pk_bf16_f32 v144, v128, v129\n"
    "v_mfma_f32_16x16x32_bf16 v[88:91], a[180:183], v[152:155], v[88:91]\n"
    "v_exp_f32 v131, v131\n"
    "s_cmp_lt_i32 s65, -25\n"
    "s_cselect_b32 s66, 1, 0\n"
    "v_mfma_f32_16x16x32_bf16 v[104:107], a[184:187], v[152:155], v[104:107]\n"
    "buffer_load_dwordx4 v194, s[20:23], s28 offen offset:2048 nt lds\n"
    "v_cvt_pk_bf16_f32 v145, v130, v131\n"
    "v_mfma_f32_16x16x32_bf16 v[120:123], a[188:191], v[152:155], v[120:123]\n"
    "v_exp_f32 v132, v132\n"
    "s_cmp_eq_u32 s31, 7\n"
    "s_cselect_b32 s66, 0, s66\n"
    "v_mfma_f32_16x16x32_bf16 a[200:203], v[176:179], v[152:155], a[200:203]\n"
    "ds_read_b64_tr_b16 a[138:139], v186 offset:53248\n"
    "ds_read_b64_tr_b16 a[140:141], v187 offset:49152\n"
    "v_mfma_f32_16x16x32_bf16 v[136:139], a[64:67], a[16:19], v[164:167]\n"
    "s_and_b32 s67, s67, s66\n"
    "ds_read_b64_tr_b16 a[142:143], v187 offset:53248\n"
    "v_exp_f32 v133, v133\n"
    "v_mfma_f32_16x16x32_bf16 v[136:139], a[68:71], a[20:23], v[136:139]\n"
    "buffer_load_dwordx4 v195, s[20:23], s28 offen offset:3072 nt lds\n"
    "v_cvt_pk_bf16_f32 v146, v132, v133\n"
    "v_mfma_f32_16x16x32_bf16 v[136:139], a[72:75], a[24:27], v[136:139]\n"
    "v_exp_f32 v134, v134\n"
    "s_cmp_lg_u32 s67, 0\n"
    "s_cselect_b32 s56, 160, s56\n"
    "s_cselect_b32 s45, 148, s45\n"
    "v_mfma_f32_16x16x32_bf16 v[136:139], a[76:79], a[28:31], v[136:139]\n"
    "s_add_u32 s28, s28, 16384\n"
    "s_and_b32 s28, s28, 0x7ffff\n"
    "s_add_u32 m0, s30, 0x14000\n"
    "v_mfma_f32_16x16x32_bf16 v[140:143], a[80:83], a[16:19], v[164:167]\n"
    "s_mov_b32 s67, s66\n"
    "v_exp_f32 v135, v135\n"
    "v_mfma_f32_16x16x32_bf16 v[140:143], a[84:87], a[20:23], v[140:143]\n"
    "buffer_load_dwordx4 v196, s[24:27], s29 offen offset:0 nt lds\n"
    "v_mfma_f32_16x16x32_bf16 v[140:143], a[88:91], a[24:27], v[140:143]\n"
    "ds_read_b64_tr_b16 a[144:145], v188 offset:49152\n"
    "ds_read_b64_tr_b16 a[146:147], v188 offset:53248\n"
    "v_mfma_f32_16x16x32_bf16 v[140:143], a[92:95], a[28:31], v[140:143]\n"
    "v_exp_f32 v136, v136\n"
    "v_mfma_f32_16x16x32_bf16 v[12:15], a[160:163], v[156:159], v[12:15]\n"
    "ds_read_b64_tr_b16 a[148:149], v189 offset:49152\n"
    "v_cvt_pk_bf16_f32 v147, v134, v135\n"
    "v_mfma_f32_16x16x32_bf16 v[28:31], a[164:167], v[156:159], v[28:31]\n"
    "v_exp_f32 v137, v137\n"
    "v_mfma_f32_16x16x32_bf16 v[44:47], a[168:171], v[156:159], v[44:47]\n"
    "buffer_load_dwordx4 v197, s[24:27], s29 offen offset:1024 nt lds\n"
    "v_cvt_pk_bf16_f32 v148, v136, v137\n"
    "v_mfma_f32_16x16x32_bf16 v[60:63], a[172:175], v[156:159], v[60:63]\n"
    "v_exp_f32 v138, v138\n"
    "v_mfma_f32_16x16x32_bf16 v[76:79], a[176:179], v[156:159], v[76:79]\n"
    "ds_read_b64_tr_b16 a[150:151], v189 offset:53248\n"
    "ds_read_b64_tr_b16 a[152:153], v190 offset:49152\n"
    "v_mfma_f32_16x16x32_bf16 v[92:95], a[180:183], v[156:159], v[92:95]\n"
    "v_exp_f32 v139, v139\n"
    "v_mfma_f32_16x16x32_bf16 v[108:111], a[184:187], v[156:159], v[108:111]\n"
    "buffer_load_dwordx4 v196, s[24:27], s29 offen offset:2048 nt lds\n"
    "v_cvt_pk_bf16_f32 v149, v138, v139\n"
    "v_mfma_f32_16x16x32_bf16 v[124:127], a[188:191], v[156:159], v[124:127]\n"
    "v_exp_f32 v140, v140\n"
    "v_mfma_f32_16x16x32_bf16 a[204:207], v[176:179], v[156:159], a[204:207]\n"
    "ds_read_b64_tr_b16 a[154:155], v190 offset:53248\n"
    "ds_read_b64_tr_b16 a[156:157], v191 offset:49152\n"
    "v_mfma_f32_16x16x32_bf16 v[128:131], a[64:67], a[32:35], v[168:171]\n"
    "v_exp_f32 v141, v141\n"
    "v_mfma_f32_16x16x32_bf16 v[128:131], a[68:71], a[36:39], v[128:131]\n"
    "buffer_load_dwordx4 v197, s[24:27], s29 offen offset:3072 nt lds\n"
    "v_mfma_f32_16x16x32_bf16 v[128:131], a[72:75], a[40:43], v[128:131]\n"
    "v_exp_f32 v142, v142\n"
    "v_mfma_f32_16x16x32_bf16 v[128:131], a[76:79], a[44:47], v[128:131]\n"
    "s_add_u32 s29, s29, 16384\n"
    "s_and_b32 s29, s29, 0x7ffff\n"
    "ds_read_b64_tr_b16 a[158:159], v191 offset:53248\n"
    "v_mfma_f32_16x16x32_bf16 v[132:135], a[80:83], a[32:35], v[168:171]\n"
    "v_exp_f32 v143, v143\n"
    "v_mfma_f32_16x16x32_bf16 v[132:135], a[84:87], a[36:39], v[132:135]\n"
    "v_cvt_pk_bf16_f32 v150, v140, v141\n"
    "v_cvt_pk_bf16_f32 v151, v142, v143\n"
    "v_mfma_f32_16x16x32_bf16 v[132:135], a[88:91], a[40:43], v[132:135]\n"
    "v_mfma_f32_16x16x32_bf16 v[132:135], a[92:95], a[44:47], v[132:135]\n"
    "s_waitcnt lgkmcnt(0)\n"
    "v_exp_f32 v128, v128\n"
    "v_mfma_f32_16x16x32_bf16 v[0:3], a[128:131], v[144:147], v[0:3]\n"
    "ds_read_b128 a[96:99], v180 offset:57344\n"
    "v_mfma_f32_16x16x32_bf16 v[16:19], a[132:135], v[144:147], v[16:19]\n"
    "v_exp_f32 v129, v129\n"
    "v_mfma_f32_16x16x32_bf16 v[32:35], a[136:139], v[144:147], v[32:35]\n"
    "ds_read_b128 a[100:103], v181 offset:57344\n"
    "v_cvt_pk_bf16_f32 v152, v128, v129\n"
    "v_mfma_f32_16x16x32_bf16 v[48:51], a[140:143], v[144:147], v[48:51]\n"
    "v_exp_f32 v130, v130\n"
    "v_mfma_f32_16x16x32_bf16 v[64:67], a[144:147], v[144:147], v[64:67]\n"
    "ds_read_b128 a[104:107], v182 offset:57344\n"
    "v_mfma_f32_16x16x32_bf16 v[80:83], a[148:151], v[144:147], v[80:83]\n"
    "v_exp_f32 v131, v131\n"
    "v_mfma_f32_16x16x32_bf16 v[96:99], a[152:155], v[144:147], v[96:99]\n"
    "ds_read_b128 a[108:111], v183 offset:57344\n"
    "v_cvt_pk_bf16_f32 v153, v130, v131\n"
    "v_mfma_f32_16x16x32_bf16 v[112:115], a[156:159], v[144:147], v[112:115]\n"
    "v_exp_f32 v132, v132\n"
    "v_mfma_f32_16x16x32_bf16 a[192:195], v[176:179], v[144:147], a[192:195]\n"
    "ds_read_b128 a[112:115], v180 offset:61440\n"
    "v_mfma_f32_16x16x32_bf16 v[136:139], a[64:67], a[48:51], v[172:175]\n"
    "v_exp_f32 v133, v133\n"
    "v_mfma_f32_16x16x32_bf16 v[136:139], a[68:71], a[52:55], v[136:139]\n"
    "ds_read_b128 a[116:119], v181 offset:61440\n"
    "v_cvt_pk_bf16_f32 v154, v132, v133\n"
    "v_mfma_f32_16x16x32_bf16 v[136:139], a[72:75], a[56:59], v[136:139]\n"
    "v_exp_f32 v134, v134\n"
    "v_mfma_f32_16x16x32_bf16 v[136:139], a[76:79], a[60:63], v[136:139]\n"
    "v_mfma_f32_16x16x32_bf16 v[140:143], a[80:83], a[48:51], v[172:175]\n"
    "v_exp_f32 v135, v135\n"
    "v_mfma_f32_16x16x32_bf16 v[140:143], a[84:87], a[52:55], v[140:143]\n"
    "ds_read_b128 a[120:123], v182 offset:61440\n"
    "v_cvt_pk_bf16_f32 v155, v134, v135\n"
    "v_mfma_f32_16x16x32_bf16 v[140:143], a[88:91], a[56:59], v[140:143]\n"
    "ds_read_b128 a[124:127], v183 offset:61440\n"
    "v_mfma_f32_16x16x32_bf16 v[140:143], a[92:95], a[60:63], v[140:143]\n"
    "v_exp_f32 v136, v136\n"
    "v_mfma_f32_16x16x32_bf16 v[4:7], a[128:131], v[148:151], v[4:7]\n"
    "v_mfma_f32_16x16x32_bf16 v[20:23], a[132:135], v[148:151], v[20:23]\n"
    "v_exp_f32 v137, v137\n"
    "v_mfma_f32_16x16x32_bf16 v[36:39], a[136:139], v[148:151], v[36:39]\n"
    "v_cvt_pk_bf16_f32 v156, v136, v137\n"
    "v_mfma_f32_16x16x32_bf16 v[52:55], a[140:143], v[148:151], v[52:55]\n"
    "v_exp_f32 v138, v138\n"
    "v_mfma_f32_16x16x32_bf16 v[68:71], a[144:147], v[148:151], v[68:71]\n"
    "ds_read_b64_tr_b16 a[160:161], v184 offset:57344\n"
    "v_mfma_f32_16x16x32_bf16 v[84:87], a[148:151], v[148:151], v[84:87]\n"
    "v_exp_f32 v139, v139\n"
    "v_mfma_f32_16x16x32_bf16 v[100:103], a[152:155], v[148:151], v[100:103]\n"
    "ds_read_b64_tr_b16 a[162:163], v184 offset:61440\n"
    "v_cvt_pk_bf16_f32 v157, v138, v139\n"
    "v_mfma_f32_16x16x32_bf16 v[116:119], a[156:159], v[148:151], v[116:119]\n"
    "v_exp_f32 v140, v140\n"
    "v_mfma_f32_16x16x32_bf16 a[196:199], v[176:179], v[148:151], a[196:199]\n"
    "s_waitcnt lgkmcnt(1)\n"
    "ds_read_b64_tr_b16 a[164:165], v185 offset:57344\n"
    "v_mfma_f32_16x16x32_bf16 v[128:131], a[96:99], a[0:3], v[160:163]\n"
    "v_exp_f32 v141, v141\n"
    "v_mfma_f32_16x16x32_bf16 v[128:131], a[100:103], a[4:7], v[128:131]\n"
    "v_cvt_pk_bf16_f32 v158, v140, v141\n"
    "v_mfma_f32_16x16x32_bf16 v[128:131], a[104:107], a[8:11], v[128:131]\n"
    "v_exp_f32 v142, v142\n"
    "v_mfma_f32_16x16x32_bf16 v[128:131], a[108:111], a[12:15], v[128:131]\n"
    "ds_read_b64_tr_b16 a[166:167], v185 offset:61440\n"
    "v_mfma_f32_16x16x32_bf16 v[132:135], a[112:115], a[0:3], v[160:163]\n"
    "v_exp_f32 v143, v143\n"
    "v_mfma_f32_16x16x32_bf16 v[132:135], a[116:119], a[4:7], v[132:135]\n"
    "ds_read_b64_tr_b16 a[168:169], v186 offset:57344\n"
    "v_cvt_pk_bf16_f32 v159, v142, v143\n"
    "v_mfma_f32_16x16x32_bf16 v[132:135], a[120:123], a[8:11], v[132:135]\n"
    "v_mfma_f32_16x16x32_bf16 v[132:135], a[124:127], a[12:15], v[132:135]\n"
    "v_exp_f32 v128, v128\n"
    "v_mfma_f32_16x16x32_bf16 v[8:11], a[128:131], v[152:155], v[8:11]\n"
    "ds_read_b64_tr_b16 a[170:171], v186 offset:61440\n"
    "v_mfma_f32_16x16x32_bf16 v[24:27], a[132:135], v[152:155], v[24:27]\n"
    "v_exp_f32 v129, v129\n"
    "v_mfma_f32_16x16x32_bf16 v[40:43], a[136:139], v[152:155], v[40:43]\n"
    "ds_read_b64_tr_b16 a[172:173], v187 offset:57344\n"
    "v_cvt_pk_bf16_f32 v144, v128, v129\n"
    "v_mfma_f32_16x16x32_bf16 v[56:59], a[140:143], v[152:155], v[56:59]\n"
    "v_exp_f32 v130, v130\n"
    "v_mfma_f32_16x16x32_bf16 v[72:75], a[144:147], v[152:155], v[72:75]\n"
    "ds_read_b64_tr_b16 a[174:175], v187 offset:61440\n"
    "v_mfma_f32_16x16x32_bf16 v[88:91], a[148:151], v[152:155], v[88:91]\n"
    "v_exp_f32 v131, v131\n"
    "v_mfma_f32_16x16x32_bf16 v[104:107], a[152:155], v[152:155], v[104:107]\n"
    "ds_read_b64_tr_b16 a[176:177], v188 offset:57344\n"
    "v_cvt_pk_bf16_f32 v145, v130, v131\n"
    "v_mfma_f32_16x16x32_bf16 v[120:123], a[156:159], v[152:155], v[120:123]\n"
    "v_exp_f32 v132, v132\n"
    "v_mfma_f32_16x16x32_bf16 a[200:203], v[176:179], v[152:155], a[200:203]\n"
    "ds_read_b64_tr_b16 a[178:179], v188 offset:61440\n"
    "v_mfma_f32_16x16x32_bf16 v[136:139], a[96:99], a[16:19], v[164:167]\n"
    "v_exp_f32 v133, v133\n"
    "v_mfma_f32_16x16x32_bf16 v[136:139], a[100:103], a[20:23], v[136:139]\n"
    "v_cvt_pk_bf16_f32 v146, v132, v133\n"
    "v_mfma_f32_16x16x32_bf16 v[136:139], a[104:107], a[24:27], v[136:139]\n"
    "v_exp_f32 v134, v134\n"
    "v_mfma_f32_16x16x32_bf16 v[136:139], a[108:111], a[28:31], v[136:139]\n"
    "ds_read_b64_tr_b16 a[180:181], v189 offset:57344\n"
    "v_mfma_f32_16x16x32_bf16 v[140:143], a[112:115], a[16:19], v[164:167]\n"
    "v_exp_f32 v135, v135\n"
    "v_mfma_f32_16x16x32_bf16 v[140:143], a[116:119], a[20:23], v[140:143]\n"
    "ds_read_b64_tr_b16 a[182:183], v189 offset:61440\n"
    "v_cvt_pk_bf16_f32 v147, v134, v135\n"
    "v_mfma_f32_16x16x32_bf16 v[140:143], a[120:123], a[24:27], v[140:143]\n"
    "v_mfma_f32_16x16x32_bf16 v[140:143], a[124:127], a[28:31], v[140:143]\n"
    "v_exp_f32 v136, v136\n"
    "v_mfma_f32_16x16x32_bf16 v[12:15], a[128:131], v[156:159], v[12:15]\n"
    "ds_read_b64_tr_b16 a[184:185], v190 offset:57344\n"
    "v_mfma_f32_16x16x32_bf16 v[28:31], a[132:135], v[156:159], v[28:31]\n"
    "v_exp_f32 v137, v137\n"
    "v_mfma_f32_16x16x32_bf16 v[44:47], a[136:139], v[156:159], v[44:47]\n"
    "ds_read_b64_tr_b16 a[186:187], v190 offset:61440\n"
    "v_cvt_pk_bf16_f32 v148, v136, v137\n"
    "v_mfma_f32_16x16x32_bf16 v[60:63], a[140:143], v[156:159], v[60:63]\n"
    "v_exp_f32 v138, v138\n"
    "v_mfma_f32_16x16x32_bf16 v[76:79], a[144:147], v[156:159], v[76:79]\n"
    "ds_read_b64_tr_b16 a[188:189], v191 offset:57344\n"
    "v_mfma_f32_16x16x32_bf16 v[92:95], a[148:151], v[156:159], v[92:95]\n"
    "v_exp_f32 v139, v139\n"
    "v_mfma_f32_16x16x32_bf16 v[108:111], a[152:155], v[156:159], v[108:111]\n"
    "ds_read_b64_tr_b16 a[190:191], v191 offset:61440\n"
    "v_cvt_pk_bf16_f32 v149, v138, v139\n"
    "v_mfma_f32_16x16x32_bf16 v[124:127], a[156:159], v[156:159], v[124:127]\n"
    "v_exp_f32 v140, v140\n"
    "v_mfma_f32_16x16x32_bf16 a[204:207], v[176:179], v[156:159], a[204:207]\n"
    "v_mfma_f32_16x16x32_bf16 v[128:131], a[96:99], a[32:35], v[168:171]\n"
    "v_exp_f32 v141, v141\n"
    "v_mfma_f32_16x16x32_bf16 v[128:131], a[100:103], a[36:39], v[128:131]\n"
    "v_cvt_pk_bf16_f32 v150, v140, v141\n"
    "v_mfma_f32_16x16x32_bf16 v[128:131], a[104:107], a[40:43], v[128:131]\n"
    "v_exp_f32 v142, v142\n"
    "v_mfma_f32_16x16x32_bf16 v[128:131], a[108:111], a[44:47], v[128:131]\n"
    "ds_read_b128 a[64:67], v180 offset:0\n"
    "v_mfma_f32_16x16x32_bf16 v[132:135], a[112:115], a[32:35], v[168:171]\n"
    "v_exp_f32 v143, v143\n"
    "v_mfma_f32_16x16x32_bf16 v[132:135], a[116:119], a[36:39], v[132:135]\n"
    "ds_read_b128 a[68:71], v181 offset:0\n"
    "v_cvt_pk_bf16_f32 v151, v142, v143\n"
    "v_mfma_f32_16x16x32_bf16 v[132:135], a[120:123], a[40:43], v[132:135]\n"
    "v_mfma_f32_16x16x32_bf16 v[132:135], a[124:127], a[44:47], v[132:135]\n"
    "s_waitcnt lgkmcnt(1)\n"
    "v_exp_f32 v128, v128\n"
    "v_mfma_f32_16x16x32_bf16 v[0:3], a[160:163], v[144:147], v[0:3]\n"
    "ds_read_b128 a[72:75], v182 offset:0\n"
    "v_mfma_f32_16x16x32_bf16 v[16:19], a[164:167], v[144:147], v[16:19]\n"
    "v_exp_f32 v129, v129\n"
    "v_mfma_f32_16x16x32_bf16 v[32:35], a[168:171], v[144:147], v[32:35]\n"
    "v_cvt_pk_bf16_f32 v152, v128, v129\n"
    "v_mfma_f32_16x16x32_bf16 v[48:51], a[172:175], v[144:147], v[48:51]\n"
    "v_exp_f32 v130, v130\n"
    "v_mfma_f32_16x16x32_bf16 v[64:67], a[176:179], v[144:147], v[64:67]\n"
    "ds_read_b128 a[76:79], v183 offset:0\n"
    "v_mfma_f32_16x16x32_bf16 v[80:83], a[180:183], v[144:147], v[80:83]\n"
    "v_exp_f32 v131, v131\n"
    "v_mfma_f32_16x16x32_bf16 v[96:99], a[184:187], v[144:147], v[96:99]\n"
    "ds_read_b128 a[80:83], v180 offset:4096\n"
    "v_cvt_pk_bf16_f32 v153, v130, v131\n"
    "v_mfma_f32_16x16x32_bf16 v[112:115], a[188:191], v[144:147], v[112:115]\n"
    "v_exp_f32 v132, v132\n"
    "v_mfma_f32_16x16x32_bf16 a[192:195], v[176:179], v[144:147], a[192:195]\n"
    "s_memrealtime s[64:65]\n"
    "v_mfma_f32_16x16x32_bf16 v[136:139], a[96:99], a[48:51], v[172:175]\n"
    "v_exp_f32 v133, v133\n"
    "v_mfma_f32_16x16x32_bf16 v[136:139], a[100:103], a[52:55], v[136:139]\n"
    "ds_read_b128 a[84:87], v181 offset:4096\n"
    "v_cvt_pk_bf16_f32 v154, v132, v133\n"
    "v_mfma_f32_16x16x32_bf16 v[136:139], a[104:107], a[56:59], v[136:139]\n"
    "v_exp_f32 v134, v134\n"
    "v_mfma_f32_16x16x32_bf16 v[136:139], a[108:111], a[60:63], v[136:139]\n"
    "ds_read_b128 a[88:91], v182 offset:4096\n"
    "v_mfma_f32_16x16x32_bf16 v[140:143], a[112:115], a[48:51], v[172:175]\n"
    "v_exp_f32 v135, v135\n"
    "v_mfma_f32_16x16x32_bf16 v[140:143], a[116:119], a[52:55], v[140:143]\n"
    "v_cvt_pk_bf16_f32 v155, v134, v135\n"
    "v_mfma_f32_16x16x32_bf16 v[140:143], a[120:123], a[56:59], v[140:143]\n"
    "ds_read_b128 a[92:95], v183 offset:4096\n"
    "v_mfma_f32_16x16x32_bf16 v[140:143], a[124:127], a[60:63], v[140:143]\n"
    "v_exp_f32 v136, v136\n"
    "v_mfma_f32_16x16x32_bf16 v[4:7], a[160:163], v[148:151], v[4:7]\n"
    "v_mfma_f32_16x16x32_bf16 v[20:23], a[164:167], v[148:151], v[20:23]\n"
    "v_exp_f32 v137, v137\n"
    "v_mfma_f32_16x16x32_bf16 v[36:39], a[168:171], v[148:151], v[36:39]\n"
    "v_cvt_pk_bf16_f32 v156, v136, v137\n"
    "v_mfma_f32_16x16x32_bf16 v[52:55], a[172:175], v[148:151], v[52:55]\n"
    "v_exp_f32 v138, v138\n"
    "v_mfma_f32_16x16x32_bf16 v[68:71], a[176:179], v[148:151], v[68:71]\n"
    "v_mfma_f32_16x16x32_bf16 v[84:87], a[180:183], v[148:151], v[84:87]\n"
    "v_exp_f32 v139, v139\n"
    "v_mfma_f32_16x16x32_bf16 v[100:103], a[184:187], v[148:151], v[100:103]\n"
    "v_cvt_pk_bf16_f32 v157, v138, v139\n"
    "v_mfma_f32_16x16x32_bf16 v[116:119], a[188:191], v[148:151], v[116:119]\n"
    "v_exp_f32 v140, v140\n"
    "v_mfma_f32_16x16x32_bf16 a[196:199], v[176:179], v[148:151], a[196:199]\n"
    "v_mfma_f32_16x16x32_bf16 v[128:131], a[64:67], a[0:3], v[160:163]\n"
    "s_waitcnt vmcnt(8)\n"
    "s_waitcnt lgkmcnt(0)\n"
    "v_exp_f32 v141, v141\n"
    "v_mfma_f32_16x16x32_bf16 v[128:131], a[68:71], a[4:7], v[128:131]\n"
    "s_barrier\n"
    "v_cvt_pk_bf16_f32 v158, v140, v141\n"
    "v_mfma_f32_16x16x32_bf16 v[128:131], a[72:75], a[8:11], v[128:131]\n"
    "s_add_u32 s59, s59, s56\n"
    "global_load_dwordx2 v[248:249], v238, s[32:33] sc1\n"
    "v_exp_f32 v142, v142\n"
    "v_mfma_f32_16x16x32_bf16 v[128:131], a[76:79], a[12:15], v[128:131]\n"
    "s_sub_u32 s65, s59, s64\n"
    "s_add_u32 m0, s30, 0xc000\n"
    "ds_read_b64_tr_b16 a[128:129], v184 offset:0\n"
    "v_mfma_f32_16x16x32_bf16 v[132:135], a[80:83], a[0:3], v[160:163]\n"
    "v_exp_f32 v143, v143\n"
    "s_cmp_gt_i32 s65, 0\n"
    "s_cselect_b32 s59, s59, s64\n"
    "s_cbranch_scc1 PC3%=\n"
    "PC3D%=:\n"
    "v_mfma_f32_16x16x32_bf16 v[132:135], a[84:87], a[4:7], v[132:135]\n"
    "buffer_load_dwordx4 v192, s[20:23], s28 offen offset:0 nt lds\n"
    "v_mfma_f32_16x16x32_bf16 v[132:135], a[88:91], a[8:11], v[132:135]\n"
    "ds_read_b64_tr_b16 a[130:131], v184 offset:4096\n"
    "ds_read_b64_tr_b16 a[132:133], v185 offset:0\n"
    "v_mfma_f32_16x16x32_bf16 v[132:135], a[92:95], a[12:15], v[132:135]\n"
    "v_exp_f32 v128, v128\n"
    "s_cmp_gt_u32 s56, 148\n"
    "s_cselect_b32 s42, 2, 1\n"
    "v_mfma_f32_16x16x32_bf16 v[8:11], a[160:163], v[152:155], v[8:11]\n"
    "ds_read_b64_tr_b16 a[134:135], v185 offset:4096\n"
    "v_cvt_pk_bf16_f32 v159, v142, v143\n"
    "v_mfma_f32_16x16x32_bf16 v[24:27], a[164:167], v[152:155], v[24:27]\n"
    "s_sub_u32 s56, s56, s42\n"
    "v_exp_f32 v129, v129\n"
    "v_mfma_f32_16x16x32_bf16 v[40:43], a[168:171], v[152:155], v[40:43]\n"
    "buffer_load_dwordx4 v193, s[20:23], s28 offen offset:1024 nt lds\n"
    "v_mfma_f32_16x16x32_bf16 v[56:59], a[172:175], v[152:155], v[56:59]\n"
    "s_max_u32 s56, s56, s45\n"
    "v_exp_f32 v130, v130\n"
    "v_mfma_f32_16x16x32_bf16 v[72:75], a[176:179], v[152:155], v[72:75]\n"
    "ds_read_b64_tr_b16 a[136:137], v186 offset:0\n"
    "v_cvt_pk_bf16_f32 v144, v128, v129\n"
    "v_mfma_f32_16x16x32_bf16 v[88:91], a[180:183], v[152:155], v[88:91]\n"
    "v_exp_f32 v131, v131\n"
    "s_cmp_lt_i32 s65, -25\n"
    "s_cselect_b32 s66, 1, 0\n"
    "v_mfma_f32_16x16x32_bf16 v[104:107], a[184:187], v[152:155], v[104:107]\n"
    "buffer_load_dwordx4 v194, s[20:23], s28 offen offset:2048 nt lds\n"
    "v_cvt_pk_bf16_f32 v145, v130, v131\n"
    "v_mfma_f32_16x16x32_bf16 v[120:123], a[188:191], v[152:155], v[120:123]\n"
    "v_exp_f32 v132, v132\n"
    "s_cmp_eq_u32 s31, 7\n"
    "s_cselect_b32 s66, 0, s66\n"
    "v_mfma_f32_16x16x32_bf16 a[200:203], v[176:179], v[152:155], a[200:203]\n"
    "ds_read_b64_tr_b16 a[138:139], v186 offset:4096\n"
    "ds_read_b64_tr_b16 a[140:141], v187 offset:0\n"
    "v_mfma_f32_16x16x32_bf16 v[136:139], a[64:67], a[16:19], v[164:167]\n"
    "s_and_b32 s67, s67, s66\n"
    "ds_read_b64_tr_b16 a[142:143], v187 offset:4096\n"
    "v_exp_f32 v133, v133\n"
    "v_mfma_f32_16x16x32_bf16 v[136:139], a[68:71], a[20:23], v[136:139]\n"
    "buffer_load_dwordx4 v195, s[20:23], s28 offen offset:3072 nt lds\n"
    "v_cvt_pk_bf16_f32 v146, v132, v133\n"
    "v_mfma_f32_16x16x32_bf16 v[136:139], a[72:75], a[24:27], v[136:139]\n"
    "v_exp_f32 v134, v134\n"
    "s_cmp_lg_u32 s67, 0\n"
    "s_cselect_b32 s56, 160, s56\n"
    "s_cselect_b32 s45, 148, s45\n"
    "v_mfma_f32_16x16x32_bf16 v[136:139], a[76:79], a[28:31], v[136:139]\n"
    "s_add_u32 s28, s28, 16384\n"
    "s_and_b32 s28, s28, 0x7ffff\n"
    "s_add_u32 m0, s30, 0x18000\n"
    "v_mfma_f32_16x16x32_bf16 v[140:143], a[80:83], a[16:19], v[164:167]\n"
    "s_mov_b32 s67, s66\n"
    "v_exp_f32 v135, v135\n"
    "v_mfma_f32_16x16x32_bf16 v[140:143], a[84:87], a[20:23], v[140:143]\n"
    "buffer_load_dwordx4 v196, s[24:27], s29 offen offset:0 nt lds\n"
    "v_mfma_f32_16x16x32_bf16 v[140:143], a[88:91], a[24:27], v[140:143]\n"
    "ds_read_b64_tr_b16 a[144:145], v188 offset:0\n"
    "ds_read_b64_tr_b16 a[146:147], v188 offset:4096\n"
    "v_mfma_f32_16x16x32_bf16 v[140:143], a[92:95], a[28:31], v[140:143]\n"
    "v_exp_f32 v136, v136\n"
    "v_mfma_f32_16x16x32_bf16 v[12:15], a[160:163], v[156:159], v[12:15]\n"
    "ds_read_b64_tr_b16 a[148:149], v189 offset:0\n"
    "v_cvt_pk_bf16_f32 v147, v134, v135\n"
    "v_mfma_f32_16x16x32_bf16 v[28:31], a[164:167], v[156:159], v[28:31]\n"
    "v_exp_f32 v137, v137\n"
    "v_mfma_f32_16x16x32_bf16 v[44:47], a[168:171], v[156:159], v[44:47]\n"
    "buffer_load_dwordx4 v197, s[24:27], s29 offen offset:1024 nt lds\n"
    "v_cvt_pk_bf16_f32 v148, v136, v137\n"
    "v_mfma_f32_16x16x32_bf16 v[60:63], a[172:175], v[156:159], v[60:63]\n"
    "v_exp_f32 v138, v138\n"
    "v_mfma_f32_16x16x32_bf16 v[76:79], a[176:179], v[156:159], v[76:79]\n"
    "ds_read_b64_tr_b16 a[150:151], v189 offset:4096\n"
    "ds_read_b64_tr_b16 a[152:153], v190 offset:0\n"
    "v_mfma_f32_16x16x32_bf16 v[92:95], a[180:183], v[156:159], v[92:95]\n"
    "v_exp_f32 v139, v139\n"
    "v_mfma_f32_16x16x32_bf16 v[108:111], a[184:187], v[156:159], v[108:111]\n"
    "buffer_load_dwordx4 v196, s[24:27], s29 offen offset:2048 nt lds\n"
    "v_cvt_pk_bf16_f32 v149, v138, v139\n"
    "v_mfma_f32_16x16x32_bf16 v[124:127], a[188:191], v[156:159], v[124:127]\n"
    "v_exp_f32 v140, v140\n"
    "v_mfma_f32_16x16x32_bf16 a[204:207], v[176:179], v[156:159], a[204:207]\n"
    "ds_read_b64_tr_b16 a[154:155], v190 offset:4096\n"
    "ds_read_b64_tr_b16 a[156:157], v191 offset:0\n"
    "v_mfma_f32_16x16x32_bf16 v[128:131], a[64:67], a[32:35], v[168:171]\n"
    "v_exp_f32 v141, v141\n"
    "v_mfma_f32_16x16x32_bf16 v[128:131], a[68:71], a[36:39], v[128:131]\n"
    "buffer_load_dwordx4 v197, s[24:27], s29 offen offset:3072 nt lds\n"
    "v_mfma_f32_16x16x32_bf16 v[128:131], a[72:75], a[40:43], v[128:131]\n"
    "v_exp_f32 v142, v142\n"
    "v_mfma_f32_16x16x32_bf16 v[128:131], a[76:79], a[44:47], v[128:131]\n"
    "s_add_u32 s29, s29, 16384\n"
    "s_and_b32 s29, s29, 0x7ffff\n"
    "ds_read_b64_tr_b16 a[158:159], v191 offset:4096\n"
    "v_mfma_f32_16x16x32_bf16 v[132:135], a[80:83], a[32:35], v[168:171]\n"
    "v_exp_f32 v143, v143\n"
    "v_mfma_f32_16x16x32_bf16 v[132:135], a[84:87], a[36:39], v[132:135]\n"
    "v_cvt_pk_bf16_f32 v150, v140, v141\n"
    "v_cvt_pk_bf16_f32 v151, v142, v143\n"
    "v_mfma_f32_16x16x32_bf16 v[132:135], a[88:91], a[40:43], v[132:135]\n"
    "v_mfma_f32_16x16x32_bf16 v[132:135], a[92:95], a[44:47], v[132:135]\n"
    "s_waitcnt lgkmcnt(0)\n"
    "v_exp_f32 v128, v128\n"
    "v_mfma_f32_16x16x32_bf16 v[0:3], a[128:131], v[144:147], v[0:3]\n"
    "ds_read_b128 a[96:99], v180 offset:8192\n"
    "v_mfma_f32_16x16x32_bf16 v[16:19], a[132:135], v[144:147], v[16:19]\n"
    "v_exp_f32 v129, v129\n"
    "v_mfma_f32_16x16x32_bf16 v[32:35], a[136:139], v[144:147], v[32:35]\n"
    "ds_read_b128 a[100:103], v181 offset:8192\n"
    "v_cvt_pk_bf16_f32 v152, v128, v129\n"
    "v_mfma_f32_16x16x32_bf16 v[48:51], a[140:143], v[144:147], v[48:51]\n"
    "v_exp_f32 v130, v130\n"
    "v_mfma_f32_16x16x32_bf16 v[64:67], a[144:147], v[144:147], v[64:67]\n"
    "ds_read_b128 a[104:107], v182 offset:8192\n"
    "v_mfma_f32_16x16x32_bf16 v[80:83], a[148:151], v[144:147], v[80:83]\n"
    "v_exp_f32 v131, v131\n"
    "v_mfma_f32_16x16x32_bf16 v[96:99], a[152:155], v[144:147], v[96:99]\n"
    "ds_read_b128 a[108:111], v183 offset:8192\n"
    "v_cvt_pk_bf16_f32 v153, v130, v131\n"
    "v_mfma_f32_16x16x32_bf16 v[112:115], a[156:159], v[144:147], v[112:115]\n"
    "v_exp_f32 v132, v132\n"
    "v_mfma_f32_16x16x32_bf16 a[192:195], v[176:179], v[144:147], a[192:195]\n"
    "ds_read_b128 a[112:115], v180 offset:12288\n"
    "v_mfma_f32_16x16x32_bf16 v[136:139], a[64:67], a[48:51], v[172:175]\n"
    "v_exp_f32 v133, v133\n"
    "v_mfma_f32_16x16x32_bf16 v[136:139], a[68:71], a[52:55], v[136:139]\n"
    "ds_read_b128 a[116:119], v181 offset:12288\n"
    "v_cvt_pk_bf16_f32 v154, v132, v133\n"
    "v_mfma_f32_16x16x32_bf16 v[136:139], a[72:75], a[56:59], v[136:139]\n"
    "v_exp_f32 v134, v134\n"
    "v_mfma_f32_16x16x32_bf16 v[136:139], a[76:79], a[60:63], v[136:139]\n"
    "v_mfma_f32_16x16x32_bf16 v[140:143], a[80:83], a[48:51], v[172:175]\n"
    "v_exp_f32 v135, v135\n"
    "v_mfma_f32_16x16x32_bf16 v[140:143], a[84:87], a[52:55], v[140:143]\n"
    "ds_read_b128 a[120:123], v182 offset:12288\n"
    "v_cvt_pk_bf16_f32 v155, v134, v135\n"
    "v_mfma_f32_16x16x32_bf16 v[140:143], a[88:91], a[56:59], v[140:143]\n"
    "ds_read_b128 a[124:127], v183 offset:12288\n"
    "v_mfma_f32_16x16x32_bf16 v[140:143], a[92:95], a[60:63], v[140:143]\n"
    "v_exp_f32 v136, v136\n"
    "v_mfma_f32_16x16x32_bf16 v[4:7], a[128:131], v[148:151], v[4:7]\n"
    "v_mfma_f32_16x16x32_bf16 v[20:23], a[132:135], v[148:151], v[20:23]\n"
    "v_exp_f32 v137, v137\n"
    "v_mfma_f32_16x16x32_bf16 v[36:39], a[136:139], v[148:151], v[36:39]\n"
    "v_cvt_pk_bf16_f32 v156, v136, v137\n"
    "v_mfma_f32_16x16x32_bf16 v[52:55], a[140:143], v[148:151], v[52:55]\n"
    "v_exp_f32 v138, v138\n"
    "v_mfma_f32_16x16x32_bf16 v[68:71], a[144:147], v[148:151], v[68:71]\n"
    "ds_read_b64_tr_b16 a[160:161], v184 offset:8192\n"
    "v_mfma_f32_16x16x32_bf16 v[84:87], a[148:151], v[148:151], v[84:87]\n"
    "v_exp_f32 v139, v139\n"
    "v_mfma_f32_16x16x32_bf16 v[100:103], a[152:155], v[148:151], v[100:103]\n"
    "ds_read_b64_tr_b16 a[162:163], v184 offset:12288\n"
    "v_cvt_pk_bf16_f32 v157, v138, v139\n"
    "v_mfma_f32_16x16x32_bf16 v[116:119], a[156:159], v[148:151], v[116:119]\n"
    "v_exp_f32 v140, v140\n"
    "v_mfma_f32_16x16x32_bf16 a[196:199], v[176:179], v[148:151], a[196:199]\n"
    "s_waitcnt lgkmcnt(1)\n"
    "ds_read_b64_tr_b16 a[164:165], v185 offset:8192\n"
    "v_mfma_f32_16x16x32_bf16 v[128:131], a[96:99], a[0:3], v[160:163]\n"
    "v_exp_f32 v141, v141\n"
    "v_mfma_f32_16x16x32_bf16 v[128:131], a[100:103], a[4:7], v[128:131]\n"
    "v_cvt_pk_bf16_f32 v158, v140, v141\n"
    "v_mfma_f32_16x16x32_bf16 v[128:131], a[104:107], a[8:11], v[128:131]\n"
    "v_exp_f32 v142, v142\n"
    "v_mfma_f32_16x16x32_bf16 v[128:131], a[108:111], a[12:15], v[128:131]\n"
    "ds_read_b64_tr_b16 a[166:167], v185 offset:12288\n"
    "v_mfma_f32_16x16x32_bf16 v[132:135], a[112:115], a[0:3], v[160:163]\n"
    "v_exp_f32 v143, v143\n"
    "v_mfma_f32_16x16x32_bf16 v[132:135], a[116:119], a[4:7], v[132:135]\n"
    "ds_read_b64_tr_b16 a[168:169], v186 offset:8192\n"
    "v_cvt_pk_bf16_f32 v159, v142, v143\n"
    "v_mfma_f32_16x16x32_bf16 v[132:135], a[120:123], a[8:11], v[132:135]\n"
    "v_mfma_f32_16x16x32_bf16 v[132:135], a[124:127], a[12:15], v[132:135]\n"
    "v_exp_f32 v128, v128\n"
    "v_mfma_f32_16x16x32_bf16 v[8:11], a[128:131], v[152:155], v[8:11]\n"
    "ds_read_b64_tr_b16 a[170:171], v186 offset:12288\n"
    "v_mfma_f32_16x16x32_bf16 v[24:27], a[132:135], v[152:155], v[24:27]\n"
    "v_exp_f32 v129, v129\n"
    "v_mfma_f32_16x16x32_bf16 v[40:43], a[136:139], v[152:155], v[40:43]\n"
    "ds_read_b64_tr_b16 a[172:173], v187 offset:8192\n"
    "v_cvt_pk_bf16_f32 v144, v128, v129\n"
    "v_mfma_f32_16x16x32_bf16 v[56:59], a[140:143], v[152:155], v[56:59]\n"
    "v_exp_f32 v130, v130\n"
    "v_mfma_f32_16x16x32_bf16 v[72:75], a[144:147], v[152:155], v[72:75]\n"
    "ds_read_b64_tr_b16 a[174:175], v187 offset:12288\n"
    "v_mfma_f32_16x16x32_bf16 v[88:91], a[148:151], v[152:155], v[88:91]\n"
    "v_exp_f32 v131, v131\n"
    "v_mfma_f32_16x16x32_bf16 v[104:107], a[152:155], v[152:155], v[104:107]\n"
    "ds_read_b64_tr_b16 a[176:177], v188 offset:8192\n"
    "v_cvt_pk_bf16_f32 v145, v130, v131\n"
    "v_mfma_f32_16x16x32_bf16 v[120:123], a[156:159], v[152:155], v[120:123]\n"
    "v_exp_f32 v132, v132\n"
    "v_mfma_f32_16x16x32_bf16 a[200:203], v[176:179], v[152:155], a[200:203]\n"
    "ds_read_b64_tr_b16 a[178:179], v188 offset:12288\n"
    "v_mfma_f32_16x16x32_bf16 v[136:139], a[96:99], a[16:19], v[164:167]\n"
    "v_exp_f32 v133, v133\n"
    "v_mfma_f32_16x16x32_bf16 v[136:139], a[100:103], a[20:23], v[136:139]\n"
    "v_cvt_pk_bf16_f32 v146, v132, v133\n"
    "v_mfma_f32_16x16x32_bf16 v[136:139], a[104:107], a[24:27], v[136:139]\n"
    "v_exp_f32 v134, v134\n"
    "v_mfma_f32_16x16x32_bf16 v[136:139], a[108:111], a[28:31], v[136:139]\n"
    "ds_read_b64_tr_b16 a[180:181], v189 offset:8192\n"
    "v_mfma_f32_16x16x32_bf16 v[140:143], a[112:115], a[16:19], v[164:167]\n"
    "v_exp_f32 v135, v135\n"
    "v_mfma_f32_16x16x32_bf16 v[140:143], a[116:119], a[20:23], v[140:143]\n"
    "ds_read_b64_tr_b16 a[182:183], v189 offset:12288\n"
    "v_cvt_pk_bf16_f32 v147, v134, v135\n"
    "v_mfma_f32_16x16x32_bf16 v[140:143], a[120:123], a[24:27], v[140:143]\n"
    "v_mfma_f32_16x16x32_bf16 v[140:143], a[124:127], a[28:31], v[140:143]\n"
    "v_exp_f32 v136, v136\n"
    "v_mfma_f32_16x16x32_bf16 v[12:15], a[128:131], v[156:159], v[12:15]\n"
    "ds_read_b64_tr_b16 a[184:185], v190 offset:8192\n"
    "v_mfma_f32_16x16x32_bf16 v[28:31], a[132:135], v[156:159], v[28:31]\n"
    "v_exp_f32 v137, v137\n"
    "v_mfma_f32_16x16x32_bf16 v[44:47], a[136:139], v[156:159], v[44:47]\n"
    "ds_read_b64_tr_b16 a[186:187], v190 offset:12288\n"
    "v_cvt_pk_bf16_f32 v148, v136, v137\n"
    "v_mfma_f32_16x16x32_bf16 v[60:63], a[140:143], v[156:159], v[60:63]\n"
    "v_exp_f32 v138, v138\n"
    "v_mfma_f32_16x16x32_bf16 v[76:79], a[144:147], v[156:159], v[76:79]\n"
    "ds_read_b64_tr_b16 a[188:189], v191 offset:8192\n"
    "v_mfma_f32_16x16x32_bf16 v[92:95], a[148:151], v[156:159], v[92:95]\n"
    "v_exp_f32 v139, v139\n"
    "v_mfma_f32_16x16x32_bf16 v[108:111], a[152:155], v[156:159], v[108:111]\n"
    "ds_read_b64_tr_b16 a[190:191], v191 offset:12288\n"
    "v_cvt_pk_bf16_f32 v149, v138, v139\n"
    "v_mfma_f32_16x16x32_bf16 v[124:127], a[156:159], v[156:159], v[124:127]\n"
    "v_exp_f32 v140, v140\n"
    "v_mfma_f32_16x16x32_bf16 a[204:207], v[176:179], v[156:159], a[204:207]\n"
    "v_mfma_f32_16x16x32_bf16 v[128:131], a[96:99], a[32:35], v[168:171]\n"
    "v_exp_f32 v141, v141\n"
    "v_mfma_f32_16x16x32_bf16 v[128:131], a[100:103], a[36:39], v[128:131]\n"
    "v_cvt_pk_bf16_f32 v150, v140, v141\n"
    "v_mfma_f32_16x16x32_bf16 v[128:131], a[104:107], a[40:43], v[128:131]\n"
    "v_exp_f32 v142, v142\n"
    "v_mfma_f32_16x16x32_bf16 v[128:131], a[108:111], a[44:47], v[128:131]\n"
    "ds_read_b128 a[64:67], v180 offset:16384\n"
    "v_mfma_f32_16x16x32_bf16 v[132:135], a[112:115], a[32:35], v[168:171]\n"
    "v_exp_f32 v143, v143\n"
    "v_mfma_f32_16x16x32_bf16 v[132:135], a[116:119], a[36:39], v[132:135]\n"
    "ds_read_b128 a[68:71], v181 offset:16384\n"
    "v_cvt_pk_bf16_f32 v151, v142, v143\n"
    "v_mfma_f32_16x16x32_bf16 v[132:135], a[120:123], a[40:43], v[132:135]\n"
    "v_mfma_f32_16x16x32_bf16 v[132:135], a[124:127], a[44:47], v[132:135]\n"
    "s_waitcnt lgkmcnt(1)\n"
    "v_exp_f32 v128, v128\n"
    "v_mfma_f32_16x16x32_bf16 v[0:3], a[160:163], v[144:147], v[0:3]\n"
    "ds_read_b128 a[72:75], v182 offset:16384\n"
    "v_mfma_f32_16x16x32_bf16 v[16:19], a[164:167], v[144:147], v[16:19]\n"
    "v_exp_f32 v129, v129\n"
    "v_mfma_f32_16x16x32_bf16 v[32:35], a[168:171], v[144:147], v[32:35]\n"
    "v_cvt_pk_bf16_f32 v152, v128, v129\n"
    "v_mfma_f32_16x16x32_bf16 v[48:51], a[172:175], v[144:147], v[48:51]\n"
    "v_exp_f32 v130, v130\n"
    "v_mfma_f32_16x16x32_bf16 v[64:67], a[176:179], v[144:147], v[64:67]\n"
    "ds_read_b128 a[76:79], v183 offset:16384\n"
    "v_mfma_f32_16x16x32_bf16 v[80:83], a[180:183], v[144:147], v[80:83]\n"
    "v_exp_f32 v131, v131\n"
    "v_mfma_f32_16x16x32_bf16 v[96:99], a[184:187], v[144:147], v[96:99]\n"
    "ds_read_b128 a[80:83], v180 offset:20480\n"
    "v_cvt_pk_bf16_f32 v153, v130, v131\n"
    "v_mfma_f32_16x16x32_bf16 v[112:115], a[188:191], v[144:147], v[112:115]\n"
    "v_exp_f32 v132, v132\n"
    "v_mfma_f32_16x16x32_bf16 a[192:195], v[176:179], v[144:147], a[192:195]\n"
    "s_memrealtime s[64:65]\n"
    "v_mfma_f32_16x16x32_bf16 v[136:139], a[96:99], a[48:51], v[172:175]\n"
    "v_exp_f32 v133, v133\n"
    "v_mfma_f32_16x16x32_bf16 v[136:139], a[100:103], a[52:55], v[136:139]\n"
    "ds_read_b128 a[84:87], v181 offset:20480\n"
    "v_cvt_pk_bf16_f32 v154, v132, v133\n"
    "v_mfma_f32_16x16x32_bf16 v[136:139], a[104:107], a[56:59], v[136:139]\n"
    "v_exp_f32 v134, v134\n"
    "v_mfma_f32_16x16x32_bf16 v[136:139], a[108:111], a[60:63], v[136:139]\n"
    "ds_read_b128 a[88:91], v182 offset:20480\n"
    "v_mfma_f32_16x16x32_bf16 v[140:143], a[112:115], a[48:51], v[172:175]\n"
    "v_exp_f32 v135, v135\n"
    "v_mfma_f32_16x16x32_bf16 v[140:143], a[116:119], a[52:55], v[140:143]\n"
    "v_cvt_pk_bf16_f32 v155, v134, v135\n"
    "v_mfma_f32_16x16x32_bf16 v[140:143], a[120:123], a[56:59], v[140:143]\n"
    "ds_read_b128 a[92:95], v183 offset:20480\n"
    "v_mfma_f32_16x16x32_bf16 v[140:143], a[124:127], a[60:63], v[140:143]\n"
    "v_exp_f32 v136, v136\n"
    "v_mfma_f32_16x16x32_bf16 v[4:7], a[160:163], v[148:151], v[4:7]\n"
    "v_mfma_f32_16x16x32_bf16 v[20:23], a[164:167], v[148:151], v[20:23]\n"
    "v_exp_f32 v137, v137\n"
    "v_mfma_f32_16x16x32_bf16 v[36:39], a[168:171], v[148:151], v[36:39]\n"
    "v_cvt_pk_bf16_f32 v156, v136, v137\n"
    "v_mfma_f32_16x16x32_bf16 v[52:55], a[172:175], v[148:151], v[52:55]\n"
    "v_exp_f32 v138, v138\n"
    "v_mfma_f32_16x16x32_bf16 v[68:71], a[176:179], v[148:151], v[68:71]\n"
    "v_mfma_f32_16x16x32_bf16 v[84:87], a[180:183], v[148:151], v[84:87]\n"
    "v_exp_f32 v139, v139\n"
    "v_mfma_f32_16x16x32_bf16 v[100:103], a[184:187], v[148:151], v[100:103]\n"
    "v_cvt_pk_bf16_f32 v157, v138, v139\n"
    "v_mfma_f32_16x16x32_bf16 v[116:119], a[188:191], v[148:151], v[116:119]\n"
    "v_exp_f32 v140, v140\n"
    "v_mfma_f32_16x16x32_bf16 a[196:199], v[176:179], v[148:151], a[196:199]\n"
    "s_sub_u32 s31, s31, 1\n"
    "s_cmp_lg_u32 s31, 0\n"
    "s_cbranch_scc1 LOOP%=\n"
    "; ---- tiles 29..31 (+ prefetch of the wo fragments), drain, O/l -> bf16 -> publish\n"
    "v_mov_b32 v254, s96\n"
    "v_mov_b32 v255, s97\n"
    "s_waitcnt vmcnt(8)\n"
    "v_cmp_eq_u64 vcc, v[248:249], v[254:255]\n"
    "s_nop 3\n"
    "s_cmp_eq_u64 vcc, -1\n"
    "s_cbranch_scc1 WPD%=\n"
    "v_mov_b32 v254, s96\n"
    "v_mov_b32 v255, s97\n"
    "s_mov_b32 s46, 400000\n"
    "WPL%=:\n"
    "global_load_dwordx2 v[252:253], v238, s[32:33] offset:0 sc1\n"
    "s_waitcnt vmcnt(0)\n"
    "v_cmp_eq_u64 vcc, v[252:253], v[254:255]\n"
    "s_nop 3\n"
    "s_cmp_eq_u64 vcc, -1\n"
    "s_cbranch_scc1 WPLD%=\n"
    "s_sleep 2\n"
    "s_sub_u32 s46, s46, 1\n"
    "s_cmp_lg_u32 s46, 0\n"
    "s_cbranch_scc1 WPL%=\n"
    "WPLD%=:\n"
    "WPD%=:\n"
    "s_nop 0\n"
    "v_mfma_f32_16x16x32_bf16 v[128:131], a[64:67], a[0:3], v[160:163]\n"
    "s_waitcnt lgkmcnt(0)\n"
    "v_exp_f32 v141, v141\n"
    "v_mfma_f32_16x16x32_bf16 v[128:131], a[68:71], a[4:7], v[128:131]\n"
    "s_barrier\n"
    "v_cvt_pk_bf16_f32 v158, v140, v141\n"
    "v_mfma_f32_16x16x32_bf16 v[128:131], a[72:75], a[8:11], v[128:131]\n"
    "v_exp_f32 v142, v142\n"
    "v_mfma_f32_16x16x32_bf16 v[128:131], a[76:79], a[12:15], v[128:131]\n"
    "ds_read_b64_tr_b16 a[128:129], v184 offset:16384\n"
    "v_mfma_f32_16x16x32_bf16 v[132:135], a[80:83], a[0:3], v[160:163]\n"
    "v_exp_f32 v143, v143\n"
    "v_mfma_f32_16x16x32_bf16 v[132:135], a[84:87], a[4:7], v[132:135]\n"
    "ds_read_b64_tr_b16 a[130:131], v184 offset:20480\n"
    "v_cvt_pk_bf16_f32 v159, v142, v143\n"
    "v_mfma_f32_16x16x32_bf16 v[132:135], a[88:91], a[8:11], v[132:135]\n"
    "ds_read_b64_tr_b16 a[132:133], v185 offset:16384\n"
    "v_mfma_f32_16x16x32_bf16 v[132:135], a[92:95], a[12:15], v[132:135]\n"
    "v_exp_f32 v128, v128\n"
    "v_mfma_f32_16x16x32_bf16 v[8:11], a[160:163], v[152:155], v[8:11]\n"
    "ds_read_b64_tr_b16 a[134:135], v185 offset:20480\n"
    "v_mfma_f32_16x16x32_bf16 v[24:27], a[164:167], v[152:155], v[24:27]\n"
    "v_exp_f32 v129, v129\n"
    "v_mfma_f32_16x16x32_bf16 v[40:43], a[168:171], v[152:155], v[40:43]\n"
    "v_cvt_pk_bf16_f32 v144, v128, v129\n"
    "v_mfma_f32_16x16x32_bf16 v[56:59], a[172:175], v[152:155], v[56:59]\n"
    "v_exp_f32 v130, v130\n"
    "v_mfma_f32_16x16x32_bf16 v[72:75], a[176:179], v[152:155], v[72:75]\n"
    "ds_read_b64_tr_b16 a[136:137], v186 offset:16384\n"
    "v_mfma_f32_16x16x32_bf16 v[88:91], a[180:183], v[152:155], v[88:91]\n"
    "v_exp_f32 v131, v131\n"
    "v_mfma_f32_16x16x32_bf16 v[104:107], a[184:187], v[152:155], v[104:107]\n"
    "ds_read_b64_tr_b16 a[138:139], v186 offset:20480\n"
    "v_cvt_pk_bf16_f32 v145, v130, v131\n"
    "v_mfma_f32_16x16x32_bf16 v[120:123], a[188:191], v[152:155], v[120:123]\n"
    "v_exp_f32 v132, v132\n"
    "v_mfma_f32_16x16x32_bf16 a[200:203], v[176:179], v[152:155], a[200:203]\n"
    "ds_read_b64_tr_b16 a[140:141], v187 offset:16384\n"
    "v_mfma_f32_16x16x32_bf16 v[136:139], a[64:67], a[16:19], v[164:167]\n"
    "v_exp_f32 v133, v133\n"
    "v_mfma_f32_16x16x32_bf16 v[136:139], a[68:71], a[20:23], v[136:139]\n"
    "ds_read_b64_tr_b16 a[142:143], v187 offset:20480\n"
    "v_cvt_pk_bf16_f32 v146, v132, v133\n"
    "v_mfma_f32_16x16x32_bf16 v[136:139], a[72:75], a[24:27], v[136:139]\n"
    "v_exp_f32 v134, v134\n"
    "v_mfma_f32_16x16x32_bf16 v[136:139], a[76:79], a[28:31], v[136:139]\n"
    "s_add_u32 m0, s30, 0x1c000\n"
    "ds_read_b64_tr_b16 a[144:145], v188 offset:16384\n"
    "v_mfma_f32_16x16x32_bf16 v[140:143], a[80:83], a[16:19], v[164:167]\n"
    "v_exp_f32 v135, v135\n"
    "v_mfma_f32_16x16x32_bf16 v[140:143], a[84:87], a[20:23], v[140:143]\n"
    "buffer_load_dwordx4 v196, s[24:27], s29 offen offset:0 nt lds\n"
    "v_mfma_f32_16x16x32_bf16 v[140:143], a[88:91], a[24:27], v[140:143]\n"
    "ds_read_b64_tr_b16 a[146:147], v188 offset:20480\n"
    "v_cvt_pk_bf16_f32 v147, v134, v135\n"
    "v_mfma_f32_16x16x32_bf16 v[140:143], a[92:95], a[28:31], v[140:143]\n"
    "v_exp_f32 v136, v136\n"
    "v_mfma_f32_16x16x32_bf16 v[12:15], a[160:163], v[156:159], v[12:15]\n"
    "ds_read_b64_tr_b16 a[148:149], v189 offset:16384\n"
    "v_mfma_f32_16x16x32_bf16 v[28:31], a[164:167], v[156:159], v[28:31]\n"
    "v_exp_f32 v137, v137\n"
    "v_mfma_f32_16x16x32_bf16 v[44:47], a[168:171], v[156:159], v[44:47]\n"
    "buffer_load_dwordx4 v197, s[24:27], s29 offen offset:1024 nt lds\n"
    "v_cvt_pk_bf16_f32 v148, v136, v137\n"
    "v_mfma_f32_16x16x32_bf16 v[60:63], a[172:175], v[156:159], v[60:63]\n"
    "v_exp_f32 v138, v138\n"
    "v_mfma_f32_16x16x32_bf16 v[76:79], a[176:179], v[156:159], v[76:79]\n"
    "ds_read_b64_tr_b16 a[150:151], v189 offset:20480\n"
    "ds_read_b64_tr_b16 a[152:153], v190 offset:16384\n"
    "v_mfma_f32_16x16x32_bf16 v[92:95], a[180:183], v[156:159], v[92:95]\n"
    "v_exp_f32 v139, v139\n"
    "v_mfma_f32_16x16x32_bf16 v[108:111], a[184:187], v[156:159], v[108:111]\n"
    "buffer_load_dwordx4 v196, s[24:27], s29 offen offset:2048 nt lds\n"
    "v_cvt_pk_bf16_f32 v149, v138, v139\n"
    "v_mfma_f32_16x16x32_bf16 v[124:127], a[188:191], v[156:159], v[124:127]\n"
    "v_exp_f32 v140, v140\n"
    "v_mfma_f32_16x16x32_bf16 a[204:207], v[176:179], v[156:159], a[204:207]\n"
    "ds_read_b64_tr_b16 a[154:155], v190 offset:20480\n"
    "ds_read_b64_tr_b16 a[156:157], v191 offset:16384\n"
    "v_mfma_f32_16x16x32_bf16 v[128:131], a[64:67], a[32:35], v[168:171]\n"
    "v_exp_f32 v141, v141\n"
    "v_mfma_f32_16x16x32_bf16 v[128:131], a[68:71], a[36:39], v[128:131]\n"
    "buffer_load_dwordx4 v197, s[24:27], s29 offen offset:3072 nt lds\n"
    "v_mfma_f32_16x16x32_bf16 v[128:131], a[72:75], a[40:43], v[128:131]\n"
    "v_exp_f32 v142, v142\n"
    "v_mfma_f32_16x16x32_bf16 v[128:131], a[76:79], a[44:47], v[128:131]\n"
    "s_add_u32 s29, s29, 16384\n"
    "s_and_b32 s29, s29, 0x7ffff\n"
    "ds_read_b64_tr_b16 a[158:159], v191 offset:20480\n"
    "v_mfma_f32_16x16x32_bf16 v[132:135], a[80:83], a[32:35], v[168:171]\n"
    "v_exp_f32 v143, v143\n"
    "v_mfma_f32_16x16x32_bf16 v[132:135], a[84:87], a[36:39], v[132:135]\n"
    "v_cvt_pk_bf16_f32 v150, v140, v141\n"
    "v_cvt_pk_bf16_f32 v151, v142, v143\n"
    "v_mfma_f32_16x16x32_bf16 v[132:135], a[88:91], a[40:43], v[132:135]\n"
    "s_add_u32 m0, s30, 0x20000\n"
    "v_mfma_f32_16x16x32_bf16 v[132:135], a[92:95], a[44:47], v[132:135]\n"
    "s_waitcnt lgkmcnt(0)\n"
    "s_mov_b32 s43, 94208\n"
    "v_exp_f32 v128, v128\n"
    "v_mfma_f32_16x16x32_bf16 v[0:3], a[128:131], v[144:147], v[0:3]\n"
    "ds_read_b128 a[96:99], v180 offset:24576\n"
    "buffer_load_dwordx4 v237, s[52:55], s43 offen offset:0 sc1 lds\n"
    "v_mfma_f32_16x16x32_bf16 v[16:19], a[132:135], v[144:147], v[16:19]\n"
    "v_exp_f32 v129, v129\n"
    "v_mfma_f32_16x16x32_bf16 v[32:35], a[136:139], v[144:147], v[32:35]\n"
    "ds_read_b128 a[100:103], v181 offset:24576\n"
    "v_cvt_pk_bf16_f32 v152, v128, v129\n"
    "v_mfma_f32_16x16x32_bf16 v[48:51], a[140:143], v[144:147], v[48:51]\n"
    "v_exp_f32 v130, v130\n"
    "v_mfma_f32_16x16x32_bf16 v[64:67], a[144:147], v[144:147], v[64:67]\n"
    "ds_read_b128 a[104:107], v182 offset:24576\n"
    "buffer_load_dwordx4 v237, s[52:55], s43 offen offset:1024 sc1 lds\n"
    "v_mfma_f32_16x16x32_bf16 v[80:83], a[148:151], v[144:147], v[80:83]\n"
    "v_exp_f32 v131, v131\n"
    "v_mfma_f32_16x16x32_bf16 v[96:99], a[152:155], v[144:147], v[96:99]\n"
    "ds_read_b128 a[108:111], v183 offset:24576\n"
    "v_cvt_pk_bf16_f32 v153, v130, v131\n"
    "v_mfma_f32_16x16x32_bf16 v[112:115], a[156:159], v[144:147], v[112:115]\n"
    "v_exp_f32 v132, v132\n"
    "v_mfma_f32_16x16x32_bf16 a[192:195], v[176:179], v[144:147], a[192:195]\n"
    "ds_read_b128 a[112:115], v180 offset:28672\n"
    "buffer_load_dwordx4 v237, s[52:55], s43 offen offset:2048 sc1 lds\n"
    "v_mfma_f32_16x16x32_bf16 v[136:139], a[64:67], a[48:51], v[172:175]\n"
    "v_exp_f32 v133, v133\n"
    "v_mfma_f32_16x16x32_bf16 v[136:139], a[68:71], a[52:55], v[136:139]\n"
    "ds_read_b128 a[116:119], v181 offset:28672\n"
    "v_cvt_pk_bf16_f32 v154, v132, v133\n"
    "v_mfma_f32_16x16x32_bf16 v[136:139], a[72:75], a[56:59], v[136:139]\n"
    "v_exp_f32 v134, v134\n"
    "v_mfma_f32_16x16x32_bf16 v[136:139], a[76:79], a[60:63], v[136:139]\n"
    "buffer_load_dwordx4 v237, s[52:55], s43 offen offset:3072 sc1 lds\n"
    "v_mfma_f32_16x16x32_bf16 v[140:143], a[80:83], a[48:51], v[172:175]\n"
    "s_add_u32 m0, s30, 0x24000\n"
    "v_exp_f32 v135, v135\n"
    "v_mfma_f32_16x16x32_bf16 v[140:143], a[84:87], a[52:55], v[140:143]\n"
    "s_mov_b32 s43, 98304\n"
    "ds_read_b128 a[120:123], v182 offset:28672\n"
    "v_cvt_pk_bf16_f32 v155, v134, v135\n"
    "v_mfma_f32_16x16x32_bf16 v[140:143], a[88:91], a[56:59], v[140:143]\n"
    "ds_read_b128 a[124:127], v183 offset:28672\n"
    "buffer_load_dwordx4 v237, s[52:55], s43 offen offset:0 sc1 lds\n"
    "v_mfma_f32_16x16x32_bf16 v[140:143], a[92:95], a[60:63], v[140:143]\n"
    "v_exp_f32 v136, v136\n"
    "v_mfma_f32_16x16x32_bf16 v[4:7], a[128:131], v[148:151], v[4:7]\n"
    "buffer_load_dwordx4 v237, s[52:55], s43 offen offset:1024 sc1 lds\n"
    "v_mfma_f32_16x16x32_bf16 v[20:23], a[132:135], v[148:151], v[20:23]\n"
    "v_exp_f32 v137, v137\n"
    "v_add_u32 v240, 0, v237\n"
    "v_add_u32 v241, 4096, v237\n"
    "v_add_u32 v242, 8192, v237\n"
    "v_add_u32 v243, 12288, v237\n"
    "v_add_u32 v244, 0, v237\n"
    "v_add_u32 v245, 4096, v237\n"
    "v_add_u32 v246, 8192, v237\n"
    "v_add_u32 v247, 12288, v237\n"
    "v_mfma_f32_16x16x32_bf16 v[36:39], a[136:139], v[148:151], v[36:39]\n"
    "buffer_load_dwordx4 v237, s[52:55], s43 offen offset:2048 sc1 lds\n"
    "v_cvt_pk_bf16_f32 v156, v136, v137\n"
    "v_mfma_f32_16x16x32_bf16 v[52:55], a[140:143], v[148:151], v[52:55]\n"
    "v_exp_f32 v138, v138\n"
    "v_mfma_f32_16x16x32_bf16 v[68:71], a[144:147], v[148:151], v[68:71]\n"
    "ds_read_b64_tr_b16 a[160:161], v184 offset:24576\n"
    "buffer_load_dwordx4 v237, s[52:55], s43 offen offset:3072 sc1 lds\n"
    "v_mfma_f32_16x16x32_bf16 v[84:87], a[148:151], v[148:151], v[84:87]\n"
    "s_add_u32 m0, s30, 0x0\n"
    "v_exp_f32 v139, v139\n"
    "v_mfma_f32_16x16x32_bf16 v[100:103], a[152:155], v[148:151], v[100:103]\n"
    "s_mov_b32 s43, 102400\n"
    "ds_read_b64_tr_b16 a[162:163], v184 offset:28672\n"
    "v_cvt_pk_bf16_f32 v157, v138, v139\n"
    "v_mfma_f32_16x16x32_bf16 v[116:119], a[156:159], v[148:151], v[116:119]\n"
    "v_exp_f32 v140, v140\n"
    "v_mfma_f32_16x16x32_bf16 a[196:199], v[176:179], v[148:151], a[196:199]\n"
    "s_waitcnt lgkmcnt(1)\n"
    "ds_read_b64_tr_b16 a[164:165], v185 offset:24576\n"
    "buffer_load_dwordx4 v237, s[52:55], s43 offen offset:0 sc1 lds\n"
    "v_mfma_f32_16x16x32_bf16 v[128:131], a[96:99], a[0:3], v[160:163]\n"
    "v_exp_f32 v141, v141\n"
    "v_mfma_f32_16x16x32_bf16 v[128:131], a[100:103], a[4:7], v[128:131]\n"
    "buffer_load_dwordx4 v237, s[52:55], s43 offen offset:1024 sc1 lds\n"
    "v_cvt_pk_bf16_f32 v158, v140, v141\n"
    "v_mfma_f32_16x16x32_bf16 v[128:131], a[104:107], a[8:11], v[128:131]\n"
    "v_exp_f32 v142, v142\n"
    "v_mfma_f32_16x16x32_bf16 v[128:131], a[108:111], a[12:15], v[128:131]\n"
    "ds_read_b64_tr_b16 a[166:167], v185 offset:28672\n"
    "buffer_load_dwordx4 v237, s[52:55], s43 offen offset:2048 sc1 lds\n"
    "v_mfma_f32_16x16x32_bf16 v[132:135], a[112:115], a[0:3], v[160:163]\n"
    "v_exp_f32 v143, v143\n"
    "v_mfma_f32_16x16x32_bf16 v[132:135], a[116:119], a[4:7], v[132:135]\n"
    "ds_read_b64_tr_b16 a[168:169], v186 offset:24576\n"
    "v_cvt_pk_bf16_f32 v159, v142, v143\n"
    "v_mfma_f32_16x16x32_bf16 v[132:135], a[120:123], a[8:11], v[132:135]\n"
    "buffer_load_dwordx4 v237, s[52:55], s43 offen offset:3072 sc1 lds\n"
    "v_mfma_f32_16x16x32_bf16 v[132:135], a[124:127], a[12:15], v[132:135]\n"
    "v_exp_f32 v128, v128\n"
    "v_mfma_f32_16x16x32_bf16 v[8:11], a[128:131], v[152:155], v[8:11]\n"
    "ds_read_b64_tr_b16 a[170:171], v186 offset:28672\n"
    "v_mfma_f32_16x16x32_bf16 v[24:27], a[132:135], v[152:155], v[24:27]\n"
    "v_exp_f32 v129, v129\n"
    "v_mfma_f32_16x16x32_bf16 v[40:43], a[136:139], v[152:155], v[40:43]\n"
    "ds_read_b64_tr_b16 a[172:173], v187 offset:24576\n"
    "v_cvt_pk_bf16_f32 v144, v128, v129\n"
    "v_mfma_f32_16x16x32_bf16 v[56:59], a[140:143], v[152:155], v[56:59]\n"
    "v_exp_f32 v130, v130\n"
    "v_mfma_f32_16x16x32_bf16 v[72:75], a[144:147], v[152:155], v[72:75]\n"
    "ds_read_b64_tr_b16 a[174:175], v187 offset:28672\n"
    "v_mfma_f32_16x16x32_bf16 v[88:91], a[148:151], v[152:155], v[88:91]\n"
    "v_exp_f32 v131, v131\n"
    "v_mfma_f32_16x16x32_bf16 v[104:107], a[152:155], v[152:155], v[104:107]\n"
    "ds_read_b64_tr_b16 a[176:177], v188 offset:24576\n"
    "v_cvt_pk_bf16_f32 v145, v130, v131\n"
    "v_mfma_f32_16x16x32_bf16 v[120:123], a[156:159], v[152:155], v[120:123]\n"
    "v_exp_f32 v132, v132\n"
    "v_mfma_f32_16x16x32_bf16 a[200:203], v[176:179], v[152:155], a[200:203]\n"
    "ds_read_b64_tr_b16 a[178:179], v188 offset:28672\n"
    "v_mfma_f32_16x16x32_bf16 v[136:139], a[96:99], a[16:19], v[164:167]\n"
    "v_exp_f32 v133, v133\n"
    "v_mfma_f32_16x16x32_bf16 v[136:139], a[100:103], a[20:23], v[136:139]\n"
    "v_cvt_pk_bf16_f32 v146, v132, v133\n"
    "v_mfma_f32_16x16x32_bf16 v[136:139], a[104:107], a[24:27], v[136:139]\n"
    "v_exp_f32 v134, v134\n"
    "v_mfma_f32_16x16x32_bf16 v[136:139], a[108:111], a[28:31], v[136:139]\n"
    "ds_read_b64_tr_b16 a[180:181], v189 offset:24576\n"
    "v_mfma_f32_16x16x32_bf16 v[140:143], a[112:115], a[16:19], v[164:167]\n"
    "v_exp_f32 v135, v135\n"
    "v_mfma_f32_16x16x32_bf16 v[140:143], a[116:119], a[20:23], v[140:143]\n"
    "ds_read_b64_tr_b16 a[182:183], v189 offset:28672\n"
    "v_cvt_pk_bf16_f32 v147, v134, v135\n"
    "v_mfma_f32_16x16x32_bf16 v[140:143], a[120:123], a[24:27], v[140:143]\n"
    "v_mfma_f32_16x16x32_bf16 v[140:143], a[124:127], a[28:31], v[140:143]\n"
    "v_exp_f32 v136, v136\n"
    "v_mfma_f32_16x16x32_bf16 v[12:15], a[128:131], v[156:159], v[12:15]\n"
    "ds_read_b64_tr_b16 a[184:185], v190 offset:24576\n"
    "v_mfma_f32_16x16x32_bf16 v[28:31], a[132:135], v[156:159], v[28:31]\n"
    "v_exp_f32 v137, v137\n"
    "v_mfma_f32_16x16x32_bf16 v[44:47], a[136:139], v[156:159], v[44:47]\n"
    "ds_read_b64_tr_b16 a[186:187], v190 offset:28672\n"
    "v_cvt_pk_bf16_f32 v148, v136, v137\n"
    "v_mfma_f32_16x16x32_bf16 v[60:63], a[140:143], v[156:159], v[60:63]\n"
    "v_exp_f32 v138, v138\n"
    "v_mfma_f32_16x16x32_bf16 v[76:79], a[144:147], v[156:159], v[76:79]\n"
    "ds_read_b64_tr_b16 a[188:189], v191 offset:24576\n"
    "v_mfma_f32_16x16x32_bf16 v[92:95], a[148:151], v[156:159], v[92:95]\n"
    "v_exp_f32 v139, v139\n"
    "v_mfma_f32_16x16x32_bf16 v[108:111], a[152:155], v[156:159], v[108:111]\n"
    "ds_read_b64_tr_b16 a[190:191], v191 offset:28672\n"
    "v_cvt_pk_bf16_f32 v149, v138, v139\n"
    "v_mfma_f32_16x16x32_bf16 v[124:127], a[156:159], v[156:159], v[124:127]\n"
    "v_exp_f32 v140, v140\n"
    "v_mfma_f32_16x16x32_bf16 a[204:207], v[176:179], v[156:159], a[204:207]\n"
    "v_mfma_f32_16x16x32_bf16 v[128:131], a[96:99], a[32:35], v[168:171]\n"
    "v_exp_f32 v141, v141\n"
    "v_mfma_f32_16x16x32_bf16 v[128:131], a[100:103], a[36:39], v[128:131]\n"
    "v_cvt_pk_bf16_f32 v150, v140, v141\n"
    "v_mfma_f32_16x16x32_bf16 v[128:131], a[104:107], a[40:43], v[128:131]\n"
    "v_exp_f32 v142, v142\n"
    "v_mfma_f32_16x16x32_bf16 v[128:131], a[108:111], a[44:47], v[128:131]\n"
    "ds_read_b128 a[64:67], v180 offset:32768\n"
    "v_mfma_f32_16x16x32_bf16 v[132:135], a[112:115], a[32:35], v[168:171]\n"
    "v_exp_f32 v143, v143\n"
    "v_mfma_f32_16x16x32_bf16 v[132:135], a[116:119], a[36:39], v[132:135]\n"
    "ds_read_b128 a[68:71], v181 offset:32768\n"
    "v_cvt_pk_bf16_f32 v151, v142, v143\n"
    "v_mfma_f32_16x16x32_bf16 v[132:135], a[120:123], a[40:43], v[132:135]\n"
    "v_mfma_f32_16x16x32_bf16 v[132:135], a[124:127], a[44:47], v[132:135]\n"
    "s_waitcnt lgkmcnt(1)\n"
    "v_exp_f32 v128, v128\n"
    "v_mfma_f32_16x16x32_bf16 v[0:3], a[160:163], v[144:147], v[0:3]\n"
    "ds_read_b128 a[72:75], v182 offset:32768\n"
    "v_mfma_f32_16x16x32_bf16 v[16:19], a[164:167], v[144:147], v[16:19]\n"
    "v_exp_f32 v129, v129\n"
    "v_mfma_f32_16x16x32_bf16 v[32:35], a[168:171], v[144:147], v[32:35]\n"
    "v_cvt_pk_bf16_f32 v152, v128, v129\n"
    "v_mfma_f32_16x16x32_bf16 v[48:51], a[172:175], v[144:147], v[48:51]\n"
    "v_exp_f32 v130, v130\n"
    "v_mfma_f32_16x16x32_bf16 v[64:67], a[176:179], v[144:147], v[64:67]\n"
    "ds_read_b128 a[76:79], v183 offset:32768\n"
    "v_mfma_f32_16x16x32_bf16 v[80:83], a[180:183], v[144:147], v[80:83]\n"
    "v_exp_f32 v131, v131\n"
    "v_mfma_f32_16x16x32_bf16 v[96:99], a[184:187], v[144:147], v[96:99]\n"
    "ds_read_b128 a[80:83], v180 offset:36864\n"
    "v_cvt_pk_bf16_f32 v153, v130, v131\n"
    "v_mfma_f32_16x16x32_bf16 v[112:115], a[188:191], v[144:147], v[112:115]\n"
    "v_exp_f32 v132, v132\n"
    "v_mfma_f32_16x16x32_bf16 a[192:195], v[176:179], v[144:147], a[192:195]\n"
    "s_memrealtime s[64:65]\n"
    "v_mfma_f32_16x16x32_bf16 v[136:139], a[96:99], a[48:51], v[172:175]\n"
    "v_exp_f32 v133, v133\n"
    "v_mfma_f32_16x16x32_bf16 v[136:139], a[100:103], a[52:55], v[136:139]\n"
    "ds_read_b128 a[84:87], v181 offset:36864\n"
    "v_cvt_pk_bf16_f32 v154, v132, v133\n"
    "v_mfma_f32_16x16x32_bf16 v[136:139], a[104:107], a[56:59], v[136:139]\n"
    "v_exp_f32 v134, v134\n"
    "v_mfma_f32_16x16x32_bf16 v[136:139], a[108:111], a[60:63], v[136:139]\n"
    "ds_read_b128 a[88:91], v182 offset:36864\n"
    "v_mfma_f32_16x16x32_bf16 v[140:143], a[112:115], a[48:51], v[172:175]\n"
    "v_exp_f32 v135, v135\n"
    "v_mfma_f32_16x16x32_bf16 v[140:143], a[116:119], a[52:55], v[140:143]\n"
    "v_cvt_pk_bf16_f32 v155, v134, v135\n"
    "v_mfma_f32_16x16x32_bf16 v[140:143], a[120:123], a[56:59], v[140:143]\n"
    "ds_read_b128 a[92:95], v183 offset:36864\n"
    "v_mfma_f32_16x16x32_bf16 v[140:143], a[124:127], a[60:63], v[140:143]\n"
    "v_exp_f32 v136, v136\n"
    "v_mfma_f32_16x16x32_bf16 v[4:7], a[160:163], v[148:151], v[4:7]\n"
    "v_mfma_f32_16x16x32_bf16 v[20:23], a[164:167], v[148:151], v[20:23]\n"
    "v_exp_f32 v137, v137\n"
    "v_mfma_f32_16x16x32_bf16 v[36:39], a[168:171], v[148:151], v[36:39]\n"
    "v_cvt_pk_bf16_f32 v156, v136, v137\n"
    "v_mfma_f32_16x16x32_bf16 v[52:55], a[172:175], v[148:151], v[52:55]\n"
    "v_exp_f32 v138, v138\n"
    "v_mfma_f32_16x16x32_bf16 v[68:71], a[176:179], v[148:151], v[68:71]\n"
    "v_mfma_f32_16x16x32_bf16 v[84:87], a[180:183], v[148:151], v[84:87]\n"
    "v_exp_f32 v139, v139\n"
    "v_mfma_f32_16x16x32_bf16 v[100:103], a[184:187], v[148:151], v[100:103]\n"
    "v_cvt_pk_bf16_f32 v157, v138, v139\n"
    "v_mfma_f32_16x16x32_bf16 v[116:119], a[188:191], v[148:151], v[116:119]\n"
    "v_exp_f32 v140, v140\n"
    "v_mfma_f32_16x16x32_bf16 a[196:199], v[176:179], v[148:151], a[196:199]\n"
    "v_mfma_f32_16x16x32_bf16 v[128:131], a[64:67], a[0:3], v[160:163]\n"
    "s_waitcnt lgkmcnt(0)\n"
    "s_waitcnt vmcnt(16)\n"
    "v_exp_f32 v141, v141\n"
    "v_mfma_f32_16x16x32_bf16 v[128:131], a[68:71], a[4:7], v[128:131]\n"
    "s_barrier\n"
    "v_cvt_pk_bf16_f32 v158, v140, v141\n"
    "v_mfma_f32_16x16x32_bf16 v[128:131], a[72:75], a[8:11], v[128:131]\n"
    "v_exp_f32 v142, v142\n"
    "v_mfma_f32_16x16x32_bf16 v[128:131], a[76:79], a[12:15], v[128:131]\n"
    "ds_read_b64_tr_b16 a[128:129], v184 offset:32768\n"
    "v_mfma_f32_16x16x32_bf16 v[132:135], a[80:83], a[0:3], v[160:163]\n"
    "s_add_u32 m0, s30, 0x4000\n"
    "v_exp_f32 v143, v143\n"
    "v_mfma_f32_16x16x32_bf16 v[132:135], a[84:87], a[4:7], v[132:135]\n"
    "s_mov_b32 s43, 106496\n"
    "ds_read_b64_tr_b16 a[130:131], v184 offset:36864\n"
    "v_cvt_pk_bf16_f32 v159, v142, v143\n"
    "v_mfma_f32_16x16x32_bf16 v[132:135], a[88:91], a[8:11], v[132:135]\n"
    "ds_read_b64_tr_b16 a[132:133], v185 offset:32768\n"
    "buffer_load_dwordx4 v237, s[52:55], s43 offen offset:0 sc1 lds\n"
    "v_mfma_f32_16x16x32_bf16 v[132:135], a[92:95], a[12:15], v[132:135]\n"
    "v_exp_f32 v128, v128\n"
    "v_mfma_f32_16x16x32_bf16 v[8:11], a[160:163], v[152:155], v[8:11]\n"
    "ds_read_b64_tr_b16 a[134:135], v185 offset:36864\n"
    "buffer_load_dwordx4 v237, s[52:55], s43 offen offset:1024 sc1 lds\n"
    "v_mfma_f32_16x16x32_bf16 v[24:27], a[164:167], v[152:155], v[24:27]\n"
    "v_exp_f32 v129, v129\n"
    "v_mfma_f32_16x16x32_bf16 v[40:43], a[168:171], v[152:155], v[40:43]\n"
    "buffer_load_dwordx4 v237, s[52:55], s43 offen offset:2048 sc1 lds\n"
    "v_cvt_pk_bf16_f32 v144, v128, v129\n"
    "v_mfma_f32_16x16x32_bf16 v[56:59], a[172:175], v[152:155], v[56:59]\n"
    "v_exp_f32 v130, v130\n"
    "v_mfma_f32_16x16x32_bf16 v[72:75], a[176:179], v[152:155], v[72:75]\n"
    "ds_read_b64_tr_b16 a[136:137], v186 offset:32768\n"
    "buffer_load_dwordx4 v237, s[52:55], s43 offen offset:3072 sc1 lds\n"
    "v_mfma_f32_16x16x32_bf16 v[88:91], a[180:183], v[152:155], v[88:91]\n"
    "s_add_u32 m0, s30, 0x10000\n"
    "v_exp_f32 v131, v131\n"
    "v_mfma_f32_16x16x32_bf16 v[104:107], a[184:187], v[152:155], v[104:107]\n"
    "s_mov_b32 s43, 110592\n"
    "ds_read_b64_tr_b16 a[138:139], v186 offset:36864\n"
    "v_cvt_pk_bf16_f32 v145, v130, v131\n"
    "v_mfma_f32_16x16x32_bf16 v[120:123], a[188:191], v[152:155], v[120:123]\n"
    "v_exp_f32 v132, v132\n"
    "v_mfma_f32_16x16x32_bf16 a[200:203], v[176:179], v[152:155], a[200:203]\n"
    "ds_read_b64_tr_b16 a[140:141], v187 offset:32768\n"
    "buffer_load_dwordx4 v237, s[52:55], s43 offen offset:0 sc1 lds\n"
    "v_mfma_f32_16x16x32_bf16 v[136:139], a[64:67], a[16:19], v[164:167]\n"
    "v_exp_f32 v133, v133\n"
    "v_mfma_f32_16x16x32_bf16 v[136:139], a[68:71], a[20:23], v[136:139]\n"
    "ds_read_b64_tr_b16 a[142:143], v187 offset:36864\n"
    "v_cvt_pk_bf16_f32 v146, v132, v133\n"
    "v_mfma_f32_16x16x32_bf16 v[136:139], a[72:75], a[24:27], v[136:139]\n"
    "v_exp_f32 v134, v134\n"
    "v_mfma_f32_16x16x32_bf16 v[136:139], a[76:79], a[28:31], v[136:139]\n"
    "ds_read_b64_tr_b16 a[144:145], v188 offset:32768\n"
    "buffer_load_dwordx4 v237, s[52:55], s43 offen offset:1024 sc1 lds\n"
    "v_mfma_f32_16x16x32_bf16 v[140:143], a[80:83], a[16:19], v[164:167]\n"
    "v_exp_f32 v135, v135\n"
    "v_mfma_f32_16x16x32_bf16 v[140:143], a[84:87], a[20:23], v[140:143]\n"
    "ds_read_b64_tr_b16 a[146:147], v188 offset:36864\n"
    "v_cvt_pk_bf16_f32 v147, v134, v135\n"
    "v_mfma_f32_16x16x32_bf16 v[140:143], a[88:91], a[24:27], v[140:143]\n"
    "buffer_load_dwordx4 v237, s[52:55], s43 offen offset:2048 sc1 lds\n"
    "v_mfma_f32_16x16x32_bf16 v[140:143], a[92:95], a[28:31], v[140:143]\n"
    "v_exp_f32 v136, v136\n"
    "v_mfma_f32_16x16x32_bf16 v[12:15], a[160:163], v[156:159], v[12:15]\n"
    "ds_read_b64_tr_b16 a[148:149], v189 offset:32768\n"
    "buffer_load_dwordx4 v237, s[52:55], s43 offen offset:3072 sc1 lds\n"
    "v_mfma_f32_16x16x32_bf16 v[28:31], a[164:167], v[156:159], v[28:31]\n"
    "v_exp_f32 v137, v137\n"
    "v_mfma_f32_16x16x32_bf16 v[44:47], a[168:171], v[156:159], v[44:47]\n"
    "ds_read_b64_tr_b16 a[150:151], v189 offset:36864\n"
    "v_cvt_pk_bf16_f32 v148, v136, v137\n"
    "v_mfma_f32_16x16x32_bf16 v[60:63], a[172:175], v[156:159], v[60:63]\n"
    "v_exp_f32 v138, v138\n"
    "v_mfma_f32_16x16x32_bf16 v[76:79], a[176:179], v[156:159], v[76:79]\n"
    "ds_read_b64_tr_b16 a[152:153], v190 offset:32768\n"
    "v_mfma_f32_16x16x32_bf16 v[92:95], a[180:183], v[156:159], v[92:95]\n"
    "v_exp_f32 v139, v139\n"
    "v_mfma_f32_16x16x32_bf16 v[108:111], a[184:187], v[156:159], v[108:111]\n"
    "ds_read_b64_tr_b16 a[154:155], v190 offset:36864\n"
    "v_cvt_pk_bf16_f32 v149, v138, v139\n"
    "v_mfma_f32_16x16x32_bf16 v[124:127], a[188:191], v[156:159], v[124:127]\n"
    "v_exp_f32 v140, v140\n"
    "v_mfma_f32_16x16x32_bf16 a[204:207], v[176:179], v[156:159], a[204:207]\n"
    "ds_read_b64_tr_b16 a[156:157], v191 offset:32768\n"
    "global_load_dwordx4 a[208:211], v240, s[60:61] offset:0 sc1\n"
    "v_mfma_f32_16x16x32_bf16 v[128:131], a[64:67], a[32:35], v[168:171]\n"
    "v_exp_f32 v141, v141\n"
    "v_mfma_f32_16x16x32_bf16 v[128:131], a[68:71], a[36:39], v[128:131]\n"
    "ds_read_b64_tr_b16 a[158:159], v191 offset:36864\n"
    "v_cvt_pk_bf16_f32 v150, v140, v141\n"
    "v_mfma_f32_16x16x32_bf16 v[128:131], a[72:75], a[40:43], v[128:131]\n"
    "v_exp_f32 v142, v142\n"
    "v_mfma_f32_16x16x32_bf16 v[128:131], a[76:79], a[44:47], v[128:131]\n"
    "global_load_dwordx4 a[212:215], v240, s[60:61] offset:1024 sc1\n"
    "v_mfma_f32_16x16x32_bf16 v[132:135], a[80:83], a[32:35], v[168:171]\n"
    "v_exp_f32 v143, v143\n"
    "v_mfma_f32_16x16x32_bf16 v[132:135], a[84:87], a[36:39], v[132:135]\n"
    "global_load_dwordx4 a[216:219], v240, s[60:61] offset:2048 sc1\n"
    "v_cvt_pk_bf16_f32 v151, v142, v143\n"
    "v_mfma_f32_16x16x32_bf16 v[132:135], a[88:91], a[40:43], v[132:135]\n"
    "global_load_dwordx4 a[220:223], v240, s[60:61] offset:3072 sc1\n"
    "v_mfma_f32_16x16x32_bf16 v[132:135], a[92:95], a[44:47], v[132:135]\n"
    "s_waitcnt lgkmcnt(0)\n"
    "v_exp_f32 v128, v128\n"
    "v_mfma_f32_16x16x32_bf16 v[0:3], a[128:131], v[144:147], v[0:3]\n"
    "ds_read_b128 a[96:99], v180 offset:40960\n"
    "global_load_dwordx4 a[224:227], v241, s[60:61] offset:0 sc1\n"
    "v_mfma_f32_16x16x32_bf16 v[16:19], a[132:135], v[144:147], v[16:19]\n"
    "v_exp_f32 v129, v129\n"
    "v_mfma_f32_16x16x32_bf16 v[32:35], a[136:139], v[144:147], v[32:35]\n"
    "ds_read_b128 a[100:103], v181 offset:40960\n"
    "v_cvt_pk_bf16_f32 v152, v128, v129\n"
    "v_mfma_f32_16x16x32_bf16 v[48:51], a[140:143], v[144:147], v[48:51]\n"
    "v_exp_f32 v130, v130\n"
    "v_mfma_f32_16x16x32_bf16 v[64:67], a[144:147], v[144:147], v[64:67]\n"
    "ds_read_b128 a[104:107], v182 offset:40960\n"
    "global_load_dwordx4 a[228:231], v241, s[60:61] offset:1024 sc1\n"
    "v_mfma_f32_16x16x32_bf16 v[80:83], a[148:151], v[144:147], v[80:83]\n"
    "v_exp_f32 v131, v131\n"
    "v_mfma_f32_16x16x32_bf16 v[96:99], a[152:155], v[144:147], v[96:99]\n"
    "ds_read_b128 a[108:111], v183 offset:40960\n"
    "v_cvt_pk_bf16_f32 v153, v130, v131\n"
    "v_mfma_f32_16x16x32_bf16 v[112:115], a[156:159], v[144:147], v[112:115]\n"
    "v_exp_f32 v132, v132\n"
    "v_mfma_f32_16x16x32_bf16 a[192:195], v[176:179], v[144:147], a[192:195]\n"
    "ds_read_b128 a[112:115], v180 offset:45056\n"
    "global_load_dwordx4 a[232:235], v241, s[60:61] offset:2048 sc1\n"
    "v_mfma_f32_16x16x32_bf16 v[136:139], a[64:67], a[48:51], v[172:175]\n"
    "v_exp_f32 v133, v133\n"
    "v_mfma_f32_16x16x32_bf16 v[136:139], a[68:71], a[52:55], v[136:139]\n"
    "ds_read_b128 a[116:119], v181 offset:45056\n"
    "v_cvt_pk_bf16_f32 v154, v132, v133\n"
    "v_mfma_f32_16x16x32_bf16 v[136:139], a[72:75], a[56:59], v[136:139]\n"
    "v_exp_f32 v134, v134\n"
    "v_mfma_f32_16x16x32_bf16 v[136:139], a[76:79], a[60:63], v[136:139]\n"
    "global_load_dwordx4 a[236:239], v241, s[60:61] offset:3072 sc1\n"
    "v_mfma_f32_16x16x32_bf16 v[140:143], a[80:83], a[48:51], v[172:175]\n"
    "v_exp_f32 v135, v135\n"
    "v_mfma_f32_16x16x32_bf16 v[140:143], a[84:87], a[52:55], v[140:143]\n"
    "ds_read_b128 a[120:123], v182 offset:45056\n"
    "v_cvt_pk_bf16_f32 v155, v134, v135\n"
    "v_mfma_f32_16x16x32_bf16 v[140:143], a[88:91], a[56:59], v[140:143]\n"
    "ds_read_b128 a[124:127], v183 offset:45056\n"
    "global_load_dwordx4 a[240:243], v242, s[60:61] offset:0 sc1\n"
    "v_mfma_f32_16x16x32_bf16 v[140:143], a[92:95], a[60:63], v[140:143]\n"
    "v_exp_f32 v136, v136\n"
    "v_mfma_f32_16x16x32_bf16 v[4:7], a[128:131], v[148:151], v[4:7]\n"
    "global_load_dwordx4 a[244:247], v242, s[60:61] offset:1024 sc1\n"
    "v_mfma_f32_16x16x32_bf16 v[20:23], a[132:135], v[148:151], v[20:23]\n"
    "v_exp_f32 v137, v137\n"
    "v_mfma_f32_16x16x32_bf16 v[36:39], a[136:139], v[148:151], v[36:39]\n"
    "global_load_dwordx4 a[248:251], v242, s[60:61] offset:2048 sc1\n"
    "v_cvt_pk_bf16_f32 v156, v136, v137\n"
    "v_mfma_f32_16x16x32_bf16 v[52:55], a[140:143], v[148:151], v[52:55]\n"
    "v_exp_f32 v138, v138\n"
    "v_mfma_f32_16x16x32_bf16 v[68:71], a[144:147], v[148:151], v[68:71]\n"
    "ds_read_b64_tr_b16 a[160:161], v184 offset:40960\n"
    "global_load_dwordx4 a[252:255], v242, s[60:61] offset:3072 sc1\n"
    "v_mfma_f32_16x16x32_bf16 v[84:87], a[148:151], v[148:151], v[84:87]\n"
    "v_exp_f32 v139, v139\n"
    "v_mfma_f32_16x16x32_bf16 v[100:103], a[152:155], v[148:151], v[100:103]\n"
    "ds_read_b64_tr_b16 a[162:163], v184 offset:45056\n"
    "v_cvt_pk_bf16_f32 v157, v138, v139\n"
    "v_mfma_f32_16x16x32_bf16 v[116:119], a[156:159], v[148:151], v[116:119]\n"
    "v_exp_f32 v140, v140\n"
    "v_mfma_f32_16x16x32_bf16 a[196:199], v[176:179], v[148:151], a[196:199]\n"
    "s_waitcnt lgkmcnt(1)\n"
    "ds_read_b64_tr_b16 a[164:165], v185 offset:40960\n"
    "v_mfma_f32_16x16x32_bf16 v[128:131], a[96:99], a[0:3], v[160:163]\n"
    "v_exp_f32 v141, v141\n"
    "v_mfma_f32_16x16x32_bf16 v[128:131], a[100:103], a[4:7], v[128:131]\n"
    "v_cvt_pk_bf16_f32 v158, v140, v141\n"
    "v_mfma_f32_16x16x32_bf16 v[128:131], a[104:107], a[8:11], v[128:131]\n"
    "v_exp_f32 v142, v142\n"
    "v_mfma_f32_16x16x32_bf16 v[128:131], a[108:111], a[12:15], v[128:131]\n"
    "ds_read_b64_tr_b16 a[166:167], v185 offset:45056\n"
    "v_mfma_f32_16x16x32_bf16 v[132:135], a[112:115], a[0:3], v[160:163]\n"
    "v_exp_f32 v143, v143\n"
    "v_mfma_f32_16x16x32_bf16 v[132:135], a[116:119], a[4:7], v[132:135]\n"
    "ds_read_b64_tr_b16 a[168:169], v186 offset:40960\n"
    "v_cvt_pk_bf16_f32 v159, v142, v143\n"
    "v_mfma_f32_16x16x32_bf16 v[132:135], a[120:123], a[8:11], v[132:135]\n"
    "v_mfma_f32_16x16x32_bf16 v[132:135], a[124:127], a[12:15], v[132:135]\n"
    "v_exp_f32 v128, v128\n"
    "v_mfma_f32_16x16x32_bf16 v[8:11], a[128:131], v[152:155], v[8:11]\n"
    "ds_read_b64_tr_b16 a[170:171], v186 offset:45056\n"
    "v_mfma_f32_16x16x32_bf16 v[24:27], a[132:135], v[152:155], v[24:27]\n"
    "v_exp_f32 v129, v129\n"
    "v_mfma_f32_16x16x32_bf16 v[40:43], a[136:139], v[152:155], v[40:43]\n"
    "ds_read_b64_tr_b16 a[172:173], v187 offset:40960\n"
    "v_cvt_pk_bf16_f32 v144, v128, v129\n"
    "v_mfma_f32_16x16x32_bf16 v[56:59], a[140:143], v[152:155], v[56:59]\n"
    "v_exp_f32 v130, v130\n"
    "v_mfma_f32_16x16x32_bf16 v[72:75], a[144:147], v[152:155], v[72:75]\n"
    "ds_read_b64_tr_b16 a[174:175], v187 offset:45056\n"
    "v_mfma_f32_16x16x32_bf16 v[88:91], a[148:151], v[152:155], v[88:91]\n"
    "v_exp_f32 v131, v131\n"
    "v_mfma_f32_16x16x32_bf16 v[104:107], a[152:155], v[152:155], v[104:107]\n"
    "ds_read_b64_tr_b16 a[176:177], v188 offset:40960\n"
    "v_cvt_pk_bf16_f32 v145, v130, v131\n"
    "v_mfma_f32_16x16x32_bf16 v[120:123], a[156:159], v[152:155], v[120:123]\n"
    "v_exp_f32 v132, v132\n"
    "v_mfma_f32_16x16x32_bf16 a[200:203], v[176:179], v[152:155], a[200:203]\n"
    "ds_read_b64_tr_b16 a[178:179], v188 offset:45056\n"
    "v_mfma_f32_16x16x32_bf16 v[136:139], a[96:99], a[16:19], v[164:167]\n"
    "v_exp_f32 v133, v133\n"
    "v_mfma_f32_16x16x32_bf16 v[136:139], a[100:103], a[20:23], v[136:139]\n"
    "v_cvt_pk_bf16_f32 v146, v132, v133\n"
    "v_mfma_f32_16x16x32_bf16 v[136:139], a[104:107], a[24:27], v[136:139]\n"
    "v_exp_f32 v134, v134\n"
    "v_mfma_f32_16x16x32_bf16 v[136:139], a[108:111], a[28:31], v[136:139]\n"
    "ds_read_b64_tr_b16 a[180:181], v189 offset:40960\n"
    "v_mfma_f32_16x16x32_bf16 v[140:143], a[112:115], a[16:19], v[164:167]\n"
    "v_exp_f32 v135, v135\n"
    "v_mfma_f32_16x16x32_bf16 v[140:143], a[116:119], a[20:23], v[140:143]\n"
    "ds_read_b64_tr_b16 a[182:183], v189 offset:45056\n"
    "v_cvt_pk_bf16_f32 v147, v134, v135\n"
    "v_mfma_f32_16x16x32_bf16 v[140:143], a[120:123], a[24:27], v[140:143]\n"
    "v_mfma_f32_16x16x32_bf16 v[140:143], a[124:127], a[28:31], v[140:143]\n"
    "v_exp_f32 v136, v136\n"
    "v_mfma_f32_16x16x32_bf16 v[12:15], a[128:131], v[156:159], v[12:15]\n"
    "ds_read_b64_tr_b16 a[184:185], v190 offset:40960\n"
    "v_mfma_f32_16x16x32_bf16 v[28:31], a[132:135], v[156:159], v[28:31]\n"
    "v_exp_f32 v137, v137\n"
    "v_mfma_f32_16x16x32_bf16 v[44:47], a[136:139], v[156:159], v[44:47]\n"
    "ds_read_b64_tr_b16 a[186:187], v190 offset:45056\n"
    "v_cvt_pk_bf16_f32 v148, v136, v137\n"
    "v_mfma_f32_16x16x32_bf16 v[60:63], a[140:143], v[156:159], v[60:63]\n"
    "v_exp_f32 v138, v138\n"
    "v_mfma_f32_16x16x32_bf16 v[76:79], a[144:147], v[156:159], v[76:79]\n"
    "ds_read_b64_tr_b16 a[188:189], v191 offset:40960\n"
    "v_mfma_f32_16x16x32_bf16 v[92:95], a[148:151], v[156:159], v[92:95]\n"
    "v_exp_f32 v139, v139\n"
    "v_mfma_f32_16x16x32_bf16 v[108:111], a[152:155], v[156:159], v[108:111]\n"
    "ds_read_b64_tr_b16 a[190:191], v191 offset:45056\n"
    "v_cvt_pk_bf16_f32 v149, v138, v139\n"
    "v_mfma_f32_16x16x32_bf16 v[124:127], a[156:159], v[156:159], v[124:127]\n"
    "v_exp_f32 v140, v140\n"
    "v_mfma_f32_16x16x32_bf16 a[204:207], v[176:179], v[156:159], a[204:207]\n"
    "v_mfma_f32_16x16x32_bf16 v[128:131], a[96:99], a[32:35], v[168:171]\n"
    "v_exp_f32 v141, v141\n"
    "v_mfma_f32_16x16x32_bf16 v[128:131], a[100:103], a[36:39], v[128:131]\n"
    "v_cvt_pk_bf16_f32 v150, v140, v141\n"
    "v_mfma_f32_16x16x32_bf16 v[128:131], a[104:107], a[40:43], v[128:131]\n"
    "v_exp_f32 v142, v142\n"
    "v_mfma_f32_16x16x32_bf16 v[128:131], a[108:111], a[44:47], v[128:131]\n"
    "ds_read_b128 a[64:67], v180 offset:49152\n"
    "v_mfma_f32_16x16x32_bf16 v[132:135], a[112:115], a[32:35], v[168:171]\n"
    "v_exp_f32 v143, v143\n"
    "v_mfma_f32_16x16x32_bf16 v[132:135], a[116:119], a[36:39], v[132:135]\n"
    "ds_read_b128 a[68:71], v181 offset:49152\n"
    "v_cvt_pk_bf16_f32 v151, v142, v143\n"
    "v_mfma_f32_16x16x32_bf16 v[132:135], a[120:123], a[40:43], v[132:135]\n"
    "v_mfma_f32_16x16x32_bf16 v[132:135], a[124:127], a[44:47], v[132:135]\n"
    "s_waitcnt lgkmcnt(1)\n"
    "v_exp_f32 v128, v128\n"
    "v_mfma_f32_16x16x32_bf16 v[0:3], a[160:163], v[144:147], v[0:3]\n"
    "ds_read_b128 a[72:75], v182 offset:49152\n"
    "v_mfma_f32_16x16x32_bf16 v[16:19], a[164:167], v[144:147], v[16:19]\n"
    "v_exp_f32 v129, v129\n"
    "v_mfma_f32_16x16x32_bf16 v[32:35], a[168:171], v[144:147], v[32:35]\n"
    "v_cvt_pk_bf16_f32 v152, v128, v129\n"
    "v_mfma_f32_16x16x32_bf16 v[48:51], a[172:175], v[144:147], v[48:51]\n"
    "v_exp_f32 v130, v130\n"
    "v_mfma_f32_16x16x32_bf16 v[64:67], a[176:179], v[144:147], v[64:67]\n"
    "ds_read_b128 a[76:79], v183 offset:49152\n"
    "v_mfma_f32_16x16x32_bf16 v[80:83], a[180:183], v[144:147], v[80:83]\n"
    "v_exp_f32 v131, v131\n"
    "v_mfma_f32_16x16x32_bf16 v[96:99], a[184:187], v[144:147], v[96:99]\n"
    "ds_read_b128 a[80:83], v180 offset:53248\n"
    "v_cvt_pk_bf16_f32 v153, v130, v131\n"
    "v_mfma_f32_16x16x32_bf16 v[112:115], a[188:191], v[144:147], v[112:115]\n"
    "v_exp_f32 v132, v132\n"
    "v_mfma_f32_16x16x32_bf16 a[192:195], v[176:179], v[144:147], a[192:195]\n"
    "s_memrealtime s[64:65]\n"
    "v_mfma_f32_16x16x32_bf16 v[136:139], a[96:99], a[48:51], v[172:175]\n"
    "v_exp_f32 v133, v133\n"
    "v_mfma_f32_16x16x32_bf16 v[136:139], a[100:103], a[52:55], v[136:139]\n"
    "ds_read_b128 a[84:87], v181 offset:53248\n"
    "v_cvt_pk_bf16_f32 v154, v132, v133\n"
    "v_mfma_f32_16x16x32_bf16 v[136:139], a[104:107], a[56:59], v[136:139]\n"
    "v_exp_f32 v134, v134\n"
    "v_mfma_f32_16x16x32_bf16 v[136:139], a[108:111], a[60:63], v[136:139]\n"
    "ds_read_b128 a[88:91], v182 offset:53248\n"
    "v_mfma_f32_16x16x32_bf16 v[140:143], a[112:115], a[48:51], v[172:175]\n"
    "v_exp_f32 v135, v135\n"
    "v_mfma_f32_16x16x32_bf16 v[140:143], a[116:119], a[52:55], v[140:143]\n"
    "v_cvt_pk_bf16_f32 v155, v134, v135\n"
    "v_mfma_f32_16x16x32_bf16 v[140:143], a[120:123], a[56:59], v[140:143]\n"
    "ds_read_b128 a[92:95], v183 offset:53248\n"
    "v_mfma_f32_16x16x32_bf16 v[140:143], a[124:127], a[60:63], v[140:143]\n"
    "v_exp_f32 v136, v136\n"
    "v_mfma_f32_16x16x32_bf16 v[4:7], a[160:163], v[148:151], v[4:7]\n"
    "v_mfma_f32_16x16x32_bf16 v[20:23], a[164:167], v[148:151], v[20:23]\n"
    "v_exp_f32 v137, v137\n"
    "v_mfma_f32_16x16x32_bf16 v[36:39], a[168:171], v[148:151], v[36:39]\n"
    "v_cvt_pk_bf16_f32 v156, v136, v137\n"
    "v_mfma_f32_16x16x32_bf16 v[52:55], a[172:175], v[148:151], v[52:55]\n"
    "v_exp_f32 v138, v138\n"
    "v_mfma_f32_16x16x32_bf16 v[68:71], a[176:179], v[148:151], v[68:71]\n"
    "v_mfma_f32_16x16x32_bf16 v[84:87], a[180:183], v[148:151], v[84:87]\n"
    "v_exp_f32 v139, v139\n"
    "v_mfma_f32_16x16x32_bf16 v[100:103], a[184:187], v[148:151], v[100:103]\n"
    "v_cvt_pk_bf16_f32 v157, v138, v139\n"
    "v_mfma_f32_16x16x32_bf16 v[116:119], a[188:191], v[148:151], v[116:119]\n"
    "v_exp_f32 v140, v140\n"
    "v_mfma_f32_16x16x32_bf16 a[196:199], v[176:179], v[148:151], a[196:199]\n"
    "v_mfma_f32_16x16x32_bf16 v[128:131], a[64:67], a[0:3], v[160:163]\n"
    "s_waitcnt lgkmcnt(0)\n"
    "s_waitcnt vmcnt(32)\n"
    "v_exp_f32 v141, v141\n"
    "v_mfma_f32_16x16x32_bf16 v[128:131], a[68:71], a[4:7], v[128:131]\n"
    "s_barrier\n"
    "v_cvt_pk_bf16_f32 v158, v140, v141\n"
    "v_mfma_f32_16x16x32_bf16 v[128:131], a[72:75], a[8:11], v[128:131]\n"
    "v_exp_f32 v142, v142\n"
    "v_mfma_f32_16x16x32_bf16 v[128:131], a[76:79], a[12:15], v[128:131]\n"
    "ds_read_b64_tr_b16 a[128:129], v184 offset:49152\n"
    "v_mfma_f32_16x16x32_bf16 v[132:135], a[80:83], a[0:3], v[160:163]\n"
    "s_add_u32 m0, s30, 0x8000\n"
    "v_exp_f32 v143, v143\n"
    "v_mfma_f32_16x16x32_bf16 v[132:135], a[84:87], a[4:7], v[132:135]\n"
    "s_mov_b32 s43, 114688\n"
    "ds_read_b64_tr_b16 a[130:131], v184 offset:53248\n"
    "v_cvt_pk_bf16_f32 v159, v142, v143\n"
    "v_mfma_f32_16x16x32_bf16 v[132:135], a[88:91], a[8:11], v[132:135]\n"
    "ds_read_b64_tr_b16 a[132:133], v185 offset:49152\n"
    "buffer_load_dwordx4 v237, s[52:55], s43 offen offset:0 sc1 lds\n"
    "v_mfma_f32_16x16x32_bf16 v[132:135], a[92:95], a[12:15], v[132:135]\n"
    "v_exp_f32 v128, v128\n"
    "v_mfma_f32_16x16x32_bf16 v[8:11], a[160:163], v[152:155], v[8:11]\n"
    "ds_read_b64_tr_b16 a[134:135], v185 offset:53248\n"
    "buffer_load_dwordx4 v237, s[52:55], s43 offen offset:1024 sc1 lds\n"
    "v_mfma_f32_16x16x32_bf16 v[24:27], a[164:167], v[152:155], v[24:27]\n"
    "v_exp_f32 v129, v129\n"
    "v_mfma_f32_16x16x32_bf16 v[40:43], a[168:171], v[152:155], v[40:43]\n"
    "buffer_load_dwordx4 v237, s[52:55], s43 offen offset:2048 sc1 lds\n"
    "v_cvt_pk_bf16_f32 v144, v128, v129\n"
    "v_mfma_f32_16x16x32_bf16 v[56:59], a[172:175], v[152:155], v[56:59]\n"
    "v_exp_f32 v130, v130\n"
    "v_mfma_f32_16x16x32_bf16 v[72:75], a[176:179], v[152:155], v[72:75]\n"
    "ds_read_b64_tr_b16 a[136:137], v186 offset:49152\n"
    "buffer_load_dwordx4 v237, s[52:55], s43 offen offset:3072 sc1 lds\n"
    "v_mfma_f32_16x16x32_bf16 v[88:91], a[180:183], v[152:155], v[88:91]\n"
    "s_add_u32 m0, s30, 0x14000\n"
    "v_exp_f32 v131, v131\n"
    "v_mfma_f32_16x16x32_bf16 v[104:107], a[184:187], v[152:155], v[104:107]\n"
    "s_mov_b32 s43, 118784\n"
    "ds_read_b64_tr_b16 a[138:139], v186 offset:53248\n"
    "v_cvt_pk_bf16_f32 v145, v130, v131\n"
    "v_mfma_f32_16x16x32_bf16 v[120:123], a[188:191], v[152:155], v[120:123]\n"
    "v_exp_f32 v132, v132\n"
    "v_mfma_f32_16x16x32_bf16 a[200:203], v[176:179], v[152:155], a[200:203]\n"
    "ds_read_b64_tr_b16 a[140:141], v187 offset:49152\n"
    "buffer_load_dwordx4 v237, s[52:55], s43 offen offset:0 sc1 lds\n"
    "v_mfma_f32_16x16x32_bf16 v[136:139], a[64:67], a[16:19], v[164:167]\n"
    "v_exp_f32 v133, v133\n"
    "v_mfma_f32_16x16x32_bf16 v[136:139], a[68:71], a[20:23], v[136:139]\n"
    "ds_read_b64_tr_b16 a[142:143], v187 offset:53248\n"
    "v_cvt_pk_bf16_f32 v146, v132, v133\n"
    "v_mfma_f32_16x16x32_bf16 v[136:139], a[72:75], a[24:27], v[136:139]\n"
    "v_exp_f32 v134, v134\n"
    "v_mfma_f32_16x16x32_bf16 v[136:139], a[76:79], a[28:31], v[136:139]\n"
    "ds_read_b64_tr_b16 a[144:145], v188 offset:49152\n"
    "buffer_load_dwordx4 v237, s[52:55], s43 offen offset:1024 sc1 lds\n"
    "v_mfma_f32_16x16x32_bf16 v[140:143], a[80:83], a[16:19], v[164:167]\n"
    "v_exp_f32 v135, v135\n"
    "v_mfma_f32_16x16x32_bf16 v[140:143], a[84:87], a[20:23], v[140:143]\n"
    "ds_read_b64_tr_b16 a[146:147], v188 offset:53248\n"
    "v_cvt_pk_bf16_f32 v147, v134, v135\n"
    "v_mfma_f32_16x16x32_bf16 v[140:143], a[88:91], a[24:27], v[140:143]\n"
    "buffer_load_dwordx4 v237, s[52:55], s43 offen offset:2048 sc1 lds\n"
    "v_mfma_f32_16x16x32_bf16 v[140:143], a[92:95], a[28:31], v[140:143]\n"
    "v_exp_f32 v136, v136\n"
    "v_mfma_f32_16x16x32_bf16 v[12:15], a[160:163], v[156:159], v[12:15]\n"
    "ds_read_b64_tr_b16 a[148:149], v189 offset:49152\n"
    "buffer_load_dwordx4 v237, s[52:55], s43 offen offset:3072 sc1 lds\n"
    "v_mfma_f32_16x16x32_bf16 v[28:31], a[164:167], v[156:159], v[28:31]\n"
    "v_exp_f32 v137, v137\n"
    "v_mfma_f32_16x16x32_bf16 v[44:47], a[168:171], v[156:159], v[44:47]\n"
    "ds_read_b64_tr_b16 a[150:151], v189 offset:53248\n"
    "v_cvt_pk_bf16_f32 v148, v136, v137\n"
    "v_mfma_f32_16x16x32_bf16 v[60:63], a[172:175], v[156:159], v[60:63]\n"
    "v_exp_f32 v138, v138\n"
    "v_mfma_f32_16x16x32_bf16 v[76:79], a[176:179], v[156:159], v[76:79]\n"
    "ds_read_b64_tr_b16 a[152:153], v190 offset:49152\n"
    "v_mfma_f32_16x16x32_bf16 v[92:95], a[180:183], v[156:159], v[92:95]\n"
    "v_exp_f32 v139, v139\n"
    "v_mfma_f32_16x16x32_bf16 v[108:111], a[184:187], v[156:159], v[108:111]\n"
    "ds_read_b64_tr_b16 a[154:155], v190 offset:53248\n"
    "v_cvt_pk_bf16_f32 v149, v138, v139\n"
    "v_mfma_f32_16x16x32_bf16 v[124:127], a[188:191], v[156:159], v[124:127]\n"
    "v_exp_f32 v140, v140\n"
    "v_mfma_f32_16x16x32_bf16 a[204:207], v[176:179], v[156:159], a[204:207]\n"
    "ds_read_b64_tr_b16 a[156:157], v191 offset:49152\n"
    "v_mfma_f32_16x16x32_bf16 v[128:131], a[64:67], a[32:35], v[168:171]\n"
    "v_exp_f32 v141, v141\n"
    "v_mfma_f32_16x16x32_bf16 v[128:131], a[68:71], a[36:39], v[128:131]\n"
    "ds_read_b64_tr_b16 a[158:159], v191 offset:53248\n"
    "v_cvt_pk_bf16_f32 v150, v140, v141\n"
    "v_mfma_f32_16x16x32_bf16 v[128:131], a[72:75], a[40:43], v[128:131]\n"
    "v_exp_f32 v142, v142\n"
    "v_mfma_f32_16x16x32_bf16 v[128:131], a[76:79], a[44:47], v[128:131]\n"
    "v_mfma_f32_16x16x32_bf16 v[132:135], a[80:83], a[32:35], v[168:171]\n"
    "v_exp_f32 v143, v143\n"
    "v_mfma_f32_16x16x32_bf16 v[132:135], a[84:87], a[36:39], v[132:135]\n"
    "v_cvt_pk_bf16_f32 v151, v142, v143\n"
    "v_mfma_f32_16x16x32_bf16 v[132:135], a[88:91], a[40:43], v[132:135]\n"
    "v_mfma_f32_16x16x32_bf16 v[132:135], a[92:95], a[44:47], v[132:135]\n"
    "s_waitcnt lgkmcnt(0)\n"
    "s_nop 0\n"
    "v_exp_f32 v128, v128\n"
    "v_mfma_f32_16x16x32_bf16 v[0:3], a[128:131], v[144:147], v[0:3]\n"
    "ds_read_b128 a[96:99], v180 offset:57344\n"
    "v_mfma_f32_16x16x32_bf16 v[16:19], a[132:135], v[144:147], v[16:19]\n"
    "v_exp_f32 v129, v129\n"
    "v_mfma_f32_16x16x32_bf16 v[32:35], a[136:139], v[144:147], v[32:35]\n"
    "ds_read_b128 a[100:103], v181 offset:57344\n"
    "v_cvt_pk_bf16_f32 v152, v128, v129\n"
    "v_mfma_f32_16x16x32_bf16 v[48:51], a[140:143], v[144:147], v[48:51]\n"
    "v_exp_f32 v130, v130\n"
    "v_mfma_f32_16x16x32_bf16 v[64:67], a[144:147], v[144:147], v[64:67]\n"
    "ds_read_b128 a[104:107], v182 offset:57344\n"
    "v_mfma_f32_16x16x32_bf16 v[80:83], a[148:151], v[144:147], v[80:83]\n"
    "v_exp_f32 v131, v131\n"
    "v_mfma_f32_16x16x32_bf16 v[96:99], a[152:155], v[144:147], v[96:99]\n"
    "ds_read_b128 a[108:111], v183 offset:57344\n"
    "v_cvt_pk_bf16_f32 v153, v130, v131\n"
    "v_mfma_f32_16x16x32_bf16 v[112:115], a[156:159], v[144:147], v[112:115]\n"
    "v_exp_f32 v132, v132\n"
    "v_mfma_f32_16x16x32_bf16 a[192:195], v[176:179], v[144:147], a[192:195]\n"
    "ds_read_b128 a[112:115], v180 offset:61440\n"
    "v_mfma_f32_16x16x32_bf16 v[136:139], a[64:67], a[48:51], v[172:175]\n"
    "v_exp_f32 v133, v133\n"
    "v_mfma_f32_16x16x32_bf16 v[136:139], a[68:71], a[52:55], v[136:139]\n"
    "ds_read_b128 a[116:119], v181 offset:61440\n"
    "v_cvt_pk_bf16_f32 v154, v132, v133\n"
    "v_mfma_f32_16x16x32_bf16 v[136:139], a[72:75], a[56:59], v[136:139]\n"
    "v_exp_f32 v134, v134\n"
    "v_mfma_f32_16x16x32_bf16 v[136:139], a[76:79], a[60:63], v[136:139]\n"
    "v_mfma_f32_16x16x32_bf16 v[140:143], a[80:83], a[48:51], v[172:175]\n"
    "v_exp_f32 v135, v135\n"
    "v_mfma_f32_16x16x32_bf16 v[140:143], a[84:87], a[52:55], v[140:143]\n"
    "ds_read_b128 a[120:123], v182 offset:61440\n"
    "v_cvt_pk_bf16_f32 v155, v134, v135\n"
    "v_mfma_f32_16x16x32_bf16 v[140:143], a[88:91], a[56:59], v[140:143]\n"
    "ds_read_b128 a[124:127], v183 offset:61440\n"
    "v_mfma_f32_16x16x32_bf16 v[140:143], a[92:95], a[60:63], v[140:143]\n"
    "v_exp_f32 v136, v136\n"
    "v_mfma_f32_16x16x32_bf16 v[4:7], a[128:131], v[148:151], v[4:7]\n"
    "v_mfma_f32_16x16x32_bf16 v[20:23], a[132:135], v[148:151], v[20:23]\n"
    "v_exp_f32 v137, v137\n"
    "v_mfma_f32_16x16x32_bf16 v[36:39], a[136:139], v[148:151], v[36:39]\n"
    "global_load_dwordx4 a[64:67], v243, s[60:61] offset:0 sc1\n"
    "v_cvt_pk_bf16_f32 v156, v136, v137\n"
    "v_mfma_f32_16x16x32_bf16 v[52:55], a[140:143], v[148:151], v[52:55]\n"
    "v_exp_f32 v138, v138\n"
    "v_mfma_f32_16x16x32_bf16 v[68:71], a[144:147], v[148:151], v[68:71]\n"
    "ds_read_b64_tr_b16 a[160:161], v184 offset:57344\n"
    "global_load_dwordx4 a[68:71], v243, s[60:61] offset:1024 sc1\n"
    "v_mfma_f32_16x16x32_bf16 v[84:87], a[148:151], v[148:151], v[84:87]\n"
    "v_exp_f32 v139, v139\n"
    "v_mfma_f32_16x16x32_bf16 v[100:103], a[152:155], v[148:151], v[100:103]\n"
    "ds_read_b64_tr_b16 a[162:163], v184 offset:61440\n"
    "v_cvt_pk_bf16_f32 v157, v138, v139\n"
    "v_mfma_f32_16x16x32_bf16 v[116:119], a[156:159], v[148:151], v[116:119]\n"
    "v_exp_f32 v140, v140\n"
    "v_mfma_f32_16x16x32_bf16 a[196:199], v[176:179], v[148:151], a[196:199]\n"
    "s_waitcnt lgkmcnt(1)\n"
    "ds_read_b64_tr_b16 a[164:165], v185 offset:57344\n"
    "global_load_dwordx4 a[72:75], v243, s[60:61] offset:2048 sc1\n"
    "v_mfma_f32_16x16x32_bf16 v[128:131], a[96:99], a[0:3], v[160:163]\n"
    "v_exp_f32 v141, v141\n"
    "v_mfma_f32_16x16x32_bf16 v[128:131], a[100:103], a[4:7], v[128:131]\n"
    "global_load_dwordx4 a[76:79], v243, s[60:61] offset:3072 sc1\n"
    "v_cvt_pk_bf16_f32 v158, v140, v141\n"
    "v_mfma_f32_16x16x32_bf16 v[128:131], a[104:107], a[8:11], v[128:131]\n"
    "s_add_u32 s60, s60, 16384\n"
    "s_addc_u32 s61, s61, 0\n"
    "v_exp_f32 v142, v142\n"
    "v_mfma_f32_16x16x32_bf16 v[128:131], a[108:111], a[12:15], v[128:131]\n"
    "ds_read_b64_tr_b16 a[166:167], v185 offset:61440\n"
    "global_load_dwordx4 a[80:83], v240, s[60:61] offset:0 sc1\n"
    "v_mfma_f32_16x16x32_bf16 v[132:135], a[112:115], a[0:3], v[160:163]\n"
    "v_exp_f32 v143, v143\n"
    "v_mfma_f32_16x16x32_bf16 v[132:135], a[116:119], a[4:7], v[132:135]\n"
    "ds_read_b64_tr_b16 a[168:169], v186 offset:57344\n"
    "v_cvt_pk_bf16_f32 v159, v142, v143\n"
    "v_mfma_f32_16x16x32_bf16 v[132:135], a[120:123], a[8:11], v[132:135]\n"
    "global_load_dwordx4 a[84:87], v240, s[60:61] offset:1024 sc1\n"
    "v_mfma_f32_16x16x32_bf16 v[132:135], a[124:127], a[12:15], v[132:135]\n"
    "v_exp_f32 v128, v128\n"
    "v_mfma_f32_16x16x32_bf16 v[8:11], a[128:131], v[152:155], v[8:11]\n"
    "ds_read_b64_tr_b16 a[170:171], v186 offset:61440\n"
    "global_load_dwordx4 a[88:91], v240, s[60:61] offset:2048 sc1\n"
    "v_mfma_f32_16x16x32_bf16 v[24:27], a[132:135], v[152:155], v[24:27]\n"
    "v_exp_f32 v129, v129\n"
    "v_mfma_f32_16x16x32_bf16 v[40:43], a[136:139], v[152:155], v[40:43]\n"
    "ds_read_b64_tr_b16 a[172:173], v187 offset:57344\n"
    "v_cvt_pk_bf16_f32 v144, v128, v129\n"
    "v_mfma_f32_16x16x32_bf16 v[56:59], a[140:143], v[152:155], v[56:59]\n"
    "v_exp_f32 v130, v130\n"
    "v_mfma_f32_16x16x32_bf16 v[72:75], a[144:147], v[152:155], v[72:75]\n"
    "ds_read_b64_tr_b16 a[174:175], v187 offset:61440\n"
    "global_load_dwordx4 a[92:95], v240, s[60:61] offset:3072 sc1\n"
    "v_mfma_f32_16x16x32_bf16 v[88:91], a[148:151], v[152:155], v[88:91]\n"
    "v_exp_f32 v131, v131\n"
    "v_mfma_f32_16x16x32_bf16 v[104:107], a[152:155], v[152:155], v[104:107]\n"
    "ds_read_b64_tr_b16 a[176:177], v188 offset:57344\n"
    "v_cvt_pk_bf16_f32 v145, v130, v131\n"
    "v_mfma_f32_16x16x32_bf16 v[120:123], a[156:159], v[152:155], v[120:123]\n"
    "v_exp_f32 v132, v132\n"
    "v_mfma_f32_16x16x32_bf16 a[200:203], v[176:179], v[152:155], a[200:203]\n"
    "ds_read_b64_tr_b16 a[178:179], v188 offset:61440\n"
    "global_load_dwordx4 a[0:3], v241, s[60:61] offset:0 sc1\n"
    "v_mfma_f32_16x16x32_bf16 v[136:139], a[96:99], a[16:19], v[164:167]\n"
    "v_exp_f32 v133, v133\n"
    "v_mfma_f32_16x16x32_bf16 v[136:139], a[100:103], a[20:23], v[136:139]\n"
    "global_load_dwordx4 a[4:7], v241, s[60:61] offset:1024 sc1\n"
    "v_cvt_pk_bf16_f32 v146, v132, v133\n"
    "v_mfma_f32_16x16x32_bf16 v[136:139], a[104:107], a[24:27], v[136:139]\n"
    "v_exp_f32 v134, v134\n"
    "v_mfma_f32_16x16x32_bf16 v[136:139], a[108:111], a[28:31], v[136:139]\n"
    "ds_read_b64_tr_b16 a[180:181], v189 offset:57344\n"
    "global_load_dwordx4 a[8:11], v241, s[60:61] offset:2048 sc1\n"
    "v_mfma_f32_16x16x32_bf16 v[140:143], a[112:115], a[16:19], v[164:167]\n"
    "v_exp_f32 v135, v135\n"
    "v_mfma_f32_16x16x32_bf16 v[140:143], a[116:119], a[20:23], v[140:143]\n"
    "ds_read_b64_tr_b16 a[182:183], v189 offset:61440\n"
    "v_cvt_pk_bf16_f32 v147, v134, v135\n"
    "v_mfma_f32_16x16x32_bf16 v[140:143], a[120:123], a[24:27], v[140:143]\n"
    "global_load_dwordx4 a[12:15], v241, s[60:61] offset:3072 sc1\n"
    "v_mfma_f32_16x16x32_bf16 v[140:143], a[124:127], a[28:31], v[140:143]\n"
    "v_exp_f32 v136, v136\n"
    "v_mfma_f32_16x16x32_bf16 v[12:15], a[128:131], v[156:159], v[12:15]\n"
    "ds_read_b64_tr_b16 a[184:185], v190 offset:57344\n"
    "v_mfma_f32_16x16x32_bf16 v[28:31], a[132:135], v[156:159], v[28:31]\n"
    "v_exp_f32 v137, v137\n"
    "v_mfma_f32_16x16x32_bf16 v[44:47], a[136:139], v[156:159], v[44:47]\n"
    "ds_read_b64_tr_b16 a[186:187], v190 offset:61440\n"
    "v_cvt_pk_bf16_f32 v148, v136, v137\n"
    "v_mfma_f32_16x16x32_bf16 v[60:63], a[140:143], v[156:159], v[60:63]\n"
    "v_exp_f32 v138, v138\n"
    "v_mfma_f32_16x16x32_bf16 v[76:79], a[144:147], v[156:159], v[76:79]\n"
    "ds_read_b64_tr_b16 a[188:189], v191 offset:57344\n"
    "global_load_dwordx4 a[16:19], v242, s[60:61] offset:0 sc1\n"
    "v_mfma_f32_16x16x32_bf16 v[92:95], a[148:151], v[156:159], v[92:95]\n"
    "v_exp_f32 v139, v139\n"
    "v_mfma_f32_16x16x32_bf16 v[108:111], a[152:155], v[156:159], v[108:111]\n"
    "ds_read_b64_tr_b16 a[190:191], v191 offset:61440\n"
    "v_cvt_pk_bf16_f32 v149, v138, v139\n"
    "v_mfma_f32_16x16x32_bf16 v[124:127], a[156:159], v[156:159], v[124:127]\n"
    "v_exp_f32 v140, v140\n"
    "v_mfma_f32_16x16x32_bf16 a[204:207], v[176:179], v[156:159], a[204:207]\n"
    "global_load_dwordx4 a[20:23], v242, s[60:61] offset:1024 sc1\n"
    "v_mfma_f32_16x16x32_bf16 v[128:131], a[96:99], a[32:35], v[168:171]\n"
    "v_exp_f32 v141, v141\n"
    "v_mfma_f32_16x16x32_bf16 v[128:131], a[100:103], a[36:39], v[128:131]\n"
    "global_load_dwordx4 a[24:27], v242, s[60:61] offset:2048 sc1\n"
    "v_cvt_pk_bf16_f32 v150, v140, v141\n"
    "v_mfma_f32_16x16x32_bf16 v[128:131], a[104:107], a[40:43], v[128:131]\n"
    "v_exp_f32 v142, v142\n"
    "v_mfma_f32_16x16x32_bf16 v[128:131], a[108:111], a[44:47], v[128:131]\n"
    "global_load_dwordx4 a[28:31], v242, s[60:61] offset:3072 sc1\n"
    "v_mfma_f32_16x16x32_bf16 v[132:135], a[112:115], a[32:35], v[168:171]\n"
    "v_exp_f32 v143, v143\n"
    "v_mfma_f32_16x16x32_bf16 v[132:135], a[116:119], a[36:39], v[132:135]\n"
    "global_load_dwordx4 a[128:131], v243, s[60:61] offset:0 sc1\n"
    "v_cvt_pk_bf16_f32 v151, v142, v143\n"
    "v_mfma_f32_16x16x32_bf16 v[132:135], a[120:123], a[40:43], v[132:135]\n"
    "global_load_dwordx4 a[132:135], v243, s[60:61] offset:1024 sc1\n"
    "v_mfma_f32_16x16x32_bf16 v[132:135], a[124:127], a[44:47], v[132:135]\n"
    "s_waitcnt lgkmcnt(0)\n"
    "v_exp_f32 v128, v128\n"
    "v_mfma_f32_16x16x32_bf16 v[0:3], a[160:163], v[144:147], v[0:3]\n"
    "global_load_dwordx4 a[136:139], v243, s[60:61] offset:2048 sc1\n"
    "v_mfma_f32_16x16x32_bf16 v[16:19], a[164:167], v[144:147], v[16:19]\n"
    "v_exp_f32 v129, v129\n"
    "v_mfma_f32_16x16x32_bf16 v[32:35], a[168:171], v[144:147], v[32:35]\n"
    "global_load_dwordx4 a[140:143], v243, s[60:61] offset:3072 sc1\n"
    "v_cvt_pk_bf16_f32 v152, v128, v129\n"
    "v_mfma_f32_16x16x32_bf16 v[48:51], a[172:175], v[144:147], v[48:51]\n"
    "s_add_u32 s60, s60, 16384\n"
    "s_addc_u32 s61, s61, 0\n"
    "v_exp_f32 v130, v130\n"
    "v_mfma_f32_16x16x32_bf16 v[64:67], a[176:179], v[144:147], v[64:67]\n"
    "s_waitcnt vmcnt(56)\n"
    "global_load_dwordx4 a[144:147], v240, s[60:61] offset:0 sc1\n"
    "v_mfma_f32_16x16x32_bf16 v[80:83], a[180:183], v[144:147], v[80:83]\n"
    "v_exp_f32 v131, v131\n"
    "v_mfma_f32_16x16x32_bf16 v[96:99], a[184:187], v[144:147], v[96:99]\n"
    "global_load_dwordx4 a[148:151], v240, s[60:61] offset:1024 sc1\n"
    "v_cvt_pk_bf16_f32 v153, v130, v131\n"
    "v_mfma_f32_16x16x32_bf16 v[112:115], a[188:191], v[144:147], v[112:115]\n"
    "v_exp_f32 v132, v132\n"
    "v_mfma_f32_16x16x32_bf16 a[192:195], v[176:179], v[144:147], a[192:195]\n"
    "global_load_dwordx4 a[152:155], v240, s[60:61] offset:2048 sc1\n"
    "v_mfma_f32_16x16x32_bf16 v[136:139], a[96:99], a[48:51], v[172:175]\n"
    "v_exp_f32 v133, v133\n"
    "v_mfma_f32_16x16x32_bf16 v[136:139], a[100:103], a[52:55], v[136:139]\n"
    "global_load_dwordx4 a[156:159], v240, s[60:61] offset:3072 sc1\n"
    "v_cvt_pk_bf16_f32 v154, v132, v133\n"
    "v_mfma_f32_16x16x32_bf16 v[136:139], a[104:107], a[56:59], v[136:139]\n"
    "v_exp_f32 v134, v134\n"
    "v_mfma_f32_16x16x32_bf16 v[136:139], a[108:111], a[60:63], v[136:139]\n"
    "s_waitcnt vmcnt(56)\n"
    "global_load_dwordx4 a[32:35], v241, s[60:61] offset:0 sc1\n"
    "v_mfma_f32_16x16x32_bf16 v[140:143], a[112:115], a[48:51], v[172:175]\n"
    "v_exp_f32 v135, v135\n"
    "v_mfma_f32_16x16x32_bf16 v[140:143], a[116:119], a[52:55], v[140:143]\n"
    "global_load_dwordx4 a[36:39], v241, s[60:61] offset:1024 sc1\n"
    "v_cvt_pk_bf16_f32 v155, v134, v135\n"
    "v_mfma_f32_16x16x32_bf16 v[140:143], a[120:123], a[56:59], v[140:143]\n"
    "global_load_dwordx4 a[40:43], v241, s[60:61] offset:2048 sc1\n"
    "v_mfma_f32_16x16x32_bf16 v[140:143], a[124:127], a[60:63], v[140:143]\n"
    "v_exp_f32 v136, v136\n"
    "v_mfma_f32_16x16x32_bf16 v[4:7], a[160:163], v[148:151], v[4:7]\n"
    "global_load_dwordx4 a[44:47], v241, s[60:61] offset:3072 sc1\n"
    "v_mfma_f32_16x16x32_bf16 v[20:23], a[164:167], v[148:151], v[20:23]\n"
    "v_exp_f32 v137, v137\n"
    "v_mfma_f32_16x16x32_bf16 v[36:39], a[168:171], v[148:151], v[36:39]\n"
    "v_cvt_pk_bf16_f32 v156, v136, v137\n"
    "v_mfma_f32_16x16x32_bf16 v[52:55], a[172:175], v[148:151], v[52:55]\n"
    "v_exp_f32 v138, v138\n"
    "v_mfma_f32_16x16x32_bf16 v[68:71], a[176:179], v[148:151], v[68:71]\n"
    "v_mfma_f32_16x16x32_bf16 v[84:87], a[180:183], v[148:151], v[84:87]\n"
    "v_exp_f32 v139, v139\n"
    "v_mfma_f32_16x16x32_bf16 v[100:103], a[184:187], v[148:151], v[100:103]\n"
    "v_cvt_pk_bf16_f32 v157, v138, v139\n"
    "v_mfma_f32_16x16x32_bf16 v[116:119], a[188:191], v[148:151], v[116:119]\n"
    "v_exp_f32 v140, v140\n"
    "v_mfma_f32_16x16x32_bf16 a[196:199], v[176:179], v[148:151], a[196:199]\n"
    "v_mfma_f32_16x16x32_bf16 v[8:11], a[160:163], v[152:155], v[8:11]\n"
    "v_exp_f32 v141, v141\n"
    "s_nop 0\n"
    "v_cvt_pk_bf16_f32 v158, v140, v141\n"
    "v_exp_f32 v142, v142\n"
    "v_exp_f32 v143, v143\n"
    "s_nop 0\n"
    "v_cvt_pk_bf16_f32 v159, v142, v143\n"
    "v_mfma_f32_16x16x32_bf16 v[24:27], a[164:167], v[152:155], v[24:27]\n"
    "v_accvgpr_read_b32 v220, a192\n"
    "v_add_u32 v224, 0, v237\n"
    "v_rcp_f32 v212, v220\n"
    "s_nop 0\n"
    "v_mov_b32 v213, v212\n"
    "v_pk_mul_f32 v[0:1], v[0:1], v[212:213]\n"
    "v_mfma_f32_16x16x32_bf16 v[40:43], a[168:171], v[152:155], v[40:43]\n"
    "v_pk_mul_f32 v[2:3], v[2:3], v[212:213]\n"
    "v_pk_mul_f32 v[16:17], v[16:17], v[212:213]\n"
    "v_pk_mul_f32 v[18:19], v[18:19], v[212:213]\n"
    "v_cvt_pk_bf16_f32 v0, v0, v1\n"
    "v_cvt_pk_bf16_f32 v1, v2, v3\n"
    "v_mfma_f32_16x16x32_bf16 v[56:59], a[172:175], v[152:155], v[56:59]\n"
    "v_cvt_pk_bf16_f32 v2, v16, v17\n"
    "v_cvt_pk_bf16_f32 v3, v18, v19\n"
    "s_waitcnt vmcnt(56)\n"
    "global_store_dwordx4 v224, v[0:3], s[34:35] offset:0 sc1\n"
    "v_pk_mul_f32 v[32:33], v[32:33], v[212:213]\n"
    "v_pk_mul_f32 v[34:35], v[34:35], v[212:213]\n"
    "v_mfma_f32_16x16x32_bf16 v[72:75], a[176:179], v[152:155], v[72:75]\n"
    "v_pk_mul_f32 v[48:49], v[48:49], v[212:213]\n"
    "v_pk_mul_f32 v[50:51], v[50:51], v[212:213]\n"
    "v_cvt_pk_bf16_f32 v32, v32, v33\n"
    "v_cvt_pk_bf16_f32 v33, v34, v35\n"
    "v_cvt_pk_bf16_f32 v34, v48, v49\n"
    "v_mfma_f32_16x16x32_bf16 v[88:91], a[180:183], v[152:155], v[88:91]\n"
    "v_cvt_pk_bf16_f32 v35, v50, v51\n"
    "global_store_dwordx4 v224, v[32:35], s[34:35] offset:1024 sc1\n"
    "v_pk_mul_f32 v[64:65], v[64:65], v[212:213]\n"
    "v_pk_mul_f32 v[66:67], v[66:67], v[212:213]\n"
    "v_pk_mul_f32 v[80:81], v[80:81], v[212:213]\n"
    "v_mfma_f32_16x16x32_bf16 v[104:107], a[184:187], v[152:155], v[104:107]\n"
    "v_pk_mul_f32 v[82:83], v[82:83], v[212:213]\n"
    "v_cvt_pk_bf16_f32 v64, v64, v65\n"
    "v_cvt_pk_bf16_f32 v65, v66, v67\n"
    "v_cvt_pk_bf16_f32 v66, v80, v81\n"
    "v_cvt_pk_bf16_f32 v67, v82, v83\n"
    "v_mfma_f32_16x16x32_bf16 v[120:123], a[188:191], v[152:155], v[120:123]\n"
    "global_store_dwordx4 v224, v[64:67], s[34:35] offset:2048 sc1\n"
    "v_pk_mul_f32 v[96:97], v[96:97], v[212:213]\n"
    "v_pk_mul_f32 v[98:99], v[98:99], v[212:213]\n"
    "v_pk_mul_f32 v[112:113], v[112:113], v[212:213]\n"
    "v_pk_mul_f32 v[114:115], v[114:115], v[212:213]\n"
    "v_mfma_f32_16x16x32_bf16 a[200:203], v[176:179], v[152:155], a[200:203]\n"
    "v_cvt_pk_bf16_f32 v96, v96, v97\n"
    "v_cvt_pk_bf16_f32 v97, v98, v99\n"
    "v_cvt_pk_bf16_f32 v98, v112, v113\n"
    "v_cvt_pk_bf16_f32 v99, v114, v115\n"
    "global_store_dwordx4 v224, v[96:99], s[34:35] offset:3072 sc1\n"
    "v_mfma_f32_16x16x32_bf16 v[12:15], a[160:163], v[156:159], v[12:15]\n"
    "v_accvgpr_read_b32 v221, a196\n"
    "v_add_u32 v225, 4096, v237\n"
    "v_rcp_f32 v214, v221\n"
    "s_nop 0\n"
    "v_mov_b32 v215, v214\n"
    "v_mfma_f32_16x16x32_bf16 v[28:31], a[164:167], v[156:159], v[28:31]\n"
    "v_pk_mul_f32 v[4:5], v[4:5], v[214:215]\n"
    "v_pk_mul_f32 v[6:7], v[6:7], v[214:215]\n"
    "v_pk_mul_f32 v[20:21], v[20:21], v[214:215]\n"
    "v_pk_mul_f32 v[22:23], v[22:23], v[214:215]\n"
    "v_mfma_f32_16x16x32_bf16 v[44:47], a[168:171], v[156:159], v[44:47]\n"
    "v_cvt_pk_bf16_f32 v4, v4, v5\n"
    "v_cvt_pk_bf16_f32 v5, v6, v7\n"
    "v_cvt_pk_bf16_f32 v6, v20, v21\n"
    "v_cvt_pk_bf16_f32 v7, v22, v23\n"
    "v_mfma_f32_16x16x32_bf16 v[60:63], a[172:175], v[156:159], v[60:63]\n"
    "s_waitcnt vmcnt(56)\n"
    "global_store_dwordx4 v225, v[4:7], s[34:35] offset:0 sc1\n"
    "v_pk_mul_f32 v[36:37], v[36:37], v[214:215]\n"
    "v_pk_mul_f32 v[38:39], v[38:39], v[214:215]\n"
    "v_pk_mul_f32 v[52:53], v[52:53], v[214:215]\n"
    "v_mfma_f32_16x16x32_bf16 v[76:79], a[176:179], v[156:159], v[76:79]\n"
    "v_pk_mul_f32 v[54:55], v[54:55], v[214:215]\n"
    "v_cvt_pk_bf16_f32 v36, v36, v37\n"
    "v_cvt_pk_bf16_f32 v37, v38, v39\n"
    "v_cvt_pk_bf16_f32 v38, v52, v53\n"
    "v_mfma_f32_16x16x32_bf16 v[92:95], a[180:183], v[156:159], v[92:95]\n"
    "v_cvt_pk_bf16_f32 v39, v54, v55\n"
    "global_store_dwordx4 v225, v[36:39], s[34:35] offset:1024 sc1\n"
    "v_pk_mul_f32 v[68:69], v[68:69], v[214:215]\n"
    "v_pk_mul_f32 v[70:71], v[70:71], v[214:215]\n"
    "v_mfma_f32_16x16x32_bf16 v[108:111], a[184:187], v[156:159], v[108:111]\n"
    "v_pk_mul_f32 v[84:85], v[84:85], v[214:215]\n"
    "v_pk_mul_f32 v[86:87], v[86:87], v[214:215]\n"
    "v_cvt_pk_bf16_f32 v68, v68, v69\n"
    "v_cvt_pk_bf16_f32 v69, v70, v71\n"
    "v_mfma_f32_16x16x32_bf16 v[124:127], a[188:191], v[156:159], v[124:127]\n"
    "v_cvt_pk_bf16_f32 v70, v84, v85\n"
    "v_cvt_pk_bf16_f32 v71, v86, v87\n"
    "global_store_dwordx4 v225, v[68:71], s[34:35] offset:2048 sc1\n"
    "v_pk_mul_f32 v[100:101], v[100:101], v[214:215]\n"
    "v_mfma_f32_16x16x32_bf16 a[204:207], v[176:179], v[156:159], a[204:207]\n"
    "v_pk_mul_f32 v[102:103], v[102:103], v[214:215]\n"
    "v_pk_mul_f32 v[116:117], v[116:117], v[214:215]\n"
    "v_pk_mul_f32 v[118:119], v[118:119], v[214:215]\n"
    "v_cvt_pk_bf16_f32 v100, v100, v101\n"
    "v_cvt_pk_bf16_f32 v101, v102, v103\n"
    "v_cvt_pk_bf16_f32 v102, v116, v117\n"
    "v_cvt_pk_bf16_f32 v103, v118, v119\n"
    "global_store_dwordx4 v225, v[100:103], s[34:35] offset:3072 sc1\n"
    "v_accvgpr_read_b32 v222, a200\n"
    "v_add_u32 v226, 8192, v237\n"
    "v_rcp_f32 v216, v222\n"
    "s_nop 0\n"
    "v_mov_b32 v217, v216\n"
    "v_pk_mul_f32 v[8:9], v[8:9], v[216:217]\n"
    "v_pk_mul_f32 v[10:11], v[10:11], v[216:217]\n"
    "v_pk_mul_f32 v[24:25], v[24:25], v[216:217]\n"
    "v_pk_mul_f32 v[26:27], v[26:27], v[216:217]\n"
    "v_cvt_pk_bf16_f32 v8, v8, v9\n"
    "v_cvt_pk_bf16_f32 v9, v10, v11\n"
    "v_cvt_pk_bf16_f32 v10, v24, v25\n"
    "v_cvt_pk_bf16_f32 v11, v26, v27\n"
    "s_waitcnt vmcnt(56)\n"
    "global_store_dwordx4 v226, v[8:11], s[34:35] offset:0 sc1\n"
    "v_pk_mul_f32 v[40:41], v[40:41], v[216:217]\n"
    "v_pk_mul_f32 v[42:43], v[42:43], v[216:217]\n"
    "v_pk_mul_f32 v[56:57], v[56:57], v[216:217]\n"
    "v_pk_mul_f32 v[58:59], v[58:59], v[216:217]\n"
    "v_cvt_pk_bf16_f32 v40, v40, v41\n"
    "v_cvt_pk_bf16_f32 v41, v42, v43\n"
    "v_cvt_pk_bf16_f32 v42, v56, v57\n"
    "v_cvt_pk_bf16_f32 v43, v58, v59\n"
    "global_store_dwordx4 v226, v[40:43], s[34:35] offset:1024 sc1\n"
    "v_pk_mul_f32 v[72:73], v[72:73], v[216:217]\n"
    "v_pk_mul_f32 v[74:75], v[74:75], v[216:217]\n"
    "v_pk_mul_f32 v[88:89], v[88:89], v[216:217]\n"
    "v_pk_mul_f32 v[90:91], v[90:91], v[216:217]\n"
    "v_cvt_pk_bf16_f32 v72, v72, v73\n"
    "v_cvt_pk_bf16_f32 v73, v74, v75\n"
    "v_cvt_pk_bf16_f32 v74, v88, v89\n"
    "v_cvt_pk_bf16_f32 v75, v90, v91\n"
    "global_store_dwordx4 v226, v[72:75], s[34:35] offset:2048 sc1\n"
    "v_pk_mul_f32 v[104:105], v[104:105], v[216:217]\n"
    "v_pk_mul_f32 v[106:107], v[106:107], v[216:217]\n"
    "v_pk_mul_f32 v[120:121], v[120:121], v[216:217]\n"
    "v_pk_mul_f32 v[122:123], v[122:123], v[216:217]\n"
    "v_cvt_pk_bf16_f32 v104, v104, v105\n"
    "v_cvt_pk_bf16_f32 v105, v106, v107\n"
    "v_cvt_pk_bf16_f32 v106, v120, v121\n"
    "v_cvt_pk_bf16_f32 v107, v122, v123\n"
    "global_store_dwordx4 v226, v[104:107], s[34:35] offset:3072 sc1\n"
    "v_accvgpr_read_b32 v223, a204\n"
    "v_add_u32 v227, 12288, v237\n"
    "v_rcp_f32 v218, v223\n"
    "s_nop 0\n"
    "v_mov_b32 v219, v218\n"
    "v_pk_mul_f32 v[12:13], v[12:13], v[218:219]\n"
    "v_pk_mul_f32 v[14:15], v[14:15], v[218:219]\n"
    "v_pk_mul_f32 v[28:29], v[28:29], v[218:219]\n"
    "v_pk_mul_f32 v[30:31], v[30:31], v[218:219]\n"
    "v_cvt_pk_bf16_f32 v12, v12, v13\n"
    "v_cvt_pk_bf16_f32 v13, v14, v15\n"
    "v_cvt_pk_bf16_f32 v14, v28, v29\n"
    "v_cvt_pk_bf16_f32 v15, v30, v31\n"
    "s_waitcnt vmcnt(56)\n"
    "global_store_dwordx4 v227, v[12:15], s[34:35] offset:0 sc1\n"
    "v_pk_mul_f32 v[44:45], v[44:45], v[218:219]\n"
    "v_pk_mul_f32 v[46:47], v[46:47], v[218:219]\n"
    "v_pk_mul_f32 v[60:61], v[60:61], v[218:219]\n"
    "v_pk_mul_f32 v[62:63], v[62:63], v[218:219]\n"
    "v_cvt_pk_bf16_f32 v44, v44, v45\n"
    "v_cvt_pk_bf16_f32 v45, v46, v47\n"
    "v_cvt_pk_bf16_f32 v46, v60, v61\n"
    "v_cvt_pk_bf16_f32 v47, v62, v63\n"
    "global_store_dwordx4 v227, v[44:47], s[34:35] offset:1024 sc1\n"
    "v_pk_mul_f32 v[76:77], v[76:77], v[218:219]\n"
    "v_pk_mul_f32 v[78:79], v[78:79], v[218:219]\n"
    "v_pk_mul_f32 v[92:93], v[92:93], v[218:219]\n"
    "v_pk_mul_f32 v[94:95], v[94:95], v[218:219]\n"
    "v_cvt_pk_bf16_f32 v76, v76, v77\n"
    "v_cvt_pk_bf16_f32 v77, v78, v79\n"
    "v_cvt_pk_bf16_f32 v78, v92, v93\n"
    "v_cvt_pk_bf16_f32 v79, v94, v95\n"
    "global_store_dwordx4 v227, v[76:79], s[34:35] offset:2048 sc1\n"
    "v_pk_mul_f32 v[108:109], v[108:109], v[218:219]\n"
    "v_pk_mul_f32 v[110:111], v[110:111], v[218:219]\n"
    "v_pk_mul_f32 v[124:125], v[124:125], v[218:219]\n"
    "v_pk_mul_f32 v[126:127], v[126:127], v[218:219]\n"
    "v_cvt_pk_bf16_f32 v108, v108, v109\n"
    "v_cvt_pk_bf16_f32 v109, v110, v111\n"
    "v_cvt_pk_bf16_f32 v110, v124, v125\n"
    "v_cvt_pk_bf16_f32 v111, v126, v127\n"
    "global_store_dwordx4 v227, v[108:111], s[34:35] offset:3072 sc1\n"
    "v_mov_b32 v228, 0x4e800000\n"
    "v_cmp_nle_f32 vcc, v220, v228\n"
    "v_cmp_nle_f32 s[46:47], v221, v228\n"
    "s_nop 3\n"
    "s_or_b64 vcc, vcc, s[46:47]\n"
    "v_cmp_nle_f32 s[46:47], v222, v228\n"
    "s_nop 3\n"
    "s_or_b64 vcc, vcc, s[46:47]\n"
    "v_cmp_nle_f32 s[46:47], v223, v228\n"
    "s_nop 3\n"
    "s_or_b64 vcc, vcc, s[46:47]\n"
    "s_cmp_lg_u64 vcc, 0\n"
    "s_cselect_b32 s46, 1, 0\n"
    "v_mov_b32 v229, s46\n"
    "s_lshl_b32 s46, s48, 2\n"
    "s_add_u32 s46, s46, s50\n"
    "s_add_u32 s46, s46, 0x18000\n"
    "v_mov_b32 v230, s46\n"
    "ds_write_b32 v230, v229\n"
    "s_add_u32 s46, s50, 0x18000\n"
    "v_mov_b32 v231, s46\n"
    "s_waitcnt vmcnt(56)\n"
    "global_load_dwordx4 a[48:51], v242, s[60:61] offset:0 sc1\n"
    "global_load_dwordx4 a[52:55], v242, s[60:61] offset:1024 sc1\n"
    "global_load_dwordx4 a[56:59], v242, s[60:61] offset:2048 sc1\n"
    "global_load_dwordx4 a[60:63], v242, s[60:61] offset:3072 sc1\n"
    "s_waitcnt vmcnt(56)\n"
    "global_load_dwordx4 a[96:99], v243, s[60:61] offset:0 sc1\n"
    "global_load_dwordx4 a[100:103], v243, s[60:61] offset:1024 sc1\n"
    "global_load_dwordx4 a[104:107], v243, s[60:61] offset:2048 sc1\n"
    "global_load_dwordx4 a[108:111], v243, s[60:61] offset:3072 sc1\n"
    "s_add_u32 s60, s60, 16384\n"
    "s_addc_u32 s61, s61, 0\n"
    "s_waitcnt vmcnt(56)\n"
    "global_load_dwordx4 a[112:115], v240, s[60:61] offset:0 sc1\n"
    "global_load_dwordx4 a[116:119], v240, s[60:61] offset:1024 sc1\n"
    "global_load_dwordx4 a[120:123], v240, s[60:61] offset:2048 sc1\n"
    "global_load_dwordx4 a[124:127], v240, s[60:61] offset:3072 sc1\n"
    "s_waitcnt vmcnt(56)\n"
    "global_load_dwordx4 a[160:163], v241, s[60:61] offset:0 sc1\n"
    "global_load_dwordx4 a[164:167], v241, s[60:61] offset:1024 sc1\n"
    "global_load_dwordx4 a[168:171], v241, s[60:61] offset:2048 sc1\n"
    "global_load_dwordx4 a[172:175], v241, s[60:61] offset:3072 sc1\n"
    "s_waitcnt vmcnt(56)\n"
    "global_load_dwordx4 a[176:179], v242, s[60:61] offset:0 sc1\n"
    "global_load_dwordx4 a[180:183], v242, s[60:61] offset:1024 sc1\n"
    "global_load_dwordx4 a[184:187], v242, s[60:61] offset:2048 sc1\n"
    "global_load_dwordx4 a[188:191], v242, s[60:61] offset:3072 sc1\n"
    "s_waitcnt vmcnt(56)\n"
    "global_load_dwordx4 a[192:195], v243, s[60:61] offset:0 sc1\n"
    "global_load_dwordx4 a[196:199], v243, s[60:61] offset:1024 sc1\n"
    "global_load_dwordx4 a[200:203], v243, s[60:61] offset:2048 sc1\n"
    "global_load_dwordx4 a[204:207], v243, s[60:61] offset:3072 sc1\n"
    "s_add_u32 s60, s60, 16384\n"
    "s_addc_u32 s61, s61, 0\n"
    "s_waitcnt vmcnt(56)\n"
    "global_load_dwordx4 v[64:67], v240, s[60:61] offset:0 sc1\n"
    "global_load_dwordx4 v[68:71], v240, s[60:61] offset:1024 sc1\n"
    "global_load_dwordx4 v[72:75], v240, s[60:61] offset:2048 sc1\n"
    "global_load_dwordx4 v[76:79], v240, s[60:61] offset:3072 sc1\n"
    "s_waitcnt vmcnt(56)\n"
    "global_load_dwordx4 v[80:83], v241, s[60:61] offset:0 sc1\n"
    "global_load_dwordx4 v[84:87], v241, s[60:61] offset:1024 sc1\n"
    "global_load_dwordx4 v[88:91], v241, s[60:61] offset:2048 sc1\n"
    "global_load_dwordx4 v[92:95], v241, s[60:61] offset:3072 sc1\n"
    "s_waitcnt vmcnt(32)\n"
    "s_waitcnt lgkmcnt(0)\n"
    "s_barrier\n"
    "ds_read_b128 v[232:235], v231\n"
    "s_waitcnt lgkmcnt(0)\n"
    "v_or_b32 v232, v232, v233\n"
    "v_or_b32 v234, v234, v235\n"
    "v_or_b32 v232, v232, v234\n"
    "s_nop 0\n"
    "v_readfirstlane_b32 s46, v232\n"
    "s_xor_b32 s47, s51, 1\n"
    "s_nop 2\n"
    "s_and_b32 s46, s46, s47\n"
    "s_cmp_lg_u32 s46, 0\n"
    "s_cbranch_scc1 FB%=\n"
    "s_cmp_lg_u32 s48, 0\n"
    "s_cbranch_scc1 OFL%=\n"
    "s_lshl_b32 s46, s57, 5\n"
    "s_add_u32 s46, s46, s58\n"
    "s_lshl_b32 s46, s46, 3\n"
    "s_add_u32 s46, s46, 4096\n"
    "v_mov_b32 v252, s46\n"
    "v_mov_b32 v254, s96\n"
    "v_mov_b32 v255, s97\n"
    "global_store_dwordx2 v252, v[254:255], s[32:33] sc1\n"
    "OFL%=:\n"
    "global_load_dwordx4 v[96:99], v242, s[60:61] offset:0 sc1\n"
    "global_load_dwordx4 v[100:103], v242, s[60:61] offset:1024 sc1\n"
    "global_load_dwordx4 v[104:107], v242, s[60:61] offset:2048 sc1\n"
    "global_load_dwordx4 v[108:111], v242, s[60:61] offset:3072 sc1\n"
    "global_load_dwordx4 v[112:115], v243, s[60:61] offset:0 sc1\n"
    "global_load_dwordx4 v[116:119], v243, s[60:61] offset:1024 sc1\n"
    "global_load_dwordx4 v[120:123], v243, s[60:61] offset:2048 sc1\n"
    "global_load_dwordx4 v[124:127], v243, s[60:61] offset:3072 sc1\n"
    "s_add_u32 s60, s60, 16384\n"
    "s_addc_u32 s61, s61, 0\n"
    "global_load_dwordx4 v[128:131], v240, s[60:61] offset:0 sc1\n"
    "global_load_dwordx4 v[132:135], v240, s[60:61] offset:1024 sc1\n"
    "global_load_dwordx4 v[136:139], v240, s[60:61] offset:2048 sc1\n"
    "global_load_dwordx4 v[140:143], v240, s[60:61] offset:3072 sc1\n"
    "global_load_dwordx4 v[144:147], v241, s[60:61] offset:0 sc1\n"
    "global_load_dwordx4 v[148:151], v241, s[60:61] offset:1024 sc1\n"
    "global_load_dwordx4 v[152:155], v241, s[60:61] offset:2048 sc1\n"
    "global_load_dwordx4 v[156:159], v241, s[60:61] offset:3072 sc1\n"
    "; ---- wait for the 8 heads of this wave, then out tile = sum over K of wo^T O^T\n"
    "v_mov_b32 v254, s96\n"
    "v_mov_b32 v255, s97\n"
    "s_mov_b32 s46, 400000\n"
    "OPL%=:\n"
    "global_load_dwordx2 v[252:253], v239, s[32:33] offset:0 sc1\n"
    "s_waitcnt vmcnt(0)\n"
    "v_cmp_eq_u64 vcc, v[252:253], v[254:255]\n"
    "s_nop 3\n"
    "s_cmp_eq_u64 vcc, -1\n"
    "s_cbranch_scc1 OPLD%=\n"
    "s_sleep 2\n"
    "s_sub_u32 s46, s46, 1\n"
    "s_cmp_lg_u32 s46, 0\n"
    "s_cbranch_scc1 OPL%=\n"
    "OPLD%=:\n"
    "v_mov_b32 v224, s96\n"
    "v_mov_b32 v225, s97\n"
    "v_add_u32 v254, s30, v237\n"
    "v_add_u32 v255, 0x10000, v254\n"
    "v_add_u32 v253, 0x20000, v254\n"
    "s_mov_b32 s42, 0\n"
    "s_or_b32 s46, s49, s48\n"
    "s_cmp_lg_u32 s46, 0\n"
    "s_cbranch_scc1 FBI%=\n"
    "v_lshlrev_b32 v226, 5, v236\n"
    "v_add_u32 v226, 4096, v226\n"
    "global_load_dwordx4 v[228:231], v226, s[32:33] sc1\n"
    "global_load_dwordx4 v[232:235], v226, s[32:33] offset:16 sc1\n"
    "FBI%=:\n"
    "global_load_dwordx4 v[160:163], v244, s[62:63] offset:0 sc1\n"
    "global_load_dwordx4 v[164:167], v245, s[62:63] offset:0 sc1\n"
    "global_load_dwordx4 v[168:171], v246, s[62:63] offset:0 sc1\n"
    "global_load_dwordx4 v[172:175], v247, s[62:63] offset:0 sc1\n"
    "global_load_dwordx4 v[176:179], v244, s[62:63] offset:1024 sc1\n"
    "global_load_dwordx4 v[180:183], v245, s[62:63] offset:1024 sc1\n"
    "global_load_dwordx4 v[184:187], v246, s[62:63] offset:1024 sc1\n"
    "global_load_dwordx4 v[188:191], v247, s[62:63] offset:1024 sc1\n"
    "global_load_dwordx4 v[192:195], v244, s[62:63] offset:2048 sc1\n"
    "global_load_dwordx4 v[196:199], v245, s[62:63] offset:2048 sc1\n"
    "global_load_dwordx4 v[200:203], v246, s[62:63] offset:2048 sc1\n"
    "global_load_dwordx4 v[204:207], v247, s[62:63] offset:2048 sc1\n"
    "global_load_dwordx4 v[208:211], v244, s[62:63] offset:3072 sc1\n"
    "global_load_dwordx4 v[212:215], v245, s[62:63] offset:3072 sc1\n"
    "global_load_dwordx4 v[216:219], v246, s[62:63] offset:3072 sc1\n"
    "global_load_dwordx4 v[220:223], v247, s[62:63] offset:3072 sc1\n"
    "s_add_u32 m0, s30, 0xc000\n"
    "s_mov_b32 s43, 122880\n"
    "buffer_load_dwordx4 v237, s[52:55], s43 offen offset:0 sc1 lds\n"
    "buffer_load_dwordx4 v237, s[52:55], s43 offen offset:1024 sc1 lds\n"
    "buffer_load_dwordx4 v237, s[52:55], s43 offen offset:2048 sc1 lds\n"
    "buffer_load_dwordx4 v237, s[52:55], s43 offen offset:3072 sc1 lds\n"
    "s_add_u32 m0, s30, 0x1c000\n"
    "s_mov_b32 s43, 126976\n"
    "buffer_load_dwordx4 v237, s[52:55], s43 offen offset:0 sc1 lds\n"
    "buffer_load_dwordx4 v237, s[52:55], s43 offen offset:1024 sc1 lds\n"
    "buffer_load_dwordx4 v237, s[52:55], s43 offen offset:2048 sc1 lds\n"
    "buffer_load_dwordx4 v237, s[52:55], s43 offen offset:3072 sc1 lds\n"
    "s_waitcnt vmcnt(23)\n"
    "v_mfma_f32_16x16x32_bf16 v[0:3], a[208:211], v[160:163], 0\n"
    "s_waitcnt vmcnt(22)\n"
    "v_mfma_f32_16x16x32_bf16 v[4:7], a[208:211], v[164:167], 0\n"
    "s_waitcnt vmcnt(21)\n"
    "v_mfma_f32_16x16x32_bf16 v[8:11], a[208:211], v[168:171], 0\n"
    "s_waitcnt vmcnt(20)\n"
    "v_mfma_f32_16x16x32_bf16 v[12:15], a[208:211], v[172:175], 0\n"
    "v_mfma_f32_16x16x32_bf16 v[16:19], a[212:215], v[160:163], 0\n"
    "v_mfma_f32_16x16x32_bf16 v[20:23], a[212:215], v[164:167], 0\n"
    "v_mfma_f32_16x16x32_bf16 v[24:27], a[212:215], v[168:171], 0\n"
    "v_mfma_f32_16x16x32_bf16 v[28:31], a[212:215], v[172:175], 0\n"
    "v_mfma_f32_16x16x32_bf16 v[32:35], a[216:219], v[160:163], 0\n"
    "v_mfma_f32_16x16x32_bf16 v[36:39], a[216:219], v[164:167], 0\n"
    "v_mfma_f32_16x16x32_bf16 v[40:43], a[216:219], v[168:171], 0\n"
    "v_mfma_f32_16x16x32_bf16 v[44:47], a[216:219], v[172:175], 0\n"
    "v_mfma_f32_16x16x32_bf16 v[48:51], a[220:223], v[160:163], 0\n"
    "v_mfma_f32_16x16x32_bf16 v[52:55], a[220:223], v[164:167], 0\n"
    "v_mfma_f32_16x16x32_bf16 v[56:59], a[220:223], v[168:171], 0\n"
    "v_mfma_f32_16x16x32_bf16 v[60:63], a[220:223], v[172:175], 0\n"
    "s_waitcnt vmcnt(19)\n"
    "v_mfma_f32_16x16x32_bf16 v[0:3], a[224:227], v[176:179], v[0:3]\n"
    "s_waitcnt vmcnt(18)\n"
    "s_add_u32 s62, s62, 65536\n"
    "s_addc_u32 s63, s63, 0\n"
    "global_load_dwordx4 a[208:211], v244, s[62:63] offset:0 sc1\n"
    "v_mfma_f32_16x16x32_bf16 v[4:7], a[224:227], v[180:183], v[4:7]\n"
    "s_waitcnt vmcnt(18)\n"
    "v_mfma_f32_16x16x32_bf16 v[8:11], a[224:227], v[184:187], v[8:11]\n"
    "s_waitcnt vmcnt(17)\n"
    "global_load_dwordx4 a[212:215], v245, s[62:63] offset:0 sc1\n"
    "v_mfma_f32_16x16x32_bf16 v[12:15], a[224:227], v[188:191], v[12:15]\n"
    "v_mfma_f32_16x16x32_bf16 v[16:19], a[228:231], v[176:179], v[16:19]\n"
    "global_load_dwordx4 a[216:219], v246, s[62:63] offset:0 sc1\n"
    "v_mfma_f32_16x16x32_bf16 v[20:23], a[228:231], v[180:183], v[20:23]\n"
    "v_mfma_f32_16x16x32_bf16 v[24:27], a[228:231], v[184:187], v[24:27]\n"
    "global_load_dwordx4 a[220:223], v247, s[62:63] offset:0 sc1\n"
    "v_mfma_f32_16x16x32_bf16 v[28:31], a[228:231], v[188:191], v[28:31]\n"
    "v_mfma_f32_16x16x32_bf16 v[32:35], a[232:235], v[176:179], v[32:35]\n"
    "global_load_dwordx4 v[160:163], v244, s[62:63] offset:1024 sc1\n"
    "v_mfma_f32_16x16x32_bf16 v[36:39], a[232:235], v[180:183], v[36:39]\n"
    "v_mfma_f32_16x16x32_bf16 v[40:43], a[232:235], v[184:187], v[40:43]\n"
    "global_load_dwordx4 v[164:167], v245, s[62:63] offset:1024 sc1\n"
    "v_mfma_f32_16x16x32_bf16 v[44:47], a[232:235], v[188:191], v[44:47]\n"
    "v_mfma_f32_16x16x32_bf16 v[48:51], a[236:239], v[176:179], v[48:51]\n"
    "global_load_dwordx4 v[168:171], v246, s[62:63] offset:1024 sc1\n"
    "v_mfma_f32_16x16x32_bf16 v[52:55], a[236:239], v[180:183], v[52:55]\n"
    "v_mfma_f32_16x16x32_bf16 v[56:59], a[236:239], v[184:187], v[56:59]\n"
    "global_load_dwordx4 v[172:175], v247, s[62:63] offset:1024 sc1\n"
    "v_mfma_f32_16x16x32_bf16 v[60:63], a[236:239], v[188:191], v[60:63]\n"
    "s_waitcnt vmcnt(23)\n"
    "v_mfma_f32_16x16x32_bf16 v[0:3], a[240:243], v[192:195], v[0:3]\n"
    "s_waitcnt vmcnt(22)\n"
    "global_load_dwordx4 a[224:227], v244, s[62:63] offset:2048 sc1\n"
    "v_mfma_f32_16x16x32_bf16 v[4:7], a[240:243], v[196:199], v[4:7]\n"
    "s_waitcnt vmcnt(22)\n"
    "v_mfma_f32_16x16x32_bf16 v[8:11], a[240:243], v[200:203], v[8:11]\n"
    "s_waitcnt vmcnt(21)\n"
    "global_load_dwordx4 a[228:231], v245, s[62:63] offset:2048 sc1\n"
    "v_mfma_f32_16x16x32_bf16 v[12:15], a[240:243], v[204:207], v[12:15]\n"
    "v_mfma_f32_16x16x32_bf16 v[16:19], a[244:247], v[192:195], v[16:19]\n"
    "global_load_dwordx4 a[232:235], v246, s[62:63] offset:2048 sc1\n"
    "v_mfma_f32_16x16x32_bf16 v[20:23], a[244:247], v[196:199], v[20:23]\n"
    "v_mfma_f32_16x16x32_bf16 v[24:27], a[244:247], v[200:203], v[24:27]\n"
    "global_load_dwordx4 a[236:239], v247, s[62:63] offset:2048 sc1\n"
    "v_mfma_f32_16x16x32_bf16 v[28:31], a[244:247], v[204:207], v[28:31]\n"
    "s_or_b32 s46, s49, s48\n"
    "s_cmp_lg_u32 s46, 0\n"
    "s_cbranch_scc1 FBC%=\n"
    "v_mov_b32 v226, s96\n"
    "v_mov_b32 v227, s97\n"
    "s_nop 1\n"
    "v_cmp_eq_u64 vcc, v[228:229], v[226:227]\n"
    "v_cmp_eq_u64 s[44:45], v[230:231], v[226:227]\n"
    "s_nop 3\n"
    "s_and_b64 vcc, vcc, s[44:45]\n"
    "v_cmp_eq_u64 s[44:45], v[232:233], v[226:227]\n"
    "s_nop 3\n"
    "s_and_b64 vcc, vcc, s[44:45]\n"
    "v_cmp_eq_u64 s[44:45], v[234:235], v[226:227]\n"
    "s_nop 3\n"
    "s_and_b64 vcc, vcc, s[44:45]\n"
    "s_cmp_eq_u64 vcc, -1\n"
    "s_cbranch_scc0 FBC%=\n"
    "v_mov_b32 v228, 0\n"
    "global_store_dwordx2 v228, v[226:227], s[32:33] sc1\n"
    "s_mov_b32 s42, 1\n"
    "FBC%=:\n"
    "v_mfma_f32_16x16x32_bf16 v[32:35], a[248:251], v[192:195], v[32:35]\n"
    "global_load_dwordx4 v[176:179], v244, s[62:63] offset:3072 sc1\n"
    "v_mfma_f32_16x16x32_bf16 v[36:39], a[248:251], v[196:199], v[36:39]\n"
    "v_mfma_f32_16x16x32_bf16 v[40:43], a[248:251], v[200:203], v[40:43]\n"
    "global_load_dwordx4 v[180:183], v245, s[62:63] offset:3072 sc1\n"
    "v_mfma_f32_16x16x32_bf16 v[44:47], a[248:251], v[204:207], v[44:47]\n"
    "v_mfma_f32_16x16x32_bf16 v[48:51], a[252:255], v[192:195], v[48:51]\n"
    "global_load_dwordx4 v[184:187], v246, s[62:63] offset:3072 sc1\n"
    "v_mfma_f32_16x16x32_bf16 v[52:55], a[252:255], v[196:199], v[52:55]\n"
    "v_mfma_f32_16x16x32_bf16 v[56:59], a[252:255], v[200:203], v[56:59]\n"
    "global_load_dwordx4 v[188:191], v247, s[62:63] offset:3072 sc1\n"
    "v_mfma_f32_16x16x32_bf16 v[60:63], a[252:255], v[204:207], v[60:63]\n"
    "s_waitcnt vmcnt(27)\n"
    "v_mfma_f32_16x16x32_bf16 v[0:3], a[64:67], v[208:211], v[0:3]\n"
    "s_waitcnt vmcnt(26)\n"
    "s_add_u32 s62, s62, 65536\n"
    "s_addc_u32 s63, s63, 0\n"
    "global_load_dwordx4 a[240:243], v244, s[62:63] offset:0 sc1\n"
    "v_mfma_f32_16x16x32_bf16 v[4:7], a[64:67], v[212:215], v[4:7]\n"
    "s_waitcnt vmcnt(26)\n"
    "v_mfma_f32_16x16x32_bf16 v[8:11], a[64:67], v[216:219], v[8:11]\n"
    "s_waitcnt vmcnt(25)\n"
    "global_load_dwordx4 a[244:247], v245, s[62:63] offset:0 sc1\n"
    "v_mfma_f32_16x16x32_bf16 v[12:15], a[64:67], v[220:223], v[12:15]\n"
    "v_mfma_f32_16x16x32_bf16 v[16:19], a[68:71], v[208:211], v[16:19]\n"
    "global_load_dwordx4 a[248:251], v246, s[62:63] offset:0 sc1\n"
    "v_mfma_f32_16x16x32_bf16 v[20:23], a[68:71], v[212:215], v[20:23]\n"
    "v_mfma_f32_16x16x32_bf16 v[24:27], a[68:71], v[216:219], v[24:27]\n"
    "global_load_dwordx4 a[252:255], v247, s[62:63] offset:0 sc1\n"
    "v_mfma_f32_16x16x32_bf16 v[28:31], a[68:71], v[220:223], v[28:31]\n"
    "v_mfma_f32_16x16x32_bf16 v[32:35], a[72:75], v[208:211], v[32:35]\n"
    "v_mfma_f32_16x16x32_bf16 v[36:39], a[72:75], v[212:215], v[36:39]\n"
    "v_mfma_f32_16x16x32_bf16 v[40:43], a[72:75], v[216:219], v[40:43]\n"
    "v_mfma_f32_16x16x32_bf16 v[44:47], a[72:75], v[220:223], v[44:47]\n"
    "v_mfma_f32_16x16x32_bf16 v[48:51], a[76:79], v[208:211], v[48:51]\n"
    "v_mfma_f32_16x16x32_bf16 v[52:55], a[76:79], v[212:215], v[52:55]\n"
    "v_mfma_f32_16x16x32_bf16 v[56:59], a[76:79], v[216:219], v[56:59]\n"
    "v_mfma_f32_16x16x32_bf16 v[60:63], a[76:79], v[220:223], v[60:63]\n"
    "s_waitcnt vmcnt(19)\n"
    "v_mfma_f32_16x16x32_bf16 v[0:3], a[80:83], a[208:211], v[0:3]\n"
    "s_waitcnt vmcnt(18)\n"
    "global_load_dwordx4 v[192:195], v244, s[62:63] offset:1024 sc1\n"
    "v_mfma_f32_16x16x32_bf16 v[4:7], a[80:83], a[212:215], v[4:7]\n"
    "s_waitcnt vmcnt(18)\n"
    "v_mfma_f32_16x16x32_bf16 v[8:11], a[80:83], a[216:219], v[8:11]\n"
    "s_waitcnt vmcnt(17)\n"
    "global_load_dwordx4 v[196:199], v245, s[62:63] offset:1024 sc1\n"
    "v_mfma_f32_16x16x32_bf16 v[12:15], a[80:83], a[220:223], v[12:15]\n"
    "v_mfma_f32_16x16x32_bf16 v[16:19], a[84:87], a[208:211], v[16:19]\n"
    "global_load_dwordx4 v[200:203], v246, s[62:63] offset:1024 sc1\n"
    "v_mfma_f32_16x16x32_bf16 v[20:23], a[84:87], a[212:215], v[20:23]\n"
    "v_mfma_f32_16x16x32_bf16 v[24:27], a[84:87], a[216:219], v[24:27]\n"
    "global_load_dwordx4 v[204:207], v247, s[62:63] offset:1024 sc1\n"
    "v_mfma_f32_16x16x32_bf16 v[28:31], a[84:87], a[220:223], v[28:31]\n"
    "v_mfma_f32_16x16x32_bf16 v[32:35], a[88:91], a[208:211], v[32:35]\n"
    "v_mfma_f32_16x16x32_bf16 v[36:39], a[88:91], a[212:215], v[36:39]\n"
    "v_mfma_f32_16x16x32_bf16 v[40:43], a[88:91], a[216:219], v[40:43]\n"
    "v_mfma_f32_16x16x32_bf16 v[44:47], a[88:91], a[220:223], v[44:47]\n"
    "v_mfma_f32_16x16x32_bf16 v[48:51], a[92:95], a[208:211], v[48:51]\n"
    "v_mfma_f32_16x16x32_bf16 v[52:55], a[92:95], a[212:215], v[52:55]\n"
    "v_mfma_f32_16x16x32_bf16 v[56:59], a[92:95], a[216:219], v[56:59]\n"
    "v_mfma_f32_16x16x32_bf16 v[60:63], a[92:95], a[220:223], v[60:63]\n"
    "s_waitcnt vmcnt(19)\n"
    "v_mfma_f32_16x16x32_bf16 v[0:3], a[0:3], v[160:163], v[0:3]\n"
    "s_waitcnt vmcnt(18)\n"
    "global_load_dwordx4 a[64:67], v244, s[62:63] offset:2048 sc1\n"
    "v_mfma_f32_16x16x32_bf16 v[4:7], a[0:3], v[164:167], v[4:7]\n"
    "s_waitcnt vmcnt(18)\n"
    "v_mfma_f32_16x16x32_bf16 v[8:11], a[0:3], v[168:171], v[8:11]\n"
    "s_waitcnt vmcnt(17)\n"
    "global_load_dwordx4 a[68:71], v245, s[62:63] offset:2048 sc1\n"
    "v_mfma_f32_16x16x32_bf16 v[12:15], a[0:3], v[172:175], v[12:15]\n"
    "v_mfma_f32_16x16x32_bf16 v[16:19], a[4:7], v[160:163], v[16:19]\n"
    "global_load_dwordx4 a[72:75], v246, s[62:63] offset:2048 sc1\n"
    "v_mfma_f32_16x16x32_bf16 v[20:23], a[4:7], v[164:167], v[20:23]\n"
    "v_mfma_f32_16x16x32_bf16 v[24:27], a[4:7], v[168:171], v[24:27]\n"
    "global_load_dwordx4 a[76:79], v247, s[62:63] offset:2048 sc1\n"
    "v_mfma_f32_16x16x32_bf16 v[28:31], a[4:7], v[172:175], v[28:31]\n"
    "v_mfma_f32_16x16x32_bf16 v[32:35], a[8:11], v[160:163], v[32:35]\n"
    "v_mfma_f32_16x16x32_bf16 v[36:39], a[8:11], v[164:167], v[36:39]\n"
    "v_mfma_f32_16x16x32_bf16 v[40:43], a[8:11], v[168:171], v[40:43]\n"
    "v_mfma_f32_16x16x32_bf16 v[44:47], a[8:11], v[172:175], v[44:47]\n"
    "v_mfma_f32_16x16x32_bf16 v[48:51], a[12:15], v[160:163], v[48:51]\n"
    "v_mfma_f32_16x16x32_bf16 v[52:55], a[12:15], v[164:167], v[52:55]\n"
    "v_mfma_f32_16x16x32_bf16 v[56:59], a[12:15], v[168:171], v[56:59]\n"
    "v_mfma_f32_16x16x32_bf16 v[60:63], a[12:15], v[172:175], v[60:63]\n"
    "s_waitcnt vmcnt(19)\n"
    "v_mfma_f32_16x16x32_bf16 v[0:3], a[16:19], a[224:227], v[0:3]\n"
    "s_waitcnt vmcnt(18)\n"
    "global_load_dwordx4 v[208:211], v244, s[62:63] offset:3072 sc1\n"
    "v_mfma_f32_16x16x32_bf16 v[4:7], a[16:19], a[228:231], v[4:7]\n"
    "s_waitcnt vmcnt(18)\n"
    "v_mfma_f32_16x16x32_bf16 v[8:11], a[16:19], a[232:235], v[8:11]\n"
    "s_waitcnt vmcnt(17)\n"
    "global_load_dwordx4 v[212:215], v245, s[62:63] offset:3072 sc1\n"
    "v_mfma_f32_16x16x32_bf16 v[12:15], a[16:19], a[236:239], v[12:15]\n"
    "v_mfma_f32_16x16x32_bf16 v[16:19], a[20:23], a[224:227], v[16:19]\n"
    "global_load_dwordx4 v[216:219], v246, s[62:63] offset:3072 sc1\n"
    "v_mfma_f32_16x16x32_bf16 v[20:23], a[20:23], a[228:231], v[20:23]\n"
    "v_mfma_f32_16x16x32_bf16 v[24:27], a[20:23], a[232:235], v[24:27]\n"
    "global_load_dwordx4 v[220:223], v247, s[62:63] offset:3072 sc1\n"
    "v_mfma_f32_16x16x32_bf16 v[28:31], a[20:23], a[236:239], v[28:31]\n"
    "v_mfma_f32_16x16x32_bf16 v[32:35], a[24:27], a[224:227], v[32:35]\n"
    "v_mfma_f32_16x16x32_bf16 v[36:39], a[24:27], a[228:231], v[36:39]\n"
    "v_mfma_f32_16x16x32_bf16 v[40:43], a[24:27], a[232:235], v[40:43]\n"
    "v_mfma_f32_16x16x32_bf16 v[44:47], a[24:27], a[236:239], v[44:47]\n"
    "v_mfma_f32_16x16x32_bf16 v[48:51], a[28:31], a[224:227], v[48:51]\n"
    "v_mfma_f32_16x16x32_bf16 v[52:55], a[28:31], a[228:231], v[52:55]\n"
    "v_mfma_f32_16x16x32_bf16 v[56:59], a[28:31], a[232:235], v[56:59]\n"
    "v_mfma_f32_16x16x32_bf16 v[60:63], a[28:31], a[236:239], v[60:63]\n"
    "s_waitcnt vmcnt(19)\n"
    "v_mfma_f32_16x16x32_bf16 v[0:3], a[128:131], v[176:179], v[0:3]\n"
    "s_waitcnt vmcnt(18)\n"
    "s_add_u32 s62, s62, 65536\n"
    "s_addc_u32 s63, s63, 0\n"
    "global_load_dwordx4 a[80:83], v244, s[62:63] offset:0 sc1\n"
    "v_mfma_f32_16x16x32_bf16 v[4:7], a[128:131], v[180:183], v[4:7]\n"
    "s_waitcnt vmcnt(18)\n"
    "v_mfma_f32_16x16x32_bf16 v[8:11], a[128:131], v[184:187], v[8:11]\n"
    "s_waitcnt vmcnt(17)\n"
    "global_load_dwordx4 a[84:87], v245, s[62:63] offset:0 sc1\n"
    "v_mfma_f32_16x16x32_bf16 v[12:15], a[128:131], v[188:191], v[12:15]\n"
    "v_mfma_f32_16x16x32_bf16 v[16:19], a[132:135], v[176:179], v[16:19]\n"
    "global_load_dwordx4 a[88:91], v246, s[62:63] offset:0 sc1\n"
    "v_mfma_f32_16x16x32_bf16 v[20:23], a[132:135], v[180:183], v[20:23]\n"
    "v_mfma_f32_16x16x32_bf16 v[24:27], a[132:135], v[184:187], v[24:27]\n"
    "global_load_dwordx4 a[92:95], v247, s[62:63] offset:0 sc1\n"
    "v_mfma_f32_16x16x32_bf16 v[28:31], a[132:135], v[188:191], v[28:31]\n"
    "v_mfma_f32_16x16x32_bf16 v[32:35], a[136:139], v[176:179], v[32:35]\n"
    "v_mfma_f32_16x16x32_bf16 v[36:39], a[136:139], v[180:183], v[36:39]\n"
    "v_mfma_f32_16x16x32_bf16 v[40:43], a[136:139], v[184:187], v[40:43]\n"
    "v_mfma_f32_16x16x32_bf16 v[44:47], a[136:139], v[188:191], v[44:47]\n"
    "v_mfma_f32_16x16x32_bf16 v[48:51], a[140:143], v[176:179], v[48:51]\n"
    "v_mfma_f32_16x16x32_bf16 v[52:55], a[140:143], v[180:183], v[52:55]\n"
    "v_mfma_f32_16x16x32_bf16 v[56:59], a[140:143], v[184:187], v[56:59]\n"
    "v_mfma_f32_16x16x32_bf16 v[60:63], a[140:143], v[188:191], v[60:63]\n"
    "s_waitcnt vmcnt(19)\n"
    "v_mfma_f32_16x16x32_bf16 v[0:3], a[144:147], a[240:243], v[0:3]\n"
    "s_waitcnt vmcnt(18)\n"
    "global_load_dwordx4 a[208:211], v244, s[62:63] offset:1024 sc1\n"
    "v_mfma_f32_16x16x32_bf16 v[4:7], a[144:147], a[244:247], v[4:7]\n"
    "s_waitcnt vmcnt(18)\n"
    "v_mfma_f32_16x16x32_bf16 v[8:11], a[144:147], a[248:251], v[8:11]\n"
    "s_waitcnt vmcnt(17)\n"
    "global_load_dwordx4 a[212:215], v245, s[62:63] offset:1024 sc1\n"
    "v_mfma_f32_16x16x32_bf16 v[12:15], a[144:147], a[252:255], v[12:15]\n"
    "v_mfma_f32_16x16x32_bf16 v[16:19], a[148:151], a[240:243], v[16:19]\n"
    "global_load_dwordx4 a[216:219], v246, s[62:63] offset:1024 sc1\n"
    "v_mfma_f32_16x16x32_bf16 v[20:23], a[148:151], a[244:247], v[20:23]\n"
    "v_mfma_f32_16x16x32_bf16 v[24:27], a[148:151], a[248:251], v[24:27]\n"
    "global_load_dwordx4 a[220:223], v247, s[62:63] offset:1024 sc1\n"
    "v_mfma_f32_16x16x32_bf16 v[28:31], a[148:151], a[252:255], v[28:31]\n"
    "v_mfma_f32_16x16x32_bf16 v[32:35], a[152:155], a[240:243], v[32:35]\n"
    "v_mfma_f32_16x16x32_bf16 v[36:39], a[152:155], a[244:247], v[36:39]\n"
    "v_mfma_f32_16x16x32_bf16 v[40:43], a[152:155], a[248:251], v[40:43]\n"
    "v_mfma_f32_16x16x32_bf16 v[44:47], a[152:155], a[252:255], v[44:47]\n"
    "v_mfma_f32_16x16x32_bf16 v[48:51], a[156:159], a[240:243], v[48:51]\n"
    "v_mfma_f32_16x16x32_bf16 v[52:55], a[156:159], a[244:247], v[52:55]\n"
    "v_mfma_f32_16x16x32_bf16 v[56:59], a[156:159], a[248:251], v[56:59]\n"
    "v_mfma_f32_16x16x32_bf16 v[60:63], a[156:159], a[252:255], v[60:63]\n"
    "s_waitcnt vmcnt(19)\n"
    "v_mfma_f32_16x16x32_bf16 v[0:3], a[32:35], v[192:195], v[0:3]\n"
    "s_waitcnt vmcnt(18)\n"
    "global_load_dwordx4 a[0:3], v244, s[62:63] offset:2048 sc1\n"
    "v_mfma_f32_16x16x32_bf16 v[4:7], a[32:35], v[196:199], v[4:7]\n"
    "s_waitcnt vmcnt(18)\n"
    "v_mfma_f32_16x16x32_bf16 v[8:11], a[32:35], v[200:203], v[8:11]\n"
    "s_waitcnt vmcnt(17)\n"
    "global_load_dwordx4 a[4:7], v245, s[62:63] offset:2048 sc1\n"
    "v_mfma_f32_16x16x32_bf16 v[12:15], a[32:35], v[204:207], v[12:15]\n"
    "v_mfma_f32_16x16x32_bf16 v[16:19], a[36:39], v[192:195], v[16:19]\n"
    "global_load_dwordx4 a[8:11], v246, s[62:63] offset:2048 sc1\n"
    "v_mfma_f32_16x16x32_bf16 v[20:23], a[36:39], v[196:199], v[20:23]\n"
    "v_mfma_f32_16x16x32_bf16 v[24:27], a[36:39], v[200:203], v[24:27]\n"
    "global_load_dwordx4 a[12:15], v247, s[62:63] offset:2048 sc1\n"
    "v_mfma_f32_16x16x32_bf16 v[28:31], a[36:39], v[204:207], v[28:31]\n"
    "v_mfma_f32_16x16x32_bf16 v[32:35], a[40:43], v[192:195], v[32:35]\n"
    "v_mfma_f32_16x16x32_bf16 v[36:39], a[40:43], v[196:199], v[36:39]\n"
    "v_mfma_f32_16x16x32_bf16 v[40:43], a[40:43], v[200:203], v[40:43]\n"
    "v_mfma_f32_16x16x32_bf16 v[44:47], a[40:43], v[204:207], v[44:47]\n"
    "v_mfma_f32_16x16x32_bf16 v[48:51], a[44:47], v[192:195], v[48:51]\n"
    "v_mfma_f32_16x16x32_bf16 v[52:55], a[44:47], v[196:199], v[52:55]\n"
    "v_mfma_f32_16x16x32_bf16 v[56:59], a[44:47], v[200:203], v[56:59]\n"
    "v_mfma_f32_16x16x32_bf16 v[60:63], a[44:47], v[204:207], v[60:63]\n"
    "s_waitcnt vmcnt(19)\n"
    "v_mfma_f32_16x16x32_bf16 v[0:3], a[48:51], a[64:67], v[0:3]\n"
    "s_waitcnt vmcnt(18)\n"
    "global_load_dwordx4 v[160:163], v244, s[62:63] offset:3072 sc1\n"
    "v_mfma_f32_16x16x32_bf16 v[4:7], a[48:51], a[68:71], v[4:7]\n"
    "s_waitcnt vmcnt(18)\n"
    "v_mfma_f32_16x16x32_bf16 v[8:11], a[48:51], a[72:75], v[8:11]\n"
    "s_waitcnt vmcnt(17)\n"
    "global_load_dwordx4 v[164:167], v245, s[62:63] offset:3072 sc1\n"
    "v_mfma_f32_16x16x32_bf16 v[12:15], a[48:51], a[76:79], v[12:15]\n"
    "v_mfma_f32_16x16x32_bf16 v[16:19], a[52:55], a[64:67], v[16:19]\n"
    "global_load_dwordx4 v[168:171], v246, s[62:63] offset:3072 sc1\n"
    "v_mfma_f32_16x16x32_bf16 v[20:23], a[52:55], a[68:71], v[20:23]\n"
    "v_mfma_f32_16x16x32_bf16 v[24:27], a[52:55], a[72:75], v[24:27]\n"
    "global_load_dwordx4 v[172:175], v247, s[62:63] offset:3072 sc1\n"
    "v_mfma_f32_16x16x32_bf16 v[28:31], a[52:55], a[76:79], v[28:31]\n"
    "v_mfma_f32_16x16x32_bf16 v[32:35], a[56:59], a[64:67], v[32:35]\n"
    "v_mfma_f32_16x16x32_bf16 v[36:39], a[56:59], a[68:71], v[36:39]\n"
    "v_mfma_f32_16x16x32_bf16 v[40:43], a[56:59], a[72:75], v[40:43]\n"
    "v_mfma_f32_16x16x32_bf16 v[44:47], a[56:59], a[76:79], v[44:47]\n"
    "v_mfma_f32_16x16x32_bf16 v[48:51], a[60:63], a[64:67], v[48:51]\n"
    "v_mfma_f32_16x16x32_bf16 v[52:55], a[60:63], a[68:71], v[52:55]\n"
    "v_mfma_f32_16x16x32_bf16 v[56:59], a[60:63], a[72:75], v[56:59]\n"
    "v_mfma_f32_16x16x32_bf16 v[60:63], a[60:63], a[76:79], v[60:63]\n"
    "s_waitcnt vmcnt(19)\n"
    "v_mfma_f32_16x16x32_bf16 v[0:3], a[96:99], v[208:211], v[0:3]\n"
    "s_waitcnt vmcnt(18)\n"
    "s_add_u32 s62, s62, 65536\n"
    "s_addc_u32 s63, s63, 0\n"
    "global_load_dwordx4 a[16:19], v244, s[62:63] offset:0 sc1\n"
    "v_mfma_f32_16x16x32_bf16 v[4:7], a[96:99], v[212:215], v[4:7]\n"
    "s_waitcnt vmcnt(18)\n"
    "v_mfma_f32_16x16x32_bf16 v[8:11], a[96:99], v[216:219], v[8:11]\n"
    "s_waitcnt vmcnt(17)\n"
    "global_load_dwordx4 a[20:23], v245, s[62:63] offset:0 sc1\n"
    "v_mfma_f32_16x16x32_bf16 v[12:15], a[96:99], v[220:223], v[12:15]\n"
    "v_mfma_f32_16x16x32_bf16 v[16:19], a[100:103], v[208:211], v[16:19]\n"
    "global_load_dwordx4 a[24:27], v246, s[62:63] offset:0 sc1\n"
    "v_mfma_f32_16x16x32_bf16 v[20:23], a[100:103], v[212:215], v[20:23]\n"
    "v_mfma_f32_16x16x32_bf16 v[24:27], a[100:103], v[216:219], v[24:27]\n"
    "global_load_dwordx4 a[28:31], v247, s[62:63] offset:0 sc1\n"
    "v_mfma_f32_16x16x32_bf16 v[28:31], a[100:103], v[220:223], v[28:31]\n"
    "v_mfma_f32_16x16x32_bf16 v[32:35], a[104:107], v[208:211], v[32:35]\n"
    "v_mfma_f32_16x16x32_bf16 v[36:39], a[104:107], v[212:215], v[36:39]\n"
    "v_mfma_f32_16x16x32_bf16 v[40:43], a[104:107], v[216:219], v[40:43]\n"
    "v_mfma_f32_16x16x32_bf16 v[44:47], a[104:107], v[220:223], v[44:47]\n"
    "v_mfma_f32_16x16x32_bf16 v[48:51], a[108:111], v[208:211], v[48:51]\n"
    "v_mfma_f32_16x16x32_bf16 v[52:55], a[108:111], v[212:215], v[52:55]\n"
    "v_mfma_f32_16x16x32_bf16 v[56:59], a[108:111], v[216:219], v[56:59]\n"
    "v_mfma_f32_16x16x32_bf16 v[60:63], a[108:111], v[220:223], v[60:63]\n"
    "s_waitcnt vmcnt(19)\n"
    "v_mfma_f32_16x16x32_bf16 v[0:3], a[112:115], a[80:83], v[0:3]\n"
    "s_waitcnt vmcnt(18)\n"
    "global_load_dwordx4 a[224:227], v244, s[62:63] offset:1024 sc1\n"
    "v_mfma_f32_16x16x32_bf16 v[4:7], a[112:115], a[84:87], v[4:7]\n"
    "s_waitcnt vmcnt(18)\n"
    "v_mfma_f32_16x16x32_bf16 v[8:11], a[112:115], a[88:91], v[8:11]\n"
    "s_waitcnt vmcnt(17)\n"
    "global_load_dwordx4 a[228:231], v245, s[62:63] offset:1024 sc1\n"
    "v_mfma_f32_16x16x32_bf16 v[12:15], a[112:115], a[92:95], v[12:15]\n"
    "v_mfma_f32_16x16x32_bf16 v[16:19], a[116:119], a[80:83], v[16:19]\n"
    "global_load_dwordx4 a[232:235], v246, s[62:63] offset:1024 sc1\n"
    "v_mfma_f32_16x16x32_bf16 v[20:23], a[116:119], a[84:87], v[20:23]\n"
    "v_mfma_f32_16x16x32_bf16 v[24:27], a[116:119], a[88:91], v[24:27]\n"
    "global_load_dwordx4 a[236:239], v247, s[62:63] offset:1024 sc1\n"
    "v_mfma_f32_16x16x32_bf16 v[28:31], a[116:119], a[92:95], v[28:31]\n"
    "v_mfma_f32_16x16x32_bf16 v[32:35], a[120:123], a[80:83], v[32:35]\n"
    "v_mfma_f32_16x16x32_bf16 v[36:39], a[120:123], a[84:87], v[36:39]\n"
    "v_mfma_f32_16x16x32_bf16 v[40:43], a[120:123], a[88:91], v[40:43]\n"
    "v_mfma_f32_16x16x32_bf16 v[44:47], a[120:123], a[92:95], v[44:47]\n"
    "v_mfma_f32_16x16x32_bf16 v[48:51], a[124:127], a[80:83], v[48:51]\n"
    "v_mfma_f32_16x16x32_bf16 v[52:55], a[124:127], a[84:87], v[52:55]\n"
    "v_mfma_f32_16x16x32_bf16 v[56:59], a[124:127], a[88:91], v[56:59]\n"
    "v_mfma_f32_16x16x32_bf16 v[60:63], a[124:127], a[92:95], v[60:63]\n"
    "s_waitcnt vmcnt(19)\n"
    "v_mfma_f32_16x16x32_bf16 v[0:3], a[160:163], a[208:211], v[0:3]\n"
    "s_waitcnt vmcnt(18)\n"
    "global_load_dwordx4 a[128:131], v244, s[62:63] offset:2048 sc1\n"
    "v_mfma_f32_16x16x32_bf16 v[4:7], a[160:163], a[212:215], v[4:7]\n"
    "s_waitcnt vmcnt(18)\n"
    "v_mfma_f32_16x16x32_bf16 v[8:11], a[160:163], a[216:219], v[8:11]\n"
    "s_waitcnt vmcnt(17)\n"
    "global_load_dwordx4 a[132:135], v245, s[62:63] offset:2048 sc1\n"
    "v_mfma_f32_16x16x32_bf16 v[12:15], a[160:163], a[220:223], v[12:15]\n"
    "v_mfma_f32_16x16x32_bf16 v[16:19], a[164:167], a[208:211], v[16:19]\n"
    "global_load_dwordx4 a[136:139], v246, s[62:63] offset:2048 sc1\n"
    "v_mfma_f32_16x16x32_bf16 v[20:23], a[164:167], a[212:215], v[20:23]\n"
    "v_mfma_f32_16x16x32_bf16 v[24:27], a[164:167], a[216:219], v[24:27]\n"
    "global_load_dwordx4 a[140:143], v247, s[62:63] offset:2048 sc1\n"
    "v_mfma_f32_16x16x32_bf16 v[28:31], a[164:167], a[220:223], v[28:31]\n"
    "v_mfma_f32_16x16x32_bf16 v[32:35], a[168:171], a[208:211], v[32:35]\n"
    "v_mfma_f32_16x16x32_bf16 v[36:39], a[168:171], a[212:215], v[36:39]\n"
    "v_mfma_f32_16x16x32_bf16 v[40:43], a[168:171], a[216:219], v[40:43]\n"
    "v_mfma_f32_16x16x32_bf16 v[44:47], a[168:171], a[220:223], v[44:47]\n"
    "v_mfma_f32_16x16x32_bf16 v[48:51], a[172:175], a[208:211], v[48:51]\n"
    "v_mfma_f32_16x16x32_bf16 v[52:55], a[172:175], a[212:215], v[52:55]\n"
    "v_mfma_f32_16x16x32_bf16 v[56:59], a[172:175], a[216:219], v[56:59]\n"
    "v_mfma_f32_16x16x32_bf16 v[60:63], a[172:175], a[220:223], v[60:63]\n"
    "s_waitcnt vmcnt(19)\n"
    "v_mfma_f32_16x16x32_bf16 v[0:3], a[176:179], a[0:3], v[0:3]\n"
    "s_waitcnt vmcnt(18)\n"
    "global_load_dwordx4 v[176:179], v244, s[62:63] offset:3072 sc1\n"
    "v_mfma_f32_16x16x32_bf16 v[4:7], a[176:179], a[4:7], v[4:7]\n"
    "s_waitcnt vmcnt(18)\n"
    "v_mfma_f32_16x16x32_bf16 v[8:11], a[176:179], a[8:11], v[8:11]\n"
    "s_waitcnt vmcnt(17)\n"
    "global_load_dwordx4 v[180:183], v245, s[62:63] offset:3072 sc1\n"
    "v_mfma_f32_16x16x32_bf16 v[12:15], a[176:179], a[12:15], v[12:15]\n"
    "v_mfma_f32_16x16x32_bf16 v[16:19], a[180:183], a[0:3], v[16:19]\n"
    "global_load_dwordx4 v[184:187], v246, s[62:63] offset:3072 sc1\n"
    "v_mfma_f32_16x16x32_bf16 v[20:23], a[180:183], a[4:7], v[20:23]\n"
    "v_mfma_f32_16x16x32_bf16 v[24:27], a[180:183], a[8:11], v[24:27]\n"
    "global_load_dwordx4 v[188:191], v247, s[62:63] offset:3072 sc1\n"
    "v_mfma_f32_16x16x32_bf16 v[28:31], a[180:183], a[12:15], v[28:31]\n"
    "v_mfma_f32_16x16x32_bf16 v[32:35], a[184:187], a[0:3], v[32:35]\n"
    "v_mfma_f32_16x16x32_bf16 v[36:39], a[184:187], a[4:7], v[36:39]\n"
    "v_mfma_f32_16x16x32_bf16 v[40:43], a[184:187], a[8:11], v[40:43]\n"
    "v_mfma_f32_16x16x32_bf16 v[44:47], a[184:187], a[12:15], v[44:47]\n"
    "v_mfma_f32_16x16x32_bf16 v[48:51], a[188:191], a[0:3], v[48:51]\n"
    "v_mfma_f32_16x16x32_bf16 v[52:55], a[188:191], a[4:7], v[52:55]\n"
    "v_mfma_f32_16x16x32_bf16 v[56:59], a[188:191], a[8:11], v[56:59]\n"
    "v_mfma_f32_16x16x32_bf16 v[60:63], a[188:191], a[12:15], v[60:63]\n"
    "s_waitcnt vmcnt(19)\n"
    "v_mfma_f32_16x16x32_bf16 v[0:3], a[192:195], v[160:163], v[0:3]\n"
    "s_waitcnt vmcnt(18)\n"
    "s_add_u32 s62, s62, 65536\n"
    "s_addc_u32 s63, s63, 0\n"
    "global_load_dwordx4 a[144:147], v244, s[62:63] offset:0 sc1\n"
    "v_mfma_f32_16x16x32_bf16 v[4:7], a[192:195], v[164:167], v[4:7]\n"
    "s_waitcnt vmcnt(18)\n"
    "v_mfma_f32_16x16x32_bf16 v[8:11], a[192:195], v[168:171], v[8:11]\n"
    "s_waitcnt vmcnt(17)\n"
    "global_load_dwordx4 a[148:151], v245, s[62:63] offset:0 sc1\n"
    "v_mfma_f32_16x16x32_bf16 v[12:15], a[192:195], v[172:175], v[12:15]\n"
    "v_mfma_f32_16x16x32_bf16 v[16:19], a[196:199], v[160:163], v[16:19]\n"
    "global_load_dwordx4 a[152:155], v246, s[62:63] offset:0 sc1\n"
    "v_mfma_f32_16x16x32_bf16 v[20:23], a[196:199], v[164:167], v[20:23]\n"
    "v_mfma_f32_16x16x32_bf16 v[24:27], a[196:199], v[168:171], v[24:27]\n"
    "global_load_dwordx4 a[156:159], v247, s[62:63] offset:0 sc1\n"
    "v_mfma_f32_16x16x32_bf16 v[28:31], a[196:199], v[172:175], v[28:31]\n"
    "v_mfma_f32_16x16x32_bf16 v[32:35], a[200:203], v[160:163], v[32:35]\n"
    "v_mfma_f32_16x16x32_bf16 v[36:39], a[200:203], v[164:167], v[36:39]\n"
    "v_mfma_f32_16x16x32_bf16 v[40:43], a[200:203], v[168:171], v[40:43]\n"
    "v_mfma_f32_16x16x32_bf16 v[44:47], a[200:203], v[172:175], v[44:47]\n"
    "v_mfma_f32_16x16x32_bf16 v[48:51], a[204:207], v[160:163], v[48:51]\n"
    "v_mfma_f32_16x16x32_bf16 v[52:55], a[204:207], v[164:167], v[52:55]\n"
    "v_mfma_f32_16x16x32_bf16 v[56:59], a[204:207], v[168:171], v[56:59]\n"
    "v_mfma_f32_16x16x32_bf16 v[60:63], a[204:207], v[172:175], v[60:63]\n"
    "s_waitcnt vmcnt(19)\n"
    "v_mfma_f32_16x16x32_bf16 v[0:3], v[64:67], a[16:19], v[0:3]\n"
    "s_waitcnt vmcnt(18)\n"
    "global_load_dwordx4 a[240:243], v244, s[62:63] offset:1024 sc1\n"
    "v_mfma_f32_16x16x32_bf16 v[4:7], v[64:67], a[20:23], v[4:7]\n"
    "s_waitcnt vmcnt(18)\n"
    "v_mfma_f32_16x16x32_bf16 v[8:11], v[64:67], a[24:27], v[8:11]\n"
    "s_waitcnt vmcnt(17)\n"
    "global_load_dwordx4 a[244:247], v245, s[62:63] offset:1024 sc1\n"
    "v_mfma_f32_16x16x32_bf16 v[12:15], v[64:67], a[28:31], v[12:15]\n"
    "v_mfma_f32_16x16x32_bf16 v[16:19], v[68:71], a[16:19], v[16:19]\n"
    "global_load_dwordx4 a[248:251], v246, s[62:63] offset:1024 sc1\n"
    "v_mfma_f32_16x16x32_bf16 v[20:23], v[68:71], a[20:23], v[20:23]\n"
    "v_mfma_f32_16x16x32_bf16 v[24:27], v[68:71], a[24:27], v[24:27]\n"
    "global_load_dwordx4 a[252:255], v247, s[62:63] offset:1024 sc1\n"
    "v_mfma_f32_16x16x32_bf16 v[28:31], v[68:71], a[28:31], v[28:31]\n"
    "v_mfma_f32_16x16x32_bf16 v[32:35], v[72:75], a[16:19], v[32:35]\n"
    "v_mfma_f32_16x16x32_bf16 v[36:39], v[72:75], a[20:23], v[36:39]\n"
    "v_mfma_f32_16x16x32_bf16 v[40:43], v[72:75], a[24:27], v[40:43]\n"
    "v_mfma_f32_16x16x32_bf16 v[44:47], v[72:75], a[28:31], v[44:47]\n"
    "v_mfma_f32_16x16x32_bf16 v[48:51], v[76:79], a[16:19], v[48:51]\n"
    "v_mfma_f32_16x16x32_bf16 v[52:55], v[76:79], a[20:23], v[52:55]\n"
    "v_mfma_f32_16x16x32_bf16 v[56:59], v[76:79], a[24:27], v[56:59]\n"
    "v_mfma_f32_16x16x32_bf16 v[60:63], v[76:79], a[28:31], v[60:63]\n"
    "s_waitcnt vmcnt(19)\n"
    "v_mfma_f32_16x16x32_bf16 v[0:3], v[80:83], a[224:227], v[0:3]\n"
    "s_waitcnt vmcnt(18)\n"
    "global_load_dwordx4 a[32:35], v244, s[62:63] offset:2048 sc1\n"
    "v_mfma_f32_16x16x32_bf16 v[4:7], v[80:83], a[228:231], v[4:7]\n"
    "s_waitcnt vmcnt(18)\n"
    "v_mfma_f32_16x16x32_bf16 v[8:11], v[80:83], a[232:235], v[8:11]\n"
    "s_waitcnt vmcnt(17)\n"
    "global_load_dwordx4 a[36:39], v245, s[62:63] offset:2048 sc1\n"
    "v_mfma_f32_16x16x32_bf16 v[12:15], v[80:83], a[236:239], v[12:15]\n"
    "v_mfma_f32_16x16x32_bf16 v[16:19], v[84:87], a[224:227], v[16:19]\n"
    "global_load_dwordx4 a[40:43], v246, s[62:63] offset:2048 sc1\n"
    "v_mfma_f32_16x16x32_bf16 v[20:23], v[84:87], a[228:231], v[20:23]\n"
    "v_mfma_f32_16x16x32_bf16 v[24:27], v[84:87], a[232:235], v[24:27]\n"
    "global_load_dwordx4 a[44:47], v247, s[62:63] offset:2048 sc1\n"
    "v_mfma_f32_16x16x32_bf16 v[28:31], v[84:87], a[236:239], v[28:31]\n"
    "v_mfma_f32_16x16x32_bf16 v[32:35], v[88:91], a[224:227], v[32:35]\n"
    "v_mfma_f32_16x16x32_bf16 v[36:39], v[88:91], a[228:231], v[36:39]\n"
    "v_mfma_f32_16x16x32_bf16 v[40:43], v[88:91], a[232:235], v[40:43]\n"
    "v_mfma_f32_16x16x32_bf16 v[44:47], v[88:91], a[236:239], v[44:47]\n"
    "v_mfma_f32_16x16x32_bf16 v[48:51], v[92:95], a[224:227], v[48:51]\n"
    "v_mfma_f32_16x16x32_bf16 v[52:55], v[92:95], a[228:231], v[52:55]\n"
    "v_mfma_f32_16x16x32_bf16 v[56:59], v[92:95], a[232:235], v[56:59]\n"
    "v_mfma_f32_16x16x32_bf16 v[60:63], v[92:95], a[236:239], v[60:63]\n"
    "s_waitcnt vmcnt(19)\n"
    "v_mfma_f32_16x16x32_bf16 v[0:3], v[96:99], a[128:131], v[0:3]\n"
    "s_waitcnt vmcnt(18)\n"
    "global_load_dwordx4 v[192:195], v242, s[60:61] offset:0 sc1\n"
    "v_mfma_f32_16x16x32_bf16 v[4:7], v[96:99], a[132:135], v[4:7]\n"
    "s_waitcnt vmcnt(18)\n"
    "v_mfma_f32_16x16x32_bf16 v[8:11], v[96:99], a[136:139], v[8:11]\n"
    "s_waitcnt vmcnt(17)\n"
    "global_load_dwordx4 v[196:199], v242, s[60:61] offset:1024 sc1\n"
    "v_mfma_f32_16x16x32_bf16 v[12:15], v[96:99], a[140:143], v[12:15]\n"
    "v_mfma_f32_16x16x32_bf16 v[16:19], v[100:103], a[128:131], v[16:19]\n"
    "global_load_dwordx4 v[200:203], v242, s[60:61] offset:2048 sc1\n"
    "v_mfma_f32_16x16x32_bf16 v[20:23], v[100:103], a[132:135], v[20:23]\n"
    "v_mfma_f32_16x16x32_bf16 v[24:27], v[100:103], a[136:139], v[24:27]\n"
    "global_load_dwordx4 v[204:207], v242, s[60:61] offset:3072 sc1\n"
    "v_mfma_f32_16x16x32_bf16 v[28:31], v[100:103], a[140:143], v[28:31]\n"
    "v_mfma_f32_16x16x32_bf16 v[32:35], v[104:107], a[128:131], v[32:35]\n"
    "v_mfma_f32_16x16x32_bf16 v[36:39], v[104:107], a[132:135], v[36:39]\n"
    "v_mfma_f32_16x16x32_bf16 v[40:43], v[104:107], a[136:139], v[40:43]\n"
    "v_mfma_f32_16x16x32_bf16 v[44:47], v[104:107], a[140:143], v[44:47]\n"
    "v_mfma_f32_16x16x32_bf16 v[48:51], v[108:111], a[128:131], v[48:51]\n"
    "v_mfma_f32_16x16x32_bf16 v[52:55], v[108:111], a[132:135], v[52:55]\n"
    "v_mfma_f32_16x16x32_bf16 v[56:59], v[108:111], a[136:139], v[56:59]\n"
    "v_mfma_f32_16x16x32_bf16 v[60:63], v[108:111], a[140:143], v[60:63]\n"
    "s_waitcnt vmcnt(19)\n"
    "v_mfma_f32_16x16x32_bf16 v[0:3], v[112:115], v[176:179], v[0:3]\n"
    "s_waitcnt vmcnt(18)\n"
    "global_load_dwordx4 a[48:51], v244, s[62:63] offset:3072 sc1\n"
    "v_mfma_f32_16x16x32_bf16 v[4:7], v[112:115], v[180:183], v[4:7]\n"
    "s_waitcnt vmcnt(18)\n"
    "v_mfma_f32_16x16x32_bf16 v[8:11], v[112:115], v[184:187], v[8:11]\n"
    "s_waitcnt vmcnt(17)\n"
    "global_load_dwordx4 a[52:55], v245, s[62:63] offset:3072 sc1\n"
    "v_mfma_f32_16x16x32_bf16 v[12:15], v[112:115], v[188:191], v[12:15]\n"
    "v_mfma_f32_16x16x32_bf16 v[16:19], v[116:119], v[176:179], v[16:19]\n"
    "global_load_dwordx4 a[56:59], v246, s[62:63] offset:3072 sc1\n"
    "v_mfma_f32_16x16x32_bf16 v[20:23], v[116:119], v[180:183], v[20:23]\n"
    "v_mfma_f32_16x16x32_bf16 v[24:27], v[116:119], v[184:187], v[24:27]\n"
    "global_load_dwordx4 a[60:63], v247, s[62:63] offset:3072 sc1\n"
    "v_mfma_f32_16x16x32_bf16 v[28:31], v[116:119], v[188:191], v[28:31]\n"
    "v_mfma_f32_16x16x32_bf16 v[32:35], v[120:123], v[176:179], v[32:35]\n"
    "v_mfma_f32_16x16x32_bf16 v[36:39], v[120:123], v[180:183], v[36:39]\n"
    "v_mfma_f32_16x16x32_bf16 v[40:43], v[120:123], v[184:187], v[40:43]\n"
    "v_mfma_f32_16x16x32_bf16 v[44:47], v[120:123], v[188:191], v[44:47]\n"
    "v_mfma_f32_16x16x32_bf16 v[48:51], v[124:127], v[176:179], v[48:51]\n"
    "v_mfma_f32_16x16x32_bf16 v[52:55], v[124:127], v[180:183], v[52:55]\n"
    "v_mfma_f32_16x16x32_bf16 v[56:59], v[124:127], v[184:187], v[56:59]\n"
    "v_mfma_f32_16x16x32_bf16 v[60:63], v[124:127], v[188:191], v[60:63]\n"
    "s_waitcnt vmcnt(19)\n"
    "v_mfma_f32_16x16x32_bf16 v[0:3], v[128:131], a[144:147], v[0:3]\n"
    "s_waitcnt vmcnt(18)\n"
    "s_add_u32 s62, s62, 65536\n"
    "s_addc_u32 s63, s63, 0\n"
    "global_load_dwordx4 a[64:67], v244, s[62:63] offset:0 sc1\n"
    "v_mfma_f32_16x16x32_bf16 v[4:7], v[128:131], a[148:151], v[4:7]\n"
    "s_waitcnt vmcnt(18)\n"
    "v_mfma_f32_16x16x32_bf16 v[8:11], v[128:131], a[152:155], v[8:11]\n"
    "s_waitcnt vmcnt(17)\n"
    "global_load_dwordx4 a[68:71], v245, s[62:63] offset:0 sc1\n"
    "v_mfma_f32_16x16x32_bf16 v[12:15], v[128:131], a[156:159], v[12:15]\n"
    "v_mfma_f32_16x16x32_bf16 v[16:19], v[132:135], a[144:147], v[16:19]\n"
    "global_load_dwordx4 a[72:75], v246, s[62:63] offset:0 sc1\n"
    "v_mfma_f32_16x16x32_bf16 v[20:23], v[132:135], a[148:151], v[20:23]\n"
    "v_mfma_f32_16x16x32_bf16 v[24:27], v[132:135], a[152:155], v[24:27]\n"
    "global_load_dwordx4 a[76:79], v247, s[62:63] offset:0 sc1\n"
    "v_mfma_f32_16x16x32_bf16 v[28:31], v[132:135], a[156:159], v[28:31]\n"
    "v_mfma_f32_16x16x32_bf16 v[32:35], v[136:139], a[144:147], v[32:35]\n"
    "v_mfma_f32_16x16x32_bf16 v[36:39], v[136:139], a[148:151], v[36:39]\n"
    "v_mfma_f32_16x16x32_bf16 v[40:43], v[136:139], a[152:155], v[40:43]\n"
    "v_mfma_f32_16x16x32_bf16 v[44:47], v[136:139], a[156:159], v[44:47]\n"
    "v_mfma_f32_16x16x32_bf16 v[48:51], v[140:143], a[144:147], v[48:51]\n"
    "v_mfma_f32_16x16x32_bf16 v[52:55], v[140:143], a[148:151], v[52:55]\n"
    "v_mfma_f32_16x16x32_bf16 v[56:59], v[140:143], a[152:155], v[56:59]\n"
    "v_mfma_f32_16x16x32_bf16 v[60:63], v[140:143], a[156:159], v[60:63]\n"
    "s_waitcnt vmcnt(19)\n"
    "v_mfma_f32_16x16x32_bf16 v[0:3], v[144:147], a[240:243], v[0:3]\n"
    "s_waitcnt vmcnt(18)\n"
    "ds_read_b128 a[96:99], v253 offset:0\n"
    "v_mfma_f32_16x16x32_bf16 v[4:7], v[144:147], a[244:247], v[4:7]\n"
    "s_waitcnt vmcnt(17)\n"
    "ds_read_b128 a[100:103], v253 offset:1024\n"
    "v_mfma_f32_16x16x32_bf16 v[8:11], v[144:147], a[248:251], v[8:11]\n"
    "s_waitcnt vmcnt(16)\n"
    "ds_read_b128 a[104:107], v253 offset:2048\n"
    "v_mfma_f32_16x16x32_bf16 v[12:15], v[144:147], a[252:255], v[12:15]\n"
    "ds_read_b128 a[108:111], v253 offset:3072\n"
    "v_mfma_f32_16x16x32_bf16 v[16:19], v[148:151], a[240:243], v[16:19]\n"
    "global_load_dwordx4 v[208:211], v244, s[62:63] offset:1024 sc1\n"
    "v_mfma_f32_16x16x32_bf16 v[20:23], v[148:151], a[244:247], v[20:23]\n"
    "v_mfma_f32_16x16x32_bf16 v[24:27], v[148:151], a[248:251], v[24:27]\n"
    "global_load_dwordx4 v[212:215], v245, s[62:63] offset:1024 sc1\n"
    "v_mfma_f32_16x16x32_bf16 v[28:31], v[148:151], a[252:255], v[28:31]\n"
    "v_mfma_f32_16x16x32_bf16 v[32:35], v[152:155], a[240:243], v[32:35]\n"
    "global_load_dwordx4 v[216:219], v246, s[62:63] offset:1024 sc1\n"
    "v_mfma_f32_16x16x32_bf16 v[36:39], v[152:155], a[244:247], v[36:39]\n"
    "v_mfma_f32_16x16x32_bf16 v[40:43], v[152:155], a[248:251], v[40:43]\n"
    "global_load_dwordx4 v[220:223], v247, s[62:63] offset:1024 sc1\n"
    "v_mfma_f32_16x16x32_bf16 v[44:47], v[152:155], a[252:255], v[44:47]\n"
    "v_mfma_f32_16x16x32_bf16 v[48:51], v[156:159], a[240:243], v[48:51]\n"
    "v_mfma_f32_16x16x32_bf16 v[52:55], v[156:159], a[244:247], v[52:55]\n"
    "v_mfma_f32_16x16x32_bf16 v[56:59], v[156:159], a[248:251], v[56:59]\n"
    "v_mfma_f32_16x16x32_bf16 v[60:63], v[156:159], a[252:255], v[60:63]\n"
    "s_waitcnt vmcnt(15)\n"
    "v_mfma_f32_16x16x32_bf16 v[0:3], v[192:195], a[32:35], v[0:3]\n"
    "ds_read_b128 a[112:115], v253 offset:16384\n"
    "v_mfma_f32_16x16x32_bf16 v[4:7], v[192:195], a[36:39], v[4:7]\n"
    "ds_read_b128 a[116:119], v253 offset:17408\n"
    "v_mfma_f32_16x16x32_bf16 v[8:11], v[192:195], a[40:43], v[8:11]\n"
    "ds_read_b128 a[120:123], v253 offset:18432\n"
    "v_mfma_f32_16x16x32_bf16 v[12:15], v[192:195], a[44:47], v[12:15]\n"
    "s_waitcnt vmcnt(14)\n"
    "ds_read_b128 a[124:127], v253 offset:19456\n"
    "v_mfma_f32_16x16x32_bf16 v[16:19], v[196:199], a[32:35], v[16:19]\n"
    "global_load_dwordx4 a[80:83], v244, s[62:63] offset:2048 sc1\n"
    "v_mfma_f32_16x16x32_bf16 v[20:23], v[196:199], a[36:39], v[20:23]\n"
    "v_mfma_f32_16x16x32_bf16 v[24:27], v[196:199], a[40:43], v[24:27]\n"
    "global_load_dwordx4 a[84:87], v245, s[62:63] offset:2048 sc1\n"
    "v_mfma_f32_16x16x32_bf16 v[28:31], v[196:199], a[44:47], v[28:31]\n"
    "s_waitcnt vmcnt(15)\n"
    "v_mfma_f32_16x16x32_bf16 v[32:35], v[200:203], a[32:35], v[32:35]\n"
    "global_load_dwordx4 a[88:91], v246, s[62:63] offset:2048 sc1\n"
    "v_mfma_f32_16x16x32_bf16 v[36:39], v[200:203], a[36:39], v[36:39]\n"
    "v_mfma_f32_16x16x32_bf16 v[40:43], v[200:203], a[40:43], v[40:43]\n"
    "global_load_dwordx4 a[92:95], v247, s[62:63] offset:2048 sc1\n"
    "v_mfma_f32_16x16x32_bf16 v[44:47], v[200:203], a[44:47], v[44:47]\n"
    "s_waitcnt vmcnt(16)\n"
    "v_mfma_f32_16x16x32_bf16 v[48:51], v[204:207], a[32:35], v[48:51]\n"
    "v_mfma_f32_16x16x32_bf16 v[52:55], v[204:207], a[36:39], v[52:55]\n"
    "v_mfma_f32_16x16x32_bf16 v[56:59], v[204:207], a[40:43], v[56:59]\n"
    "v_mfma_f32_16x16x32_bf16 v[60:63], v[204:207], a[44:47], v[60:63]\n"
    "s_waitcnt lgkmcnt(0)\n"
    "s_waitcnt vmcnt(15)\n"
    "v_mfma_f32_16x16x32_bf16 v[0:3], a[96:99], a[48:51], v[0:3]\n"
    "s_waitcnt vmcnt(14)\n"
    "ds_read_b128 a[160:163], v254 offset:0\n"
    "v_mfma_f32_16x16x32_bf16 v[4:7], a[96:99], a[52:55], v[4:7]\n"
    "s_waitcnt vmcnt(13)\n"
    "ds_read_b128 a[164:167], v254 offset:1024\n"
    "v_mfma_f32_16x16x32_bf16 v[8:11], a[96:99], a[56:59], v[8:11]\n"
    "s_waitcnt vmcnt(12)\n"
    "ds_read_b128 a[168:171], v254 offset:2048\n"
    "v_mfma_f32_16x16x32_bf16 v[12:15], a[96:99], a[60:63], v[12:15]\n"
    "ds_read_b128 a[172:175], v254 offset:3072\n"
    "v_mfma_f32_16x16x32_bf16 v[16:19], a[100:103], a[48:51], v[16:19]\n"
    "global_load_dwordx4 a[208:211], v244, s[62:63] offset:3072 sc1\n"
    "v_mfma_f32_16x16x32_bf16 v[20:23], a[100:103], a[52:55], v[20:23]\n"
    "v_mfma_f32_16x16x32_bf16 v[24:27], a[100:103], a[56:59], v[24:27]\n"
    "global_load_dwordx4 a[212:215], v245, s[62:63] offset:3072 sc1\n"
    "v_mfma_f32_16x16x32_bf16 v[28:31], a[100:103], a[60:63], v[28:31]\n"
    "v_mfma_f32_16x16x32_bf16 v[32:35], a[104:107], a[48:51], v[32:35]\n"
    "global_load_dwordx4 a[216:219], v246, s[62:63] offset:3072 sc1\n"
    "v_mfma_f32_16x16x32_bf16 v[36:39], a[104:107], a[52:55], v[36:39]\n"
    "v_mfma_f32_16x16x32_bf16 v[40:43], a[104:107], a[56:59], v[40:43]\n"
    "global_load_dwordx4 a[220:223], v247, s[62:63] offset:3072 sc1\n"
    "v_mfma_f32_16x16x32_bf16 v[44:47], a[104:107], a[60:63], v[44:47]\n"
    "v_mfma_f32_16x16x32_bf16 v[48:51], a[108:111], a[48:51], v[48:51]\n"
    "s_add_u32 s62, s62, 65536\n"
    "s_addc_u32 s63, s63, 0\n"
    "global_load_dwordx4 a[176:179], v244, s[62:63] offset:0 sc1\n"
    "v_mfma_f32_16x16x32_bf16 v[52:55], a[108:111], a[52:55], v[52:55]\n"
    "v_mfma_f32_16x16x32_bf16 v[56:59], a[108:111], a[56:59], v[56:59]\n"
    "global_load_dwordx4 a[180:183], v245, s[62:63] offset:0 sc1\n"
    "v_mfma_f32_16x16x32_bf16 v[60:63], a[108:111], a[60:63], v[60:63]\n"
    "s_waitcnt vmcnt(17)\n"
    "v_mfma_f32_16x16x32_bf16 v[0:3], a[112:115], a[64:67], v[0:3]\n"
    "s_waitcnt vmcnt(16)\n"
    "global_load_dwordx4 a[184:187], v246, s[62:63] offset:0 sc1\n"
    "ds_read_b128 a[0:3], v254 offset:16384\n"
    "v_mfma_f32_16x16x32_bf16 v[4:7], a[112:115], a[68:71], v[4:7]\n"
    "s_waitcnt vmcnt(16)\n"
    "ds_read_b128 a[4:7], v254 offset:17408\n"
    "v_mfma_f32_16x16x32_bf16 v[8:11], a[112:115], a[72:75], v[8:11]\n"
    "s_waitcnt vmcnt(15)\n"
    "global_load_dwordx4 a[188:191], v247, s[62:63] offset:0 sc1\n"
    "ds_read_b128 a[8:11], v254 offset:18432\n"
    "v_mfma_f32_16x16x32_bf16 v[12:15], a[112:115], a[76:79], v[12:15]\n"
    "ds_read_b128 a[12:15], v254 offset:19456\n"
    "v_mfma_f32_16x16x32_bf16 v[16:19], a[116:119], a[64:67], v[16:19]\n"
    "global_load_dwordx4 a[192:195], v244, s[62:63] offset:1024 sc1\n"
    "v_mfma_f32_16x16x32_bf16 v[20:23], a[116:119], a[68:71], v[20:23]\n"
    "v_mfma_f32_16x16x32_bf16 v[24:27], a[116:119], a[72:75], v[24:27]\n"
    "global_load_dwordx4 a[196:199], v245, s[62:63] offset:1024 sc1\n"
    "v_mfma_f32_16x16x32_bf16 v[28:31], a[116:119], a[76:79], v[28:31]\n"
    "v_mfma_f32_16x16x32_bf16 v[32:35], a[120:123], a[64:67], v[32:35]\n"
    "global_load_dwordx4 a[200:203], v246, s[62:63] offset:1024 sc1\n"
    "v_mfma_f32_16x16x32_bf16 v[36:39], a[120:123], a[68:71], v[36:39]\n"
    "v_mfma_f32_16x16x32_bf16 v[40:43], a[120:123], a[72:75], v[40:43]\n"
    "global_load_dwordx4 a[204:207], v247, s[62:63] offset:1024 sc1\n"
    "v_mfma_f32_16x16x32_bf16 v[44:47], a[120:123], a[76:79], v[44:47]\n"
    "v_mfma_f32_16x16x32_bf16 v[48:51], a[124:127], a[64:67], v[48:51]\n"
    "v_mfma_f32_16x16x32_bf16 v[52:55], a[124:127], a[68:71], v[52:55]\n"
    "v_mfma_f32_16x16x32_bf16 v[56:59], a[124:127], a[72:75], v[56:59]\n"
    "v_mfma_f32_16x16x32_bf16 v[60:63], a[124:127], a[76:79], v[60:63]\n"
    "s_waitcnt lgkmcnt(0)\n"
    "s_waitcnt vmcnt(19)\n"
    "v_mfma_f32_16x16x32_bf16 v[0:3], a[160:163], v[208:211], v[0:3]\n"
    "s_waitcnt vmcnt(18)\n"
    "ds_read_b128 v[160:163], v255 offset:0\n"
    "v_mfma_f32_16x16x32_bf16 v[4:7], a[160:163], v[212:215], v[4:7]\n"
    "s_waitcnt vmcnt(17)\n"
    "ds_read_b128 v[164:167], v255 offset:1024\n"
    "v_mfma_f32_16x16x32_bf16 v[8:11], a[160:163], v[216:219], v[8:11]\n"
    "s_waitcnt vmcnt(16)\n"
    "ds_read_b128 v[168:171], v255 offset:2048\n"
    "v_mfma_f32_16x16x32_bf16 v[12:15], a[160:163], v[220:223], v[12:15]\n"
    "ds_read_b128 v[172:175], v255 offset:3072\n"
    "v_mfma_f32_16x16x32_bf16 v[16:19], a[164:167], v[208:211], v[16:19]\n"
    "global_load_dwordx4 v[64:67], v244, s[62:63] offset:2048 sc1\n"
    "v_mfma_f32_16x16x32_bf16 v[20:23], a[164:167], v[212:215], v[20:23]\n"
    "v_mfma_f32_16x16x32_bf16 v[24:27], a[164:167], v[216:219], v[24:27]\n"
    "global_load_dwordx4 v[68:71], v245, s[62:63] offset:2048 sc1\n"
    "v_mfma_f32_16x16x32_bf16 v[28:31], a[164:167], v[220:223], v[28:31]\n"
    "v_mfma_f32_16x16x32_bf16 v[32:35], a[168:171], v[208:211], v[32:35]\n"
    "global_load_dwordx4 v[72:75], v246, s[62:63] offset:2048 sc1\n"
    "v_mfma_f32_16x16x32_bf16 v[36:39], a[168:171], v[212:215], v[36:39]\n"
    "v_mfma_f32_16x16x32_bf16 v[40:43], a[168:171], v[216:219], v[40:43]\n"
    "global_load_dwordx4 v[76:79], v247, s[62:63] offset:2048 sc1\n"
    "v_mfma_f32_16x16x32_bf16 v[44:47], a[168:171], v[220:223], v[44:47]\n"
    "v_mfma_f32_16x16x32_bf16 v[48:51], a[172:175], v[208:211], v[48:51]\n"
    "v_mfma_f32_16x16x32_bf16 v[52:55], a[172:175], v[212:215], v[52:55]\n"
    "v_mfma_f32_16x16x32_bf16 v[56:59], a[172:175], v[216:219], v[56:59]\n"
    "v_mfma_f32_16x16x32_bf16 v[60:63], a[172:175], v[220:223], v[60:63]\n"
    "s_waitcnt vmcnt(19)\n"
    "v_mfma_f32_16x16x32_bf16 v[0:3], a[0:3], a[80:83], v[0:3]\n"
    "s_waitcnt vmcnt(18)\n"
    "ds_read_b128 a[16:19], v254 offset:32768\n"
    "v_mfma_f32_16x16x32_bf16 v[4:7], a[0:3], a[84:87], v[4:7]\n"
    "s_waitcnt vmcnt(17)\n"
    "ds_read_b128 a[20:23], v254 offset:33792\n"
    "v_mfma_f32_16x16x32_bf16 v[8:11], a[0:3], a[88:91], v[8:11]\n"
    "s_waitcnt vmcnt(16)\n"
    "ds_read_b128 a[24:27], v254 offset:34816\n"
    "v_mfma_f32_16x16x32_bf16 v[12:15], a[0:3], a[92:95], v[12:15]\n"
    "ds_read_b128 a[28:31], v254 offset:35840\n"
    "v_mfma_f32_16x16x32_bf16 v[16:19], a[4:7], a[80:83], v[16:19]\n"
    "global_load_dwordx4 v[80:83], v244, s[62:63] offset:3072 sc1\n"
    "v_mfma_f32_16x16x32_bf16 v[20:23], a[4:7], a[84:87], v[20:23]\n"
    "v_mfma_f32_16x16x32_bf16 v[24:27], a[4:7], a[88:91], v[24:27]\n"
    "global_load_dwordx4 v[84:87], v245, s[62:63] offset:3072 sc1\n"
    "v_mfma_f32_16x16x32_bf16 v[28:31], a[4:7], a[92:95], v[28:31]\n"
    "v_mfma_f32_16x16x32_bf16 v[32:35], a[8:11], a[80:83], v[32:35]\n"
    "global_load_dwordx4 v[88:91], v246, s[62:63] offset:3072 sc1\n"
    "v_mfma_f32_16x16x32_bf16 v[36:39], a[8:11], a[84:87], v[36:39]\n"
    "v_mfma_f32_16x16x32_bf16 v[40:43], a[8:11], a[88:91], v[40:43]\n"
    "global_load_dwordx4 v[92:95], v247, s[62:63] offset:3072 sc1\n"
    "v_mfma_f32_16x16x32_bf16 v[44:47], a[8:11], a[92:95], v[44:47]\n"
    "v_mfma_f32_16x16x32_bf16 v[48:51], a[12:15], a[80:83], v[48:51]\n"
    "v_mfma_f32_16x16x32_bf16 v[52:55], a[12:15], a[84:87], v[52:55]\n"
    "v_mfma_f32_16x16x32_bf16 v[56:59], a[12:15], a[88:91], v[56:59]\n"
    "v_mfma_f32_16x16x32_bf16 v[60:63], a[12:15], a[92:95], v[60:63]\n"
    "s_waitcnt lgkmcnt(0)\n"
    "s_waitcnt vmcnt(19)\n"
    "v_mfma_f32_16x16x32_bf16 v[0:3], v[160:163], a[208:211], v[0:3]\n"
    "s_waitcnt vmcnt(18)\n"
    "ds_read_b128 a[224:227], v255 offset:16384\n"
    "v_mfma_f32_16x16x32_bf16 v[4:7], v[160:163], a[212:215], v[4:7]\n"
    "s_waitcnt vmcnt(17)\n"
    "ds_read_b128 a[228:231], v255 offset:17408\n"
    "v_mfma_f32_16x16x32_bf16 v[8:11], v[160:163], a[216:219], v[8:11]\n"
    "s_waitcnt vmcnt(16)\n"
    "ds_read_b128 a[232:235], v255 offset:18432\n"
    "v_mfma_f32_16x16x32_bf16 v[12:15], v[160:163], a[220:223], v[12:15]\n"
    "ds_read_b128 a[236:239], v255 offset:19456\n"
    "v_mfma_f32_16x16x32_bf16 v[16:19], v[164:167], a[208:211], v[16:19]\n"
    "v_mfma_f32_16x16x32_bf16 v[20:23], v[164:167], a[212:215], v[20:23]\n"
    "v_mfma_f32_16x16x32_bf16 v[24:27], v[164:167], a[216:219], v[24:27]\n"
    "v_mfma_f32_16x16x32_bf16 v[28:31], v[164:167], a[220:223], v[28:31]\n"
    "v_mfma_f32_16x16x32_bf16 v[32:35], v[168:171], a[208:211], v[32:35]\n"
    "v_mfma_f32_16x16x32_bf16 v[36:39], v[168:171], a[212:215], v[36:39]\n"
    "v_mfma_f32_16x16x32_bf16 v[40:43], v[168:171], a[216:219], v[40:43]\n"
    "v_mfma_f32_16x16x32_bf16 v[44:47], v[168:171], a[220:223], v[44:47]\n"
    "v_mfma_f32_16x16x32_bf16 v[48:51], v[172:175], a[208:211], v[48:51]\n"
    "v_mfma_f32_16x16x32_bf16 v[52:55], v[172:175], a[212:215], v[52:55]\n"
    "v_mfma_f32_16x16x32_bf16 v[56:59], v[172:175], a[216:219], v[56:59]\n"
    "v_mfma_f32_16x16x32_bf16 v[60:63], v[172:175], a[220:223], v[60:63]\n"
    "s_waitcnt vmcnt(15)\n"
    "v_mfma_f32_16x16x32_bf16 v[0:3], a[16:19], a[176:179], v[0:3]\n"
    "s_waitcnt vmcnt(14)\n"
    "ds_read_b128 v[96:99], v254 offset:49152\n"
    "v_mfma_f32_16x16x32_bf16 v[4:7], a[16:19], a[180:183], v[4:7]\n"
    "s_waitcnt vmcnt(13)\n"
    "ds_read_b128 v[100:103], v254 offset:50176\n"
    "v_mfma_f32_16x16x32_bf16 v[8:11], a[16:19], a[184:187], v[8:11]\n"
    "s_waitcnt vmcnt(12)\n"
    "ds_read_b128 v[104:107], v254 offset:51200\n"
    "v_mfma_f32_16x16x32_bf16 v[12:15], a[16:19], a[188:191], v[12:15]\n"
    "ds_read_b128 v[108:111], v254 offset:52224\n"
    "v_mfma_f32_16x16x32_bf16 v[16:19], a[20:23], a[176:179], v[16:19]\n"
    "v_mfma_f32_16x16x32_bf16 v[20:23], a[20:23], a[180:183], v[20:23]\n"
    "v_mfma_f32_16x16x32_bf16 v[24:27], a[20:23], a[184:187], v[24:27]\n"
    "v_mfma_f32_16x16x32_bf16 v[28:31], a[20:23], a[188:191], v[28:31]\n"
    "v_mfma_f32_16x16x32_bf16 v[32:35], a[24:27], a[176:179], v[32:35]\n"
    "v_mfma_f32_16x16x32_bf16 v[36:39], a[24:27], a[180:183], v[36:39]\n"
    "v_mfma_f32_16x16x32_bf16 v[40:43], a[24:27], a[184:187], v[40:43]\n"
    "v_mfma_f32_16x16x32_bf16 v[44:47], a[24:27], a[188:191], v[44:47]\n"
    "v_mfma_f32_16x16x32_bf16 v[48:51], a[28:31], a[176:179], v[48:51]\n"
    "v_mfma_f32_16x16x32_bf16 v[52:55], a[28:31], a[180:183], v[52:55]\n"
    "v_mfma_f32_16x16x32_bf16 v[56:59], a[28:31], a[184:187], v[56:59]\n"
    "v_mfma_f32_16x16x32_bf16 v[60:63], a[28:31], a[188:191], v[60:63]\n"
    "s_waitcnt lgkmcnt(0)\n"
    "s_waitcnt vmcnt(11)\n"
    "v_mfma_f32_16x16x32_bf16 v[0:3], a[224:227], a[192:195], v[0:3]\n"
    "s_waitcnt vmcnt(10)\n"
    "ds_read_b128 a[128:131], v255 offset:49152\n"
    "v_mfma_f32_16x16x32_bf16 v[4:7], a[224:227], a[196:199], v[4:7]\n"
    "s_waitcnt vmcnt(9)\n"
    "ds_read_b128 a[132:135], v255 offset:50176\n"
    "v_mfma_f32_16x16x32_bf16 v[8:11], a[224:227], a[200:203], v[8:11]\n"
    "s_waitcnt vmcnt(8)\n"
    "ds_read_b128 a[136:139], v255 offset:51200\n"
    "v_mfma_f32_16x16x32_bf16 v[12:15], a[224:227], a[204:207], v[12:15]\n"
    "ds_read_b128 a[140:143], v255 offset:52224\n"
    "v_mfma_f32_16x16x32_bf16 v[16:19], a[228:231], a[192:195], v[16:19]\n"
    "v_mfma_f32_16x16x32_bf16 v[20:23], a[228:231], a[196:199], v[20:23]\n"
    "v_mfma_f32_16x16x32_bf16 v[24:27], a[228:231], a[200:203], v[24:27]\n"
    "v_mfma_f32_16x16x32_bf16 v[28:31], a[228:231], a[204:207], v[28:31]\n"
    "v_mfma_f32_16x16x32_bf16 v[32:35], a[232:235], a[192:195], v[32:35]\n"
    "v_mfma_f32_16x16x32_bf16 v[36:39], a[232:235], a[196:199], v[36:39]\n"
    "v_mfma_f32_16x16x32_bf16 v[40:43], a[232:235], a[200:203], v[40:43]\n"
    "v_mfma_f32_16x16x32_bf16 v[44:47], a[232:235], a[204:207], v[44:47]\n"
    "v_mfma_f32_16x16x32_bf16 v[48:51], a[236:239], a[192:195], v[48:51]\n"
    "v_mfma_f32_16x16x32_bf16 v[52:55], a[236:239], a[196:199], v[52:55]\n"
    "v_mfma_f32_16x16x32_bf16 v[56:59], a[236:239], a[200:203], v[56:59]\n"
    "v_mfma_f32_16x16x32_bf16 v[60:63], a[236:239], a[204:207], v[60:63]\n"
    "s_waitcnt vmcnt(7)\n"
    "v_mfma_f32_16x16x32_bf16 v[0:3], v[96:99], v[64:67], v[0:3]\n"
    "s_waitcnt vmcnt(6)\n"
    "v_mfma_f32_16x16x32_bf16 v[4:7], v[96:99], v[68:71], v[4:7]\n"
    "s_waitcnt vmcnt(5)\n"
    "v_mfma_f32_16x16x32_bf16 v[8:11], v[96:99], v[72:75], v[8:11]\n"
    "s_waitcnt vmcnt(4)\n"
    "v_mfma_f32_16x16x32_bf16 v[12:15], v[96:99], v[76:79], v[12:15]\n"
    "v_mfma_f32_16x16x32_bf16 v[16:19], v[100:103], v[64:67], v[16:19]\n"
    "v_mfma_f32_16x16x32_bf16 v[20:23], v[100:103], v[68:71], v[20:23]\n"
    "v_mfma_f32_16x16x32_bf16 v[24:27], v[100:103], v[72:75], v[24:27]\n"
    "v_mfma_f32_16x16x32_bf16 v[28:31], v[100:103], v[76:79], v[28:31]\n"
    "v_mfma_f32_16x16x32_bf16 v[32:35], v[104:107], v[64:67], v[32:35]\n"
    "v_mfma_f32_16x16x32_bf16 v[36:39], v[104:107], v[68:71], v[36:39]\n"
    "v_mfma_f32_16x16x32_bf16 v[40:43], v[104:107], v[72:75], v[40:43]\n"
    "v_mfma_f32_16x16x32_bf16 v[44:47], v[104:107], v[76:79], v[44:47]\n"
    "v_mfma_f32_16x16x32_bf16 v[48:51], v[108:111], v[64:67], v[48:51]\n"
    "v_mfma_f32_16x16x32_bf16 v[52:55], v[108:111], v[68:71], v[52:55]\n"
    "v_mfma_f32_16x16x32_bf16 v[56:59], v[108:111], v[72:75], v[56:59]\n"
    "v_mfma_f32_16x16x32_bf16 v[60:63], v[108:111], v[76:79], v[60:63]\n"
    "s_waitcnt lgkmcnt(0)\n"
    "s_waitcnt vmcnt(3)\n"
    "v_mfma_f32_16x16x32_bf16 v[0:3], a[128:131], v[80:83], v[0:3]\n"
    "s_waitcnt vmcnt(2)\n"
    "v_mfma_f32_16x16x32_bf16 v[4:7], a[128:131], v[84:87], v[4:7]\n"
    "s_waitcnt vmcnt(1)\n"
    "v_mfma_f32_16x16x32_bf16 v[8:11], a[128:131], v[88:91], v[8:11]\n"
    "s_waitcnt vmcnt(0)\n"
    "v_mfma_f32_16x16x32_bf16 v[12:15], a[128:131], v[92:95], v[12:15]\n"
    "v_mfma_f32_16x16x32_bf16 v[16:19], a[132:135], v[80:83], v[16:19]\n"
    "v_mfma_f32_16x16x32_bf16 v[20:23], a[132:135], v[84:87], v[20:23]\n"
    "v_mfma_f32_16x16x32_bf16 v[24:27], a[132:135], v[88:91], v[24:27]\n"
    "v_mfma_f32_16x16x32_bf16 v[28:31], a[132:135], v[92:95], v[28:31]\n"
    "v_mfma_f32_16x16x32_bf16 v[32:35], a[136:139], v[80:83], v[32:35]\n"
    "v_mfma_f32_16x16x32_bf16 v[36:39], a[136:139], v[84:87], v[36:39]\n"
    "v_mfma_f32_16x16x32_bf16 v[40:43], a[136:139], v[88:91], v[40:43]\n"
    "v_mfma_f32_16x16x32_bf16 v[44:47], a[136:139], v[92:95], v[44:47]\n"
    "v_mfma_f32_16x16x32_bf16 v[48:51], a[140:143], v[80:83], v[48:51]\n"
    "v_mfma_f32_16x16x32_bf16 v[52:55], a[140:143], v[84:87], v[52:55]\n"
    "v_mfma_f32_16x16x32_bf16 v[56:59], a[140:143], v[88:91], v[56:59]\n"
    "v_mfma_f32_16x16x32_bf16 v[60:63], a[140:143], v[92:95], v[60:63]\n"
    "v_and_b32 v200, 15, v236\n"
    "v_lshrrev_b32 v201, 4, v236\n"
    "v_lshlrev_b32 v201, 5, v201\n"
    "v_lshl_add_u32 v200, v200, 10, v201\n"
    "s_lshl_b32 s46, s48, 14\n"
    "v_add_u32 v200, s46, v200\n"
    "s_lshl_b32 s46, s48, 14\n"
    "s_add_u32 s46, s46, s50\n"
    "v_add_u32 v255, s46, v237\n"
    "ds_write_b128 v254, v[0:3] offset:0\n"
    "ds_write_b128 v254, v[16:19] offset:1024\n"
    "ds_write_b128 v254, v[32:35] offset:2048\n"
    "ds_write_b128 v254, v[48:51] offset:3072\n"
    "ds_write_b128 v254, v[4:7] offset:16384\n"
    "ds_write_b128 v254, v[20:23] offset:17408\n"
    "ds_write_b128 v254, v[36:39] offset:18432\n"
    "ds_write_b128 v254, v[52:55] offset:19456\n"
    "ds_write_b128 v254, v[8:11] offset:32768\n"
    "ds_write_b128 v254, v[24:27] offset:33792\n"
    "ds_write_b128 v254, v[40:43] offset:34816\n"
    "ds_write_b128 v254, v[56:59] offset:35840\n"
    "ds_write_b128 v254, v[12:15] offset:49152\n"
    "ds_write_b128 v254, v[28:31] offset:50176\n"
    "ds_write_b128 v254, v[44:47] offset:51200\n"
    "ds_write_b128 v254, v[60:63] offset:52224\n"
    "s_waitcnt lgkmcnt(0)\n"
    "s_barrier\n"
    "ds_read_b128 v[64:67], v255 offset:0\n"
    "ds_read_b128 v[68:71], v255 offset:1024\n"
    "ds_read_b128 v[72:75], v255 offset:2048\n"
    "ds_read_b128 v[76:79], v255 offset:3072\n"
    "ds_read_b128 v[80:83], v255 offset:4096\n"
    "ds_read_b128 v[84:87], v255 offset:5120\n"
    "ds_read_b128 v[88:91], v255 offset:6144\n"
    "ds_read_b128 v[92:95], v255 offset:7168\n"
    "ds_read_b128 v[96:99], v255 offset:8192\n"
    "ds_read_b128 v[100:103], v255 offset:9216\n"
    "ds_read_b128 v[104:107], v255 offset:10240\n"
    "ds_read_b128 v[108:111], v255 offset:11264\n"
    "ds_read_b128 v[112:115], v255 offset:12288\n"
    "ds_read_b128 v[116:119], v255 offset:13312\n"
    "ds_read_b128 v[120:123], v255 offset:14336\n"
    "ds_read_b128 v[124:127], v255 offset:15360\n"
    "s_waitcnt lgkmcnt(7)\n"
    "v_add_f32 v64, v64, v80\n"
    "s_waitcnt lgkmcnt(3)\n"
    "v_add_f32 v96, v96, v112\n"
    "v_add_f32 v65, v65, v81\n"
    "v_add_f32 v97, v97, v113\n"
    "v_add_f32 v66, v66, v82\n"
    "v_add_f32 v98, v98, v114\n"
    "v_add_f32 v67, v67, v83\n"
    "v_add_f32 v99, v99, v115\n"
    "v_add_f32 v68, v68, v84\n"
    "s_waitcnt lgkmcnt(0)\n"
    "v_add_f32 v100, v100, v116\n"
    "v_add_f32 v69, v69, v85\n"
    "v_add_f32 v101, v101, v117\n"
    "v_add_f32 v70, v70, v86\n"
    "v_add_f32 v102, v102, v118\n"
    "v_add_f32 v71, v71, v87\n"
    "v_add_f32 v103, v103, v119\n"
    "v_add_f32 v72, v72, v88\n"
    "v_add_f32 v104, v104, v120\n"
    "v_add_f32 v73, v73, v89\n"
    "v_add_f32 v105, v105, v121\n"
    "v_add_f32 v74, v74, v90\n"
    "v_add_f32 v106, v106, v122\n"
    "v_add_f32 v75, v75, v91\n"
    "v_add_f32 v107, v107, v123\n"
    "v_add_f32 v76, v76, v92\n"
    "v_add_f32 v108, v108, v124\n"
    "v_add_f32 v77, v77, v93\n"
    "v_add_f32 v109, v109, v125\n"
    "v_add_f32 v78, v78, v94\n"
    "v_add_f32 v110, v110, v126\n"
    "v_add_f32 v79, v79, v95\n"
    "v_add_f32 v111, v111, v127\n"
    "v_add_f32 v64, v64, v96\n"
    "v_add_f32 v65, v65, v97\n"
    "v_add_f32 v66, v66, v98\n"
    "v_add_f32 v67, v67, v99\n"
    "v_add_f32 v68, v68, v100\n"
    "v_add_f32 v69, v69, v101\n"
    "v_add_f32 v70, v70, v102\n"
    "v_add_f32 v71, v71, v103\n"
    "v_add_f32 v72, v72, v104\n"
    "v_add_f32 v73, v73, v105\n"
    "v_add_f32 v74, v74, v106\n"
    "v_add_f32 v75, v75, v107\n"
    "v_add_f32 v76, v76, v108\n"
    "v_add_f32 v77, v77, v109\n"
    "v_add_f32 v78, v78, v110\n"
    "v_add_f32 v79, v79, v111\n"
    "v_cvt_pk_bf16_f32 v128, v64, v65\n"
    "v_cvt_pk_bf16_f32 v129, v66, v67\n"
    "v_cvt_pk_bf16_f32 v130, v68, v69\n"
    "v_cvt_pk_bf16_f32 v131, v70, v71\n"
    "v_cvt_pk_bf16_f32 v132, v72, v73\n"
    "v_cvt_pk_bf16_f32 v133, v74, v75\n"
    "v_cvt_pk_bf16_f32 v134, v76, v77\n"
    "v_cvt_pk_bf16_f32 v135, v78, v79\n"
    "global_store_dwordx4 v200, v[128:131], s[68:69]\n"
    "global_store_dwordx4 v200, v[132:135], s[68:69] offset:16\n"
    "s_or_b32 s46, s49, s48\n"
    "s_or_b32 s46, s46, s42\n"
    "s_cmp_lg_u32 s46, 0\n"
    "s_cbranch_scc1 BUMPD%=\n"
    "v_lshlrev_b32 v200, 5, v236\n"
    "v_add_u32 v200, 4096, v200\n"
    "v_mov_b32 v210, s96\n"
    "v_mov_b32 v211, s97\n"
    "s_mov_b32 s46, 400000\n"
    "BUMPL%=:\n"
    "global_load_dwordx4 v[202:205], v200, s[32:33] sc1\n"
    "global_load_dwordx4 v[206:209], v200, s[32:33] offset:16 sc1\n"
    "s_waitcnt vmcnt(0)\n"
    "v_cmp_eq_u64 vcc, v[202:203], v[210:211]\n"
    "v_cmp_eq_u64 s[60:61], v[204:205], v[210:211]\n"
    "s_nop 3\n"
    "s_and_b64 vcc, vcc, s[60:61]\n"
    "v_cmp_eq_u64 s[60:61], v[206:207], v[210:211]\n"
    "s_nop 3\n"
    "s_and_b64 vcc, vcc, s[60:61]\n"
    "v_cmp_eq_u64 s[60:61], v[208:209], v[210:211]\n"
    "s_nop 3\n"
    "s_and_b64 vcc, vcc, s[60:61]\n"
    "s_cmp_eq_u64 vcc, -1\n"
    "s_cbranch_scc1 BUMPS%=\n"
    "s_sleep 2\n"
    "s_sub_u32 s46, s46, 1\n"
    "s_cmp_lg_u32 s46, 0\n"
    "s_cbranch_scc1 BUMPL%=\n"
    "BUMPS%=:\n"
    "v_mov_b32 v200, 0\n"
    "global_store_dwordx2 v200, v[210:211], s[32:33] sc1\n"
    "BUMPD%=:\n"
    "s_waitcnt vmcnt(0)\n"
    "s_branch END%=\n"
    "; ---- guard tripped: start again with the true row maximum\n"
    "FB%=:\n"
    "s_waitcnt vmcnt(0) lgkmcnt(0)\n"
    "s_barrier\n"
    "s_mov_b32 s51, 1\n"
    "s_branch START%=\n"
    "TM%=:\n"
    "v_and_b32 v212, 15, v236\n"
    "v_lshrrev_b32 v213, 4, v236\n"
    "v_lshlrev_b32 v213, 4, v213\n"
    "v_lshl_add_u32 v212, v212, 8, v213\n"
    "v_mov_b32 v228, 0xff800000\n"
    "v_mov_b32 v229, 0xff800000\n"
    "v_mov_b32 v230, 0xff800000\n"
    "v_mov_b32 v231, 0xff800000\n"
    "s_mov_b32 s42, 0\n"
    "s_mov_b32 s31, 32\n"
    "TML%=:\n"
    "buffer_load_dwordx4 a[64:67], v212, s[20:23], s42 offen offset:0\n"
    "buffer_load_dwordx4 a[68:71], v212, s[20:23], s42 offen offset:64\n"
    "buffer_load_dwordx4 a[72:75], v212, s[20:23], s42 offen offset:128\n"
    "buffer_load_dwordx4 a[76:79], v212, s[20:23], s42 offen offset:192\n"
    "s_add_u32 s42, s42, 4096\n"
    "buffer_load_dwordx4 a[80:83], v212, s[20:23], s42 offen offset:0\n"
    "buffer_load_dwordx4 a[84:87], v212, s[20:23], s42 offen offset:64\n"
    "buffer_load_dwordx4 a[88:91], v212, s[20:23], s42 offen offset:128\n"
    "buffer_load_dwordx4 a[92:95], v212, s[20:23], s42 offen offset:192\n"
    "s_add_u32 s42, s42, 4096\n"
    "buffer_load_dwordx4 a[96:99], v212, s[20:23], s42 offen offset:0\n"
    "buffer_load_dwordx4 a[100:103], v212, s[20:23], s42 offen offset:64\n"
    "buffer_load_dwordx4 a[104:107], v212, s[20:23], s42 offen offset:128\n"
    "buffer_load_dwordx4 a[108:111], v212, s[20:23], s42 offen offset:192\n"
    "s_add_u32 s42, s42, 4096\n"
    "buffer_load_dwordx4 a[112:115], v212, s[20:23], s42 offen offset:0\n"
    "buffer_load_dwordx4 a[116:119], v212, s[20:23], s42 offen offset:64\n"
    "buffer_load_dwordx4 a[120:123], v212, s[20:23], s42 offen offset:128\n"
    "buffer_load_dwordx4 a[124:127], v212, s[20:23], s42 offen offset:192\n"
    "s_add_u32 s42, s42, 4096\n"
    "s_waitcnt vmcnt(0)\n"
    "v_mfma_f32_16x16x32_bf16 v[128:131], a[64:67], a[0:3], 0\n"
    "v_mfma_f32_16x16x32_bf16 v[128:131], a[68:71], a[4:7], v[128:131]\n"
    "v_mfma_f32_16x16x32_bf16 v[128:131], a[72:75], a[8:11], v[128:131]\n"
    "v_mfma_f32_16x16x32_bf16 v[128:131], a[76:79], a[12:15], v[128:131]\n"
    "v_mfma_f32_16x16x32_bf16 v[132:135], a[80:83], a[0:3], 0\n"
    "v_mfma_f32_16x16x32_bf16 v[132:135], a[84:87], a[4:7], v[132:135]\n"
    "v_mfma_f32_16x16x32_bf16 v[132:135], a[88:91], a[8:11], v[132:135]\n"
    "v_mfma_f32_16x16x32_bf16 v[132:135], a[92:95], a[12:15], v[132:135]\n"
    "s_nop 7\n"
    "v_max3_f32 v228, v228, v128, v129\n"
    "v_max3_f32 v228, v228, v130, v131\n"
    "v_max3_f32 v228, v228, v132, v133\n"
    "v_max3_f32 v228, v228, v134, v135\n"
    "v_mfma_f32_16x16x32_bf16 v[136:139], a[64:67], a[16:19], 0\n"
    "v_mfma_f32_16x16x32_bf16 v[136:139], a[68:71], a[20:23], v[136:139]\n"
    "v_mfma_f32_16x16x32_bf16 v[136:139], a[72:75], a[24:27], v[136:139]\n"
    "v_mfma_f32_16x16x32_bf16 v[136:139], a[76:79], a[28:31], v[136:139]\n"
    "v_mfma_f32_16x16x32_bf16 v[140:143], a[80:83], a[16:19], 0\n"
    "v_mfma_f32_16x16x32_bf16 v[140:143], a[84:87], a[20:23], v[140:143]\n"
    "v_mfma_f32_16x16x32_bf16 v[140:143], a[88:91], a[24:27], v[140:143]\n"
    "v_mfma_f32_16x16x32_bf16 v[140:143], a[92:95], a[28:31], v[140:143]\n"
    "s_nop 7\n"
    "v_max3_f32 v229, v229, v136, v137\n"
    "v_max3_f32 v229, v229, v138, v139\n"
    "v_max3_f32 v229, v229, v140, v141\n"
    "v_max3_f32 v229, v229, v142, v143\n"
    "v_mfma_f32_16x16x32_bf16 v[128:131], a[64:67], a[32:35], 0\n"
    "v_mfma_f32_16x16x32_bf16 v[128:131], a[68:71], a[36:39], v[128:131]\n"
    "v_mfma_f32_16x16x32_bf16 v[128:131], a[72:75], a[40:43], v[128:131]\n"
    "v_mfma_f32_16x16x32_bf16 v[128:131], a[76:79], a[44:47], v[128:131]\n"
    "v_mfma_f32_16x16x32_bf16 v[132:135], a[80:83], a[32:35], 0\n"
    "v_mfma_f32_16x16x32_bf16 v[132:135], a[84:87], a[36:39], v[132:135]\n"
    "v_mfma_f32_16x16x32_bf16 v[132:135], a[88:91], a[40:43], v[132:135]\n"
    "v_mfma_f32_16x16x32_bf16 v[132:135], a[92:95], a[44:47], v[132:135]\n"
    "s_nop 7\n"
    "v_max3_f32 v230, v230, v128, v129\n"
    "v_max3_f32 v230, v230, v130, v131\n"
    "v_max3_f32 v230, v230, v132, v133\n"
    "v_max3_f32 v230, v230, v134, v135\n"
    "v_mfma_f32_16x16x32_bf16 v[136:139], a[64:67], a[48:51], 0\n"
    "v_mfma_f32_16x16x32_bf16 v[136:139], a[68:71], a[52:55], v[136:139]\n"
    "v_mfma_f32_16x16x32_bf16 v[136:139], a[72:75], a[56:59], v[136:139]\n"
    "v_mfma_f32_16x16x32_bf16 v[136:139], a[76:79], a[60:63], v[136:139]\n"
    "v_mfma_f32_16x16x32_bf16 v[140:143], a[80:83], a[48:51], 0\n"
    "v_mfma_f32_16x16x32_bf16 v[140:143], a[84:87], a[52:55], v[140:143]\n"
    "v_mfma_f32_16x16x32_bf16 v[140:143], a[88:91], a[56:59], v[140:143]\n"
    "v_mfma_f32_16x16x32_bf16 v[140:143], a[92:95], a[60:63], v[140:143]\n"
    "s_nop 7\n"
    "v_max3_f32 v231, v231, v136, v137\n"
    "v_max3_f32 v231, v231, v138, v139\n"
    "v_max3_f32 v231, v231, v140, v141\n"
    "v_max3_f32 v231, v231, v142, v143\n"
    "v_mfma_f32_16x16x32_bf16 v[128:131], a[96:99], a[0:3], 0\n"
    "v_mfma_f32_16x16x32_bf16 v[128:131], a[100:103], a[4:7], v[128:131]\n"
    "v_mfma_f32_16x16x32_bf16 v[128:131], a[104:107], a[8:11], v[128:131]\n"
    "v_mfma_f32_16x16x32_bf16 v[128:131], a[108:111], a[12:15], v[128:131]\n"
    "v_mfma_f32_16x16x32_bf16 v[132:135], a[112:115], a[0:3], 0\n"
    "v_mfma_f32_16x16x32_bf16 v[132:135], a[116:119], a[4:7], v[132:135]\n"
    "v_mfma_f32_16x16x32_bf16 v[132:135], a[120:123], a[8:11], v[132:135]\n"
    "v_mfma_f32_16x16x32_bf16 v[132:135], a[124:127], a[12:15], v[132:135]\n"
    "s_nop 7\n"
    "v_max3_f32 v228, v228, v128, v129\n"
    "v_max3_f32 v228, v228, v130, v131\n"
    "v_max3_f32 v228, v228, v132, v133\n"
    "v_max3_f32 v228, v228, v134, v135\n"
    "v_mfma_f32_16x16x32_bf16 v[136:139], a[96:99], a[16:19], 0\n"
    "v_mfma_f32_16x16x32_bf16 v[136:139], a[100:103], a[20:23], v[136:139]\n"
    "v_mfma_f32_16x16x32_bf16 v[136:139], a[104:107], a[24:27], v[136:139]\n"
    "v_mfma_f32_16x16x32_bf16 v[136:139], a[108:111], a[28:31], v[136:139]\n"
    "v_mfma_f32_16x16x32_bf16 v[140:143], a[112:115], a[16:19], 0\n"
    "v_mfma_f32_16x16x32_bf16 v[140:143], a[116:119], a[20:23], v[140:143]\n"
    "v_mfma_f32_16x16x32_bf16 v[140:143], a[120:123], a[24:27], v[140:143]\n"
    "v_mfma_f32_16x16x32_bf16 v[140:143], a[124:127], a[28:31], v[140:143]\n"
    "s_nop 7\n"
    "v_max3_f32 v229, v229, v136, v137\n"
    "v_max3_f32 v229, v229, v138, v139\n"
    "v_max3_f32 v229, v229, v140, v141\n"
    "v_max3_f32 v229, v229, v142, v143\n"
    "v_mfma_f32_16x16x32_bf16 v[128:131], a[96:99], a[32:35], 0\n"
    "v_mfma_f32_16x16x32_bf16 v[128:131], a[100:103], a[36:39], v[128:131]\n"
    "v_mfma_f32_16x16x32_bf16 v[128:131], a[104:107], a[40:43], v[128:131]\n"
    "v_mfma_f32_16x16x32_bf16 v[128:131], a[108:111], a[44:47], v[128:131]\n"
    "v_mfma_f32_16x16x32_bf16 v[132:135], a[112:115], a[32:35], 0\n"
    "v_mfma_f32_16x16x32_bf16 v[132:135], a[116:119], a[36:39], v[132:135]\n"
    "v_mfma_f32_16x16x32_bf16 v[132:135], a[120:123], a[40:43], v[132:135]\n"
    "v_mfma_f32_16x16x32_bf16 v[132:135], a[124:127], a[44:47], v[132:135]\n"
    "s_nop 7\n"
    "v_max3_f32 v230, v230, v128, v129\n"
    "v_max3_f32 v230, v230, v130, v131\n"
    "v_max3_f32 v230, v230, v132, v133\n"
    "v_max3_f32 v230, v230, v134, v135\n"
    "v_mfma_f32_16x16x32_bf16 v[136:139], a[96:99], a[48:51], 0\n"
    "v_mfma_f32_16x16x32_bf16 v[136:139], a[100:103], a[52:55], v[136:139]\n"
    "v_mfma_f32_16x16x32_bf16 v[136:139], a[104:107], a[56:59], v[136:139]\n"
    "v_mfma_f32_16x16x32_bf16 v[136:139], a[108:111], a[60:63], v[136:139]\n"
    "v_mfma_f32_16x16x32_bf16 v[140:143], a[112:115], a[48:51], 0\n"
    "v_mfma_f32_16x16x32_bf16 v[140:143], a[116:119], a[52:55], v[140:143]\n"
    "v_mfma_f32_16x16x32_bf16 v[140:143], a[120:123], a[56:59], v[140:143]\n"
    "v_mfma_f32_16x16x32_bf16 v[140:143], a[124:127], a[60:63], v[140:143]\n"
    "s_nop 7\n"
    "v_max3_f32 v231, v231, v136, v137\n"
    "v_max3_f32 v231, v231, v138, v139\n"
    "v_max3_f32 v231, v231, v140, v141\n"
    "v_max3_f32 v231, v231, v142, v143\n"
    "s_sub_u32 s31, s31, 1\n"
    "s_cmp_lg_u32 s31, 0\n"
    "s_cbranch_scc1 TML%=\n"
    "v_mov_b32 v214, v228\n"
    "v_mov_b32 v215, v214\n"
    "s_nop 1\n"
    "v_permlane32_swap_b32 v214, v215\n"
    "v_max_f32 v214, v214, v215\n"
    "v_mov_b32 v215, v214\n"
    "s_nop 1\n"
    "v_permlane16_swap_b32 v214, v215\n"
    "v_max_f32 v214, v214, v215\n"
    "v_sub_f32 v160, 0, v214\n"
    "v_sub_f32 v161, 0, v214\n"
    "v_sub_f32 v162, 0, v214\n"
    "v_sub_f32 v163, 0, v214\n"
    "v_mov_b32 v214, v229\n"
    "v_mov_b32 v215, v214\n"
    "s_nop 1\n"
    "v_permlane32_swap_b32 v214, v215\n"
    "v_max_f32 v214, v214, v215\n"
    "v_mov_b32 v215, v214\n"
    "s_nop 1\n"
    "v_permlane16_swap_b32 v214, v215\n"
    "v_max_f32 v214, v214, v215\n"
    "v_sub_f32 v164, 0, v214\n"
    "v_sub_f32 v165, 0, v214\n"
    "v_sub_f32 v166, 0, v214\n"
    "v_sub_f32 v167, 0, v214\n"
    "v_mov_b32 v214, v230\n"
    "v_mov_b32 v215, v214\n"
    "s_nop 1\n"
    "v_permlane32_swap_b32 v214, v215\n"
    "v_max_f32 v214, v214, v215\n"
    "v_mov_b32 v215, v214\n"
    "s_nop 1\n"
    "v_permlane16_swap_b32 v214, v215\n"
    "v_max_f32 v214, v214, v215\n"
    "v_sub_f32 v168, 0, v214\n"
    "v_sub_f32 v169, 0, v214\n"
    "v_sub_f32 v170, 0, v214\n"
    "v_sub_f32 v171, 0, v214\n"
    "v_mov_b32 v214, v231\n"
    "v_mov_b32 v215, v214\n"
    "s_nop 1\n"
    "v_permlane32_swap_b32 v214, v215\n"
    "v_max_f32 v214, v214, v215\n"
    "v_mov_b32 v215, v214\n"
    "s_nop 1\n"
    "v_permlane16_swap_b32 v214, v215\n"
    "v_max_f32 v214, v214, v215\n"
    "v_sub_f32 v172, 0, v214\n"
    "v_sub_f32 v173, 0, v214\n"
    "v_sub_f32 v174, 0, v214\n"
    "v_sub_f32 v175, 0, v214\n"
    "ds_read_b128 a[64:67], v180 offset:0\n"
    "ds_read_b128 a[68:71], v181 offset:0\n"
    "ds_read_b128 a[72:75], v182 offset:0\n"
    "ds_read_b128 a[76:79], v183 offset:0\n"
    "ds_read_b128 a[80:83], v180 offset:4096\n"
    "ds_read_b128 a[84:87], v181 offset:4096\n"
    "ds_read_b128 a[88:91], v182 offset:4096\n"
    "ds_read_b128 a[92:95], v183 offset:4096\n"
    "s_waitcnt lgkmcnt(0)\n"
    "s_branch MAXD%=\n"
    "PC0%=:\n"
    "s_sleep 1\n"
    "s_sub_u32 s65, s65, 4\n"
    "s_cmp_gt_i32 s65, 0\n"
    "s_cbranch_scc1 PC0%=\n"
    "s_branch PC0D%=\n"
    "PC1%=:\n"
    "s_sleep 1\n"
    "s_sub_u32 s65, s65, 4\n"
    "s_cmp_gt_i32 s65, 0\n"
    "s_cbranch_scc1 PC1%=\n"
    "s_branch PC1D%=\n"
    "PC2%=:\n"
    "s_sleep 1\n"
    "s_sub_u32 s65, s65, 4\n"
    "s_cmp_gt_i32 s65, 0\n"
    "s_cbranch_scc1 PC2%=\n"
    "s_branch PC2D%=\n"
    "PC3%=:\n"
    "s_sleep 1\n"
    "s_sub_u32 s65, s65, 4\n"
    "s_cmp_gt_i32 s65, 0\n"
    "s_cbranch_scc1 PC3%=\n"
    "s_branch PC3D%=\n"
    "END%=:\n"
  :: "s"(x),"s"(kc),"s"(vc),"s"(wq),"s"(wo),"s"(ws),"s"(out),"s"(wid),"s"(bid),"s"((unsigned)(size_t)lds)
  : "memory","vcc","scc","v0","v1","v2","v3","v4","v5","v6","v7","v8","v9","v10","v11","v12","v13","v14","v15","v16","v17","v18","v19","v20","v21","v22","v23","v24","v25","v26","v27","v28","v29","v30","v31","v32","v33","v34","v35","v36","v37","v38","v39","v40","v41","v42","v43","v44","v45","v46","v47","v48","v49","v50","v51","v52","v53","v54","v55","v56","v57","v58","v59","v60","v61","v62","v63","v64","v65","v66","v67","v68","v69","v70","v71","v72","v73","v74","v75","v76","v77","v78","v79","v80","v81","v82","v83","v84","v85","v86","v87","v88","v89","v90","v91","v92","v93","v94","v95","v96","v97","v98","v99","v100","v101","v102","v103","v104","v105","v106","v107","v108","v109","v110","v111","v112","v113","v114","v115","v116","v117","v118","v119","v120","v121","v122","v123","v124","v125","v126","v127","v128","v129","v130","v131","v132","v133","v134","v135","v136","v137","v138","v139","v140","v141","v142","v143","v144","v145","v146","v147","v148","v149","v150","v151","v152","v153","v154","v155","v156","v157","v158","v159","v160","v161","v162","v163","v164","v165","v166","v167","v168","v169","v170","v171","v172","v173","v174","v175","v176","v177","v178","v179","v180","v181","v182","v183","v184","v185","v186","v187","v188","v189","v190","v191","v192","v193","v194","v195","v196","v197","v198","v199","v200","v201","v202","v203","v204","v205","v206","v207","v208","v209","v210","v211","v212","v213","v214","v215","v216","v217","v218","v219","v220","v221","v222","v223","v224","v225","v226","v227","v228","v229","v230","v231","v232","v233","v234","v235","v236","v237","v238","v239","v240","v241","v242","v243","v244","v245","v246","v247","v248","v249","v250","v251","v252","v253","v254","v255","a0","a1","a2","a3","a4","a5","a6","a7","a8","a9","a10","a11","a12","a13","a14","a15","a16","a17","a18","a19","a20","a21","a22","a23","a24","a25","a26","a27","a28","a29","a30","a31","a32","a33","a34","a35","a36","a37","a38","a39","a40","a41","a42","a43","a44","a45","a46","a47","a48","a49","a50","a51","a52","a53","a54","a55","a56","a57","a58","a59","a60","a61","a62","a63","a64","a65","a66","a67","a68","a69","a70","a71","a72","a73","a74","a75","a76","a77","a78","a79","a80","a81","a82","a83","a84","a85","a86","a87","a88","a89","a90","a91","a92","a93","a94","a95","a96","a97","a98","a99","a100","a101","a102","a103","a104","a105","a106","a107","a108","a109","a110","a111","a112","a113","a114","a115","a116","a117","a118","a119","a120","a121","a122","a123","a124","a125","a126","a127","a128","a129","a130","a131","a132","a133","a134","a135","a136","a137","a138","a139","a140","a141","a142","a143","a144","a145","a146","a147","a148","a149","a150","a151","a152","a153","a154","a155","a156","a157","a158","a159","a160","a161","a162","a163","a164","a165","a166","a167","a168","a169","a170","a171","a172","a173","a174","a175","a176","a177","a178","a179","a180","a181","a182","a183","a184","a185","a186","a187","a188","a189","a190","a191","a192","a193","a194","a195","a196","a197","a198","a199","a200","a201","a202","a203","a204","a205","a206","a207","a208","a209","a210","a211","a212","a213","a214","a215","a216","a217","a218","a219","a220","a221","a222","a223","a224","a225","a226","a227","a228","a229","a230","a231","a232","a233","a234","a235","a236","a237","a238","a239","a240","a241","a242","a243","a244","a245","a246","a247","a248","a249","a250","a251","a252","a253","a254","a255","s20","s21","s22","s23","s24","s25","s26","s27","s28","s29","s30","s31","s32","s33","s34","s35","s36","s37","s38","s39","s40","s41","s42","s43","s44","s45","s46","s47","s48","s49","s50","s51","s52","s53","s54","s55","s56","s57","s58","s59","s60","s61","s62","s63","s64","s65","s66","s67","s68","s69","s70","s71","s72","s73","s74","s75","s76","s77","s78","s79","s80","s81","s82","s83","s84","s85","s86","s87","s88","s89","s90","s91","s92","s93","s94","s95","s96","s97","s98","s99","s100","s101");
}
extern "C" void kernel_launch(void* const* d_in, const int* in_sizes, int n_in, void* d_out, int out_size, void* d_ws, size_t ws_size, hipStream_t stream) {
  mha_block_fused<<<256,256,0,stream>>>(d_in[0],d_in[1],d_in[2],d_in[3],d_in[4],d_ws,d_out);
}
```
